```python
import torch

def _softmax(x, axis=-1):
    return torch.softmax(x, dim=axis)
def _gelu(x):
    return torch.nn.functional.gelu(x, approximate="tanh")
def _silu(x):
    return torch.nn.functional.silu(x)
def _sigmoid(x):
    return torch.sigmoid(x)
def _layernorm(x, axis=-1, eps=1e-5):
    m = x.mean(dim=axis, keepdim=True)
    v = x.var(dim=axis, keepdim=True, unbiased=False)
    return (x - m) / torch.sqrt(v + eps)
def _scatter_add(tgt, idx, src, axis=0):
    return tgt.clone().index_add_(axis, idx, src)
def _segment_sum(src, seg, n):
    out = torch.zeros((n,) + src.shape[1:], dtype=src.dtype)
    return out.index_add_(0, seg, src)
def _pad(x, pw):
    flat = tuple(v for pair in reversed(pw) for v in pair)
    return torch.nn.functional.pad(x, flat)

kernel_name = 'parallel_sum__ssm_conv_s4__geglu_ffn__prompt'

def setup_inputs():

    _w = (lambda a: (lambda t: t.to(torch.bfloat16) if t.is_floating_point() else t)(torch.as_tensor(a)))
    def _A_setup():
        B, L, D, N = 1, 2048, 512, 128
        A_re = -(lambda _x: torch.exp(torch.as_tensor(_x)))((lambda *s: torch.randn(*s, dtype=torch.bfloat16) * 3.2)(D, N))
        A_im = torch.pi * (lambda *s: torch.randn(*s, dtype=torch.bfloat16) * 0.2)(D, N)
        B_re = (lambda *s: torch.randn(*s, dtype=torch.bfloat16) * 0.4)(D, N)
        B_im = (lambda *s: torch.randn(*s, dtype=torch.bfloat16) * 0.4)(D, N)
        C_re = (lambda *s: torch.randn(*s, dtype=torch.bfloat16) * 0.4)(D, N)
        C_im = (lambda *s: torch.randn(*s, dtype=torch.bfloat16) * 0.4)(D, N)
        log_dt = (lambda *s: torch.randn(*s, dtype=torch.bfloat16) * 0.2)(D) * 0.5 - 4.0
        Dskip = (lambda *s: torch.randn(*s, dtype=torch.bfloat16) * 0.4)(D)
        return {
            "u": _w((lambda *s: torch.randn(*s, dtype=torch.bfloat16) * 0.05)(B, L, D)),
            "A_re": _w(A_re),
            "A_im": _w(A_im),
            "B_re": _w(B_re),
            "B_im": _w(B_im),
            "C_re": _w(C_re),
            "C_im": _w(C_im),
            "log_dt": _w(log_dt),
            "Dskip": _w(Dskip),
        }
    _A_in = _A_setup()

    def _B_setup():
        B, S, D, F = 1, 2048, 512, 1792
        return {
            "x": _w((lambda *s: torch.randn(*s, dtype=torch.bfloat16) * 0.05)(B, S, D)),
            "w_gate": _w((lambda *s: torch.randn(*s, dtype=torch.bfloat16) * 0.05)(D, F)),
            "w_up": _w((lambda *s: torch.randn(*s, dtype=torch.bfloat16) * 0.05)(D, F)),
            "w_down": _w((lambda *s: torch.randn(*s, dtype=torch.bfloat16) * 0.05)(F, D)),
        }
    _B_in = _B_setup()

    return {
        "A_u": _A_in["u"],
        "A_A_re": _A_in["A_re"],
        "A_A_im": _A_in["A_im"],
        "A_B_re": _A_in["B_re"],
        "A_B_im": _A_in["B_im"],
        "A_C_re": _A_in["C_re"],
        "A_C_im": _A_in["C_im"],
        "A_log_dt": _A_in["log_dt"],
        "A_Dskip": _A_in["Dskip"],
        "B_x": _B_in["x"],
        "B_w_gate": _B_in["w_gate"],
        "B_w_up": _B_in["w_up"],
        "B_w_down": _B_in["w_down"],
    }

def reference(A_u, A_A_re, A_A_im, A_B_re, A_B_im, A_C_re, A_C_im, A_log_dt, A_Dskip, B_x, B_w_gate, B_w_up, B_w_down):
    def _A_ref(u, A_re, A_im, B_re, B_im, C_re, C_im, log_dt, Dskip):
        Bsz, L, D = u.shape
        dt = torch.exp(log_dt)[:, None]
        A = A_re + 1j * A_im
        Bc = B_re + 1j * B_im
        Cc = C_re + 1j * C_im
        logdA = dt * A
        dB = dt * Bc
        l = (lambda x, dt: x.to(dt))(torch.arange(L), A_re.dtype)
        P = torch.exp(logdA[:, :, None] * l[None, None, :])
        K = torch.einsum("dn,dnl,dn->dl", Cc, P, dB).real
        L2 = 2 * L
        Kp = _pad(K, ((0, 0), (0, L)))
        up = (lambda x, *d: x.permute(*d))(u, 0, 2, 1)
        up = _pad(up, ((0, 0), (0, 0), (0, L)))
        Kf = (lambda x, axes=(-2,-1): torch.fft.rfft2(x.float(), dim=axes))(Kp[:, None, :], axes=(-2, -1))
        Uf = (lambda x, axes=(-2,-1): torch.fft.rfft2(x.float(), dim=axes))(up[:, :, None, :], axes=(-2, -1))
        Yf = Uf * Kf[None, :, :, :]
        y = (lambda x, s, axes=(-2,-1): torch.fft.irfft2(x, s=s, dim=axes).to(torch.bfloat16))(Yf, (1, L2), axes=(-2, -1))[:, :, 0, :L]
        y = (lambda x, *d: x.permute(*d))(y, 0, 2, 1) + u * Dskip[None, None, :]
        return y

    def _B_ref(x, w_gate, w_up, w_down):
        return (_gelu(x @ w_gate) * (x @ w_up)) @ w_down

    x = A_u
    xn = _layernorm(x, axis=-1)
    return x + _A_ref(xn, A_A_re, A_A_im, A_B_re, A_B_im, A_C_re, A_C_im, A_log_dt, A_Dskip) + _B_ref(xn, B_w_gate, B_w_up, B_w_down)
```

```cpp
#include <hip/hip_runtime.h>
#include <hip/hip_bf16.h>
#include <stdint.h>
#define ROUND_L 1
typedef unsigned short u16;
__device__ __forceinline__ float bf2f(u16 v){ return __uint_as_float(((unsigned)v)<<16); }
__device__ __forceinline__ u16 f2bf(float f){ unsigned u=__float_as_uint(f); u += 0x7FFF + ((u>>16)&1); return (u16)(u>>16); }
__device__ __forceinline__ float rbf(float f){ return bf2f(f2bf(f)); }
constexpr int L=2048, D=512, N=128, F=1792;

__global__ void k_ln(const u16* x, u16* xn){
  int t=blockIdx.x; int tid=threadIdx.x;
  float v[8]; float s=0;
  for(int i=0;i<8;i++){ v[i]=bf2f(x[t*D+tid*8+i]); s+=v[i]; }
  for(int o=32;o;o>>=1) s+=__shfl_xor(s,o);
  float m=s/D; float q=0;
  for(int i=0;i<8;i++){ float d=v[i]-m; q+=d*d; }
  for(int o=32;o;o>>=1) q+=__shfl_xor(q,o);
  float var=rbf(q/D); float mb=rbf(m);
  float sd=rbf(sqrtf(rbf(var+1e-5f)));
  for(int i=0;i<8;i++) xn[t*D+tid*8+i]=f2bf(rbf(v[i]-mb)/sd);
}
__global__ void k_kgen(const u16* Are,const u16* Aim,const u16* Bre,const u16* Bim,const u16* Cre,const u16* Cim,const u16* ldt,float* K){
  int d=blockIdx.x;
  __shared__ float ar[N],ai[N],wr[N],wi[N];
  float dt=rbf(expf(bf2f(ldt[d])));
  if(threadIdx.x<N){ int n=threadIdx.x; int i=d*N+n;
    ar[n]=dt*bf2f(Are[i]); ai[n]=dt*bf2f(Aim[i]);
    float br=dt*bf2f(Bre[i]), bi=dt*bf2f(Bim[i]), cr=bf2f(Cre[i]), ci=bf2f(Cim[i]);
    wr[n]=cr*br-ci*bi; wi[n]=cr*bi+ci*br; }
  __syncthreads();
  for(int l=threadIdx.x;l<L;l+=blockDim.x){
    float lf = ROUND_L ? rbf((float)l) : (float)l;
    float acc=0;
    for(int n=0;n<N;n++){ float e=expf(ar[n]*lf); float s,c; sincosf(ai[n]*lf,&s,&c); acc+=e*(wr[n]*c-wi[n]*s); }
    K[d*L+l]=acc;
  }
}
__global__ void k_conv(const u16* xn,const float* K,float* y){
  int d=blockIdx.x; __shared__ float u[L], k[L];
  for(int i=threadIdx.x;i<L;i+=blockDim.x){ u[i]=bf2f(xn[i*D+d]); k[i]=K[d*L+i]; }
  __syncthreads();
  for(int t=threadIdx.x;t<L;t+=blockDim.x){ float a=0; for(int s=0;s<=t;s++) a+=k[t-s]*u[s]; y[t*D+d]=a; }
}
__global__ void k_g1(const u16* xn,const u16* wg,const u16* wu,u16* h){
  int t=blockIdx.x; __shared__ float xs[D];
  for(int i=threadIdx.x;i<D;i+=blockDim.x) xs[i]=bf2f(xn[t*D+i]);
  __syncthreads();
  for(int f=threadIdx.x;f<F;f+=blockDim.x){ float g=0,u=0; for(int k=0;k<D;k++){ g+=xs[k]*bf2f(wg[k*F+f]); u+=xs[k]*bf2f(wu[k*F+f]); }
    float ge=0.5f*g*(1.f+tanhf(0.7978845608f*(g+0.044715f*g*g*g))); h[t*F+f]=f2bf(ge*u); }
}
__global__ void k_g2(const u16* x,const u16* xn,const u16* h,const u16* wd,const float* y,const u16* Dk,u16* out){
  int t=blockIdx.x; __shared__ float hs[F];
  for(int i=threadIdx.x;i<F;i+=blockDim.x) hs[i]=bf2f(h[t*F+i]);
  __syncthreads();
  for(int d=threadIdx.x;d<D;d+=blockDim.x){ float a=0; for(int k=0;k<F;k++) a+=hs[k]*bf2f(wd[k*D+d]);
    out[t*D+d]=f2bf(bf2f(x[t*D+d]) + y[t*D+d] + bf2f(xn[t*D+d])*bf2f(Dk[d]) + a); }
}
extern "C" void kernel_launch(void* const* d_in, const int* in_sizes, int n_in, void* d_out, int out_size, void* d_ws, size_t ws_size, hipStream_t stream){
  const u16* x=(const u16*)d_in[0];
  char* ws=(char*)d_ws;
  u16* xn=(u16*)ws; float* K=(float*)(ws+(4<<20)); float* y=(float*)(ws+(12<<20)); u16* h=(u16*)(ws+(20<<20));
  k_ln<<<L,64,0,stream>>>(x,xn);
  k_kgen<<<D,256,0,stream>>>((const u16*)d_in[1],(const u16*)d_in[2],(const u16*)d_in[3],(const u16*)d_in[4],(const u16*)d_in[5],(const u16*)d_in[6],(const u16*)d_in[7],K);
  k_conv<<<D,256,0,stream>>>(xn,K,y);
  k_g1<<<L,256,0,stream>>>(xn,(const u16*)d_in[10],(const u16*)d_in[11],h);
  k_g2<<<L,256,0,stream>>>(x,xn,h,(const u16*)d_in[12],y,(const u16*)d_in[8],(u16*)d_out);
}
```

```cpp
#include <hip/hip_runtime.h>
#include <hip/hip_bf16.h>
#include <stdint.h>
#define ROUND_L 1
typedef unsigned short u16;
__device__ __forceinline__ float bf2f(u16 v){ return __uint_as_float(((unsigned)v)<<16); }
__device__ __forceinline__ u16 f2bf(float f){ unsigned u=__float_as_uint(f); u += 0x7FFF + ((u>>16)&1); return (u16)(u>>16); }
__device__ __forceinline__ float rbf(float f){ return bf2f(f2bf(f)); }
constexpr int L=2048, D=512, N=128, F=1792;

typedef unsigned long long u64;
typedef int i32x4 __attribute__((ext_vector_type(4)));
__device__ __forceinline__ i32x4 mksrd(const void* p, unsigned bytes){
  u64 a=(u64)p; i32x4 r; r[0]=(int)(unsigned)a; r[1]=(int)(unsigned)(a>>32); r[2]=(int)bytes; r[3]=0x00020000; return r; }
__global__ void __launch_bounds__(512,2) kw(char* buf, unsigned bytes, int n){
  extern __shared__ char sm[];
  const int tid=threadIdx.x, lane=tid&63; const int w=__builtin_amdgcn_readfirstlane(tid>>6);
  i32x4 s=mksrd(buf,bytes);
  unsigned base = blockIdx.x*(unsigned)n*8192 + w*1024;
  unsigned voff = lane*16;
  i32x4 v={tid,tid,tid,tid};
  u64 rts=__builtin_amdgcn_s_memrealtime();
  while(__builtin_amdgcn_s_memrealtime()-rts < 300) __builtin_amdgcn_s_sleep(2);
  __syncthreads();
  u64 rt0=__builtin_amdgcn_s_memrealtime();
  for(int i=0;i<n;i++) asm volatile("buffer_store_dwordx4 %3, %1, %0, %2 offen sc1\n s_nop 1" :: "s"(s), "v"(voff), "s"(base+i*8192), "v"(v) : "memory");
  asm volatile("s_waitcnt vmcnt(0)":::"memory");
  __syncthreads();
  u64 rt1=__builtin_amdgcn_s_memrealtime();
  u64 dur=rt1-rt0; if(dur>3000) dur=3000;
  while(__builtin_amdgcn_s_memrealtime()-rt0 < dur*200) __builtin_amdgcn_s_sleep(16);
}

__global__ void k_ln(const u16* x, u16* xn){
  int t=blockIdx.x; int tid=threadIdx.x;
  float v[8]; float s=0;
  for(int i=0;i<8;i++){ v[i]=bf2f(x[t*D+tid*8+i]); s+=v[i]; }
  for(int o=32;o;o>>=1) s+=__shfl_xor(s,o);
  float m=s/D; float q=0;
  for(int i=0;i<8;i++){ float d=v[i]-m; q+=d*d; }
  for(int o=32;o;o>>=1) q+=__shfl_xor(q,o);
  float var=rbf(q/D); float mb=rbf(m);
  float sd=rbf(sqrtf(rbf(var+1e-5f)));
  for(int i=0;i<8;i++) xn[t*D+tid*8+i]=f2bf(rbf(v[i]-mb)/sd);
}
__global__ void k_kgen(const u16* Are,const u16* Aim,const u16* Bre,const u16* Bim,const u16* Cre,const u16* Cim,const u16* ldt,float* K){
  int d=blockIdx.x;
  __shared__ float ar[N],ai[N],wr[N],wi[N];
  float dt=rbf(expf(bf2f(ldt[d])));
  if(threadIdx.x<N){ int n=threadIdx.x; int i=d*N+n;
    ar[n]=dt*bf2f(Are[i]); ai[n]=dt*bf2f(Aim[i]);
    float br=dt*bf2f(Bre[i]), bi=dt*bf2f(Bim[i]), cr=bf2f(Cre[i]), ci=bf2f(Cim[i]);
    wr[n]=cr*br-ci*bi; wi[n]=cr*bi+ci*br; }
  __syncthreads();
  for(int l=threadIdx.x;l<L;l+=blockDim.x){
    float lf = ROUND_L ? rbf((float)l) : (float)l;
    float acc=0;
    for(int n=0;n<N;n++){ float e=expf(ar[n]*lf); float s,c; sincosf(ai[n]*lf,&s,&c); acc+=e*(wr[n]*c-wi[n]*s); }
    K[d*L+l]=acc;
  }
}
__global__ void k_conv(const u16* xn,const float* K,float* y){
  int d=blockIdx.x; __shared__ float u[L], k[L];
  for(int i=threadIdx.x;i<L;i+=blockDim.x){ u[i]=bf2f(xn[i*D+d]); k[i]=K[d*L+i]; }
  __syncthreads();
  for(int t=threadIdx.x;t<L;t+=blockDim.x){ float a=0; for(int s=0;s<=t;s++) a+=k[t-s]*u[s]; y[t*D+d]=a; }
}
__global__ void k_g1(const u16* xn,const u16* wg,const u16* wu,u16* h){
  int t=blockIdx.x; __shared__ float xs[D];
  for(int i=threadIdx.x;i<D;i+=blockDim.x) xs[i]=bf2f(xn[t*D+i]);
  __syncthreads();
  for(int f=threadIdx.x;f<F;f+=blockDim.x){ float g=0,u=0; for(int k=0;k<D;k++){ g+=xs[k]*bf2f(wg[k*F+f]); u+=xs[k]*bf2f(wu[k*F+f]); }
    float ge=0.5f*g*(1.f+tanhf(0.7978845608f*(g+0.044715f*g*g*g))); h[t*F+f]=f2bf(ge*u); }
}
__global__ void k_g2(const u16* x,const u16* xn,const u16* h,const u16* wd,const float* y,const u16* Dk,u16* out){
  int t=blockIdx.x; __shared__ float hs[F];
  for(int i=threadIdx.x;i<F;i+=blockDim.x) hs[i]=bf2f(h[t*F+i]);
  __syncthreads();
  for(int d=threadIdx.x;d<D;d+=blockDim.x){ float a=0; for(int k=0;k<F;k++) a+=hs[k]*bf2f(wd[k*D+d]);
    out[t*D+d]=f2bf(bf2f(x[t*D+d]) + y[t*D+d] + bf2f(xn[t*D+d])*bf2f(Dk[d]) + a); }
}
extern "C" void kernel_launch(void* const* d_in, const int* in_sizes, int n_in, void* d_out, int out_size, void* d_ws, size_t ws_size, hipStream_t stream){
  const u16* x=(const u16*)d_in[0];
  char* ws=(char*)d_ws;
  u16* xn=(u16*)ws; float* K=(float*)(ws+(4<<20)); float* y=(float*)(ws+(12<<20)); u16* h=(u16*)(ws+(20<<20));
  hipFuncSetAttribute((const void*)kw, hipFuncAttributeMaxDynamicSharedMemorySize, 160*1024); kw<<<256,512,100*1024,stream>>>((char*)d_out,2u<<20,1);
  k_ln<<<L,64,0,stream>>>(x,xn);
  k_kgen<<<D,256,0,stream>>>((const u16*)d_in[1],(const u16*)d_in[2],(const u16*)d_in[3],(const u16*)d_in[4],(const u16*)d_in[5],(const u16*)d_in[6],(const u16*)d_in[7],K);
  k_conv<<<D,256,0,stream>>>(xn,K,y);
  k_g1<<<L,256,0,stream>>>(xn,(const u16*)d_in[10],(const u16*)d_in[11],h);
  k_g2<<<L,256,0,stream>>>(x,xn,h,(const u16*)d_in[12],y,(const u16*)d_in[8],(u16*)d_out);
}
```

```cpp
#include <hip/hip_runtime.h>
#include <hip/hip_bf16.h>
#include <stdint.h>
#pragma clang diagnostic ignored "-Wint-to-pointer-cast"

typedef unsigned long long u64;
typedef unsigned short u16;
typedef __bf16 bf2 __attribute__((ext_vector_type(2)));
typedef __bf16 bf4 __attribute__((ext_vector_type(4)));
typedef __bf16 bf8 __attribute__((ext_vector_type(8)));
typedef float f2 __attribute__((ext_vector_type(2)));
typedef float f4 __attribute__((ext_vector_type(4)));
typedef int i4 __attribute__((ext_vector_type(4)));
typedef unsigned u2 __attribute__((ext_vector_type(2)));
typedef unsigned u4 __attribute__((ext_vector_type(4)));
typedef __amdgpu_buffer_rsrc_t rsrc_t;
#define LDSP(T, a) ((__attribute__((address_space(3))) T*)(a))
#define DEV __device__ __forceinline__
#define SB() __builtin_amdgcn_sched_barrier(0)
#define SC1 16

constexpr int L = 2048, D = 512, NS = 128, F = 1792;

constexpr unsigned WS_EPOCH = 0;
constexpr unsigned WS_FLN = 4096;
constexpr unsigned WS_FK = 8192;
constexpr unsigned WS_FY = 16384;
constexpr unsigned WS_FH = 32768;
constexpr unsigned WS_STATS = 65536;
constexpr unsigned WS_CPART = 131072;
constexpr unsigned WS_XNT = 1u << 20;
constexpr unsigned WS_KEXP = 3u << 20;
constexpr unsigned WS_YT = 5u << 20;
constexpr unsigned WS_H = 7u << 20;
constexpr unsigned WS_BYTES = WS_H + 32u * 56u * 4096u;
constexpr unsigned WS_DBG = 16u << 20;

constexpr unsigned SLOT = 49152;
constexpr unsigned LKC = 114688;
constexpr unsigned KCS = 4160, KCH = 4 * KCS;
constexpr unsigned LEX = 147968;
constexpr unsigned LU = LEX;
constexpr unsigned UCH = 4608;
constexpr unsigned LCS = LEX + 9216;
constexpr unsigned LST = LEX + 10240;
constexpr unsigned LSY = LEX + 10752;
constexpr unsigned LJK = LEX + 11264;
constexpr unsigned LDS_BYTES = 163840;
constexpr unsigned KGB = 61440, KGW = 10240;

struct Params {
  const u16 *x, *are, *aim, *bre, *bim, *cre, *cim, *ldt, *dsk, *wg, *wu, *wd;
  u16* out;
  char* ws;
};

DEV i4 mksrd(const void* p, unsigned bytes) {
  u64 a = (u64)p; i4 r; r[0] = (int)(unsigned)a; r[1] = (int)(unsigned)(a >> 32); r[2] = (int)bytes; r[3] = 0x00020000; return r;
}
DEV rsrc_t mkrs(const void* p, unsigned bytes) { return __builtin_amdgcn_make_buffer_rsrc((void*)p, 0, (int)bytes, 0x00020000); }
DEV void glds(i4 srd, unsigned voff, unsigned soff, unsigned dst) {
  asm volatile("s_mov_b32 m0, %3\n\ts_nop 0\n\tbuffer_load_dwordx4 %1, %0, %2 offen lds" ::"s"(srd), "v"(voff), "s"(soff), "s"(dst) : "memory");
}
DEV void touch(i4 srd, unsigned voff, unsigned soff) {
  asm volatile("s_mov_b32 m0, %3\n\ts_nop 0\n\tbuffer_load_dword %1, %0, %2 offen lds" ::"s"(srd), "v"(voff), "s"(soff), "s"(LJK) : "memory");
}
template <int N> DEV void waitv() { asm volatile("s_waitcnt vmcnt(%0)" ::"i"(N) : "memory"); }
DEV void bar() { asm volatile("s_waitcnt lgkmcnt(0)\n\ts_barrier" ::: "memory"); }
DEV bf4 trd(unsigned a) { return __builtin_amdgcn_ds_read_tr16_b64_v4bf16(LDSP(bf4, a)); }
DEV bf8 cat(bf4 a, bf4 b) { return __builtin_shufflevector(a, b, 0, 1, 2, 3, 4, 5, 6, 7); }
template <class TA, class TB> DEV void mma(f4& c, TA a, TB b) { asm volatile("v_mfma_f32_16x16x32_bf16 %0, %1, %2, %0" : "+v"(c) : "v"(a), "v"(b)); }
template <class TA, class TB> DEV void mma0(f4& c, TA a, TB b) { asm volatile("v_mfma_f32_16x16x32_bf16 %0, %1, %2, 0" : "=&v"(c) : "v"(a), "v"(b)); }
DEV void mma_fence() { asm volatile("s_nop 7\n\ts_nop 1" ::: "memory"); }
DEV float blo(unsigned v) { return __uint_as_float(v << 16); }
DEV float bhi(unsigned v) { return __uint_as_float(v & 0xffff0000u); }
DEV f2 unpk(unsigned v) { return f2{blo(v), bhi(v)}; }
DEV unsigned pk(f2 v) { return __builtin_bit_cast(unsigned, __builtin_convertvector(v, bf2)); }
DEV float rbf(float v) { return blo(pk(f2{v, v})); }
DEV f2 fma2(f2 a, f2 b, f2 c) { return __builtin_elementwise_fma(a, b, c); }
template <int CTRL> DEV float dpp_add(float v) {
  return v + __builtin_bit_cast(float, __builtin_amdgcn_update_dpp(0, __builtin_bit_cast(int, v), CTRL, 0xf, 0xf, true));
}
DEV void swap16(unsigned& a, unsigned& b) { auto r = __builtin_amdgcn_permlane16_swap(a, b, false, false); a = r[0]; b = r[1]; }
DEV void swap32(unsigned& a, unsigned& b) { auto r = __builtin_amdgcn_permlane32_swap(a, b, false, false); a = r[0]; b = r[1]; }
DEV float swap16_add(float a, float b) { unsigned x = __float_as_uint(a), y = __float_as_uint(b); swap16(x, y); return __uint_as_float(x) + __uint_as_float(y); }
DEV float swap32_add(float a, float b) { unsigned x = __float_as_uint(a), y = __float_as_uint(b); swap32(x, y); return __uint_as_float(x) + __uint_as_float(y); }
DEV float red2(float a, float b) {
  float s = swap32_add(a, b);
  s = swap16_add(s, s);
  s = dpp_add<0x128>(s); s = dpp_add<0x124>(s); s = dpp_add<0x122>(s); s = dpp_add<0x121>(s);
  return s;
}
DEV float rdl(float v, int l) { return __builtin_bit_cast(float, __builtin_amdgcn_readlane(__builtin_bit_cast(int, v), l)); }
DEV unsigned uni(unsigned v) { return (unsigned)__builtin_amdgcn_readfirstlane((int)v); }
DEV u4 row_load(i4 srd, unsigned voff, unsigned soff) {
  u4 r; asm volatile("buffer_load_dwordx4 %0, %2, %1, %3 offen" : "=v"(r) : "s"(srd), "v"(voff), "s"(soff) : "memory"); return r;
}
constexpr unsigned SPIN_CAP = 1u << 15;
DEV u2 ld_flag(rsrc_t rs, unsigned voff, unsigned soff) { return __builtin_amdgcn_raw_buffer_load_b64(rs, voff, soff, SC1); }
DEV void st_flag(rsrc_t rs, u2 tag, unsigned soff, int lane) { if (lane == 0) __builtin_amdgcn_raw_buffer_store_b64(tag, rs, 0, soff, SC1); }
DEV void wait_flags(rsrc_t rs, u2 tag, u2 v, unsigned voff, unsigned soff) {
#pragma nounroll
  for (unsigned spin = 0; spin < SPIN_CAP; ++spin) {
    if (__all(v[0] == tag[0] && v[1] == tag[1])) break;
    __builtin_amdgcn_s_sleep(1);
    asm volatile("" ::: "memory");
    v = ld_flag(rs, voff, soff);
  }
  asm volatile("" ::: "memory");
}
DEV void lds_post(unsigned a, int lane) { asm volatile("s_waitcnt lgkmcnt(0)" ::: "memory"); if (lane == 0) *LDSP(volatile unsigned, a) = 1u; }
DEV void lds_wait(unsigned a) {
#pragma nounroll
  for (unsigned spin = 0; spin < (SPIN_CAP << 6); ++spin) { if (__builtin_amdgcn_readfirstlane(*LDSP(volatile unsigned, a)) != 0) break; }
  asm volatile("" ::: "memory");
}

#define STAMP(i)

struct Ctx {
  int xc, jj, w, v, lane, n, q;
  u2 tag;
  rsrc_t ws;
};

DEV void ln_compute(const Ctx& c, u4 ra, u4 rb, int tgp) {
  f2 a[4], b[4];
#pragma unroll
  for (int i = 0; i < 4; i++) { a[i] = unpk(ra[i]); b[i] = unpk(rb[i]); }
  f2 sa = (a[0] + a[1]) + (a[2] + a[3]), sb = (b[0] + b[1]) + (b[2] + b[3]);
  const float mean = red2(sa.x + sa.y, sb.x + sb.y) * (1.f / D);
  const float ma = rdl(mean, 0), mb = rdl(mean, 32);
  f2 qa = {0.f, 0.f}, qb = {0.f, 0.f};
#pragma unroll
  for (int i = 0; i < 4; i++) { f2 da = a[i] - ma, db = b[i] - mb; qa = fma2(da, da, qa); qb = fma2(db, db, qb); }
  const float var = red2(qa.x + qa.y, qb.x + qb.y) * (1.f / D);
  const float sd = __builtin_amdgcn_sqrtf(rbf(rbf(var) + 1e-5f));
  const unsigned st = pk(f2{mean, sd});
  const float r = __builtin_amdgcn_rcpf(bhi(st)), o = -blo(st) * r;
  const float r0 = rdl(r, 0), r1 = rdl(r, 32), o0 = rdl(o, 0), o1 = rdl(o, 32);
  u4 na, nb;
#pragma unroll
  for (int i = 0; i < 4; i++) { na[i] = pk(fma2(a[i], f2{r0, r0}, f2{o0, o0})); nb[i] = pk(fma2(b[i], f2{r1, r1}, f2{o1, o1})); }
  *LDSP(u4, LU + (2 * c.v) * 1088 + c.lane * 16) = na;
  *LDSP(u4, LU + (2 * c.v + 1) * 1088 + c.lane * 16) = nb;
  u2 s2 = {(unsigned)__builtin_amdgcn_readlane((int)st, 0), (unsigned)__builtin_amdgcn_readlane((int)st, 32)};
  if (c.lane == 0) __builtin_amdgcn_raw_buffer_store_b64(s2, c.ws, 0, WS_STATS + (tgp * 8 + 2 * c.v) * 4, SC1);
}
DEV void ln_store(const Ctx& c, int tgp, int i) {
  const unsigned a = LU + (c.n >> 2) * 1088 + (128 * c.v + 64 * i + 16 * c.q + 4 * (c.n & 3)) * 2;
  bf8 t = cat(trd(a), trd(a + 4 * 1088));
  __builtin_amdgcn_raw_buffer_store_b128(__builtin_bit_cast(u4, t), c.ws, c.lane * 16, WS_XNT + tgp * 8192 + (128 * c.v + 64 * i) * 16, SC1);
}
struct LnFlags { u4 a, b; };
DEV LnFlags lnflags_load(const Ctx& c) {
  LnFlags f;
  f.a = __builtin_amdgcn_raw_buffer_load_b128(c.ws, c.lane * 16, WS_FLN, SC1);
  f.b = __builtin_amdgcn_raw_buffer_load_b128(c.ws, c.lane * 16, WS_FLN + 1024, SC1);
  return f;
}
DEV void lnflags_wait(const Ctx& c, LnFlags f) {
#pragma nounroll
  for (unsigned spin = 0; spin < SPIN_CAP; ++spin) {
    const unsigned d = ((f.a[0] ^ c.tag[0]) | (f.a[2] ^ c.tag[0]) | (f.b[0] ^ c.tag[0]) | (f.b[2] ^ c.tag[0])) |
                       ((f.a[1] ^ c.tag[1]) | (f.a[3] ^ c.tag[1]) | (f.b[1] ^ c.tag[1]) | (f.b[3] ^ c.tag[1]));
    if (__all(d == 0)) break;
    __builtin_amdgcn_s_sleep(1);
    asm volatile("" ::: "memory");
    f = lnflags_load(c);
  }
  asm volatile("" ::: "memory");
  if (blockIdx.x == 0 && c.w == 0) st_flag(c.ws, c.tag, WS_EPOCH, c.lane);
}
DEV u4 u_load(const Ctx& c, int i) {
  const int cp = c.xc * 32 + c.jj;
  return __builtin_amdgcn_raw_buffer_load_b128(c.ws, (c.lane >> 1) * 8192 + (c.lane & 1) * 16, WS_XNT + (64 * c.v + 32 * i) * 8192 + cp * 32, SC1);
}
DEV void u_write(const Ctx& c, u4 r0, u4 r1) {
  const unsigned a = LU + (c.lane & 1) * UCH + 512 + (64 * c.v + (c.lane >> 1)) * 16;
  *LDSP(u4, a) = r0;
  *LDSP(u4, a + 512) = r1;
  if (c.v == 0) *LDSP(u4, LU + (c.lane & 1) * UCH + (c.lane >> 1) * 16) = u4{0, 0, 0, 0};
}

struct WdSrc { i4 srd; unsigned voff; int rot; };
DEV WdSrc wd_src(const Ctx& c, const Params& P) {
  WdSrc s; s.srd = mksrd(P.wd, F * D * 2);
  const int r = c.lane >> 3;
  s.voff = r * 1024 + (c.jj >> 2) * 128 + (((c.lane & 7) ^ (r & 6)) * 16);
  s.rot = ((c.jj >> 2) * 14) >> 3;
  return s;
}
DEV int kstep(const Ctx& c, int rot, int i) { int k = i + rot; k = k >= 14 ? k - 14 : k; return 14 * c.v + k; }
DEV void wd_issue(const Ctx& c, const WdSrc& s, int i, int r) {
  glds(s.srd, s.voff, kstep(c, s.rot, i) * 32768 + r * 8192, (4 * (i % 7) + c.v) * 4096 + r * 1024);
}

struct Frags { bf4 a[4][2]; bf8 b[4]; };
DEV void rd_piece(Frags& Fr, int i, unsigned wa0, unsigned wa1, unsigned xa, int ks) {
  if (i == 0) Fr.b[0] = *LDSP(bf8, xa);
  else if (i < 9) { const int t = (i - 1) >> 1, h = (i - 1) & 1; Fr.a[t][h] = trd(((t & 1) ? wa1 : wa0) + (t >> 1) * 16384 + ks * 8192 + h * 1024); }
  else Fr.b[i - 8] = *LDSP(bf8, xa + (i - 8) * 2048);
}
constexpr unsigned slot_of(int s) { return (unsigned)((s + 1) % 3) * SLOT; }

struct KRaw { u4 a[2]; u2 p[2]; };

template <int ROLE>
DEV void gemm1(Ctx& c, const Params& P, const WdSrc& wds, f4 (&acc)[4][4], u2& kfl) {
  const int p = (c.xc & 3) * 4 + (c.jj & 3), ft = (c.xc >> 2) * 7 + (c.jj >> 2);
  const int tgp = (c.xc & 3) * 64 + (c.xc >> 2) * 32 + c.jj;
  const int lane = c.lane, n = c.n, q = c.q, v = c.v;
  const i4 sx = mksrd(P.x, L * D * 2), sg = mksrd(P.wg, D * F * 2), su = mksrd(P.wu, D * F * 2);
  const int k0 = 2 * (c.xc & 3);
  const int xr = lane >> 3;
  const unsigned vx0 = (p * 128 + 16 * v + xr) * 1024 + (((lane & 7) ^ (xr >> 1)) * 16);
  const unsigned vx1 = (vx0 ^ 64) + 8192;
  const unsigned vw = (8 * v + q) * 3584 + ft * 256 + ((n ^ ((q | ((v & 1) << 2)) << 1)) * 16);
  auto issue = [&](int s, int sh, int i) {
    const int sk = (s + k0) & 7;
    const unsigned base = slot_of(s);
    if (i < 2) glds(sx, i ? vx1 : vx0, sk * 128 + sh * 65536, base + (8 * sh + 2 * v + i) * 1024);
    else { const int m = (i - 2) >> 1, r = i & 1;
      glds(m ? su : sg, vw, (sk * 64 + 32 * sh + 4 * r) * 3584, base + 16384 + m * 16384 + (8 * sh + 2 * v + r) * 1024); }
  };
  u4 xa_, xb_;
  if (ROLE == 0) {
    xa_ = row_load(sx, lane * 16, (tgp * 8 + 2 * v) * 1024);
    xb_ = row_load(sx, lane * 16, (tgp * 8 + 2 * v + 1) * 1024);
    SB();
  }
#pragma unroll
  for (int i = 0; i < 6; i++) issue(0, ROLE, i);
  asm volatile("s_barrier" ::: "memory");
  if (ROLE == 1) {
#pragma unroll
    for (int i = 0; i < 12; i++) issue(1, i & 1, i >> 1);
    waitv<12>();
  } else {
    asm volatile("s_waitcnt vmcnt(6)" : "+v"(xa_), "+v"(xb_)::"memory");
    ln_compute(c, xa_, xb_, tgp);
    waitv<1>();
  }
  STAMP(1);
  const unsigned xa0 = (ROLE * 64 + n) * 128 + ((q ^ (n >> 1)) * 16), xa1 = xa0 ^ 64;
  const unsigned wa0 = 16384 + (8 * q + (n >> 2)) * 256 + (((2 * v) ^ ((n >> 2) | ((q & 1) << 2))) * 32) + (n & 3) * 8, wa1 = wa0 ^ 32;
  unsigned wc0 = wa0 + slot_of(0), wc1 = wa1 + slot_of(0), xn0 = xa0 + slot_of(0);
  asm volatile("" : "+v"(wc0), "+v"(wc1), "+v"(xn0) : "s"(c.tag[0]), "s"(c.tag[1]));
  bar();
  STAMP(2);
  Frags F0, F1;
#pragma unroll
  for (int i = 0; i < 12; i++) rd_piece(F0, i, wc0, wc1, xn0, 0);
  SB();
  auto colsum = [&](const Frags& Fr, int rel) {
    if (ROLE == 0 && (c.jj & 3) == rel) {
      u4 sel[4];
#pragma unroll
      for (int t = 0; t < 4; t++) { const unsigned o = n == t ? 0x3f803f80u : 0u; sel[t] = u4{o, o, o, o}; asm volatile("" : "+v"(sel[t])); }
      asm volatile("s_nop 1");
      f4 cs;
      mma0(cs, cat(Fr.a[0][0], Fr.a[0][1]), sel[0]);
#pragma unroll
      for (int t = 1; t < 4; t++) mma(cs, cat(Fr.a[t][0], Fr.a[t][1]), sel[t]);
      mma_fence();
      asm volatile("" : "+v"(cs));
      if (n < 4) __builtin_amdgcn_raw_buffer_store_b128(__builtin_bit_cast(u4, cs), c.ws, n * 64 + q * 16, WS_CPART + (ft * 16 + p) * 1024 + v * 256, SC1);
    }
    SB();
  };
  LnFlags lf; u4 cpr[4], ur[2]; unsigned str;
#pragma unroll
  for (int s = 0; s < 8; s++) {
    unsigned wn0 = wa0 + slot_of(s + 1), wn1 = wa1 + slot_of(s + 1), xc1 = xa1 + slot_of(s);
    xn0 = xa0 + slot_of(s + 1);
    asm volatile("" : "+v"(wn0), "+v"(wn1), "+v"(xn0), "+v"(xc1));
#pragma unroll
    for (int i = 0; i < 16; i++) {
      if (s == 0) mma0(acc[i >> 2][i & 3], cat(F0.a[i & 3][0], F0.a[i & 3][1]), F0.b[i >> 2]);
      else mma(acc[i >> 2][i & 3], cat(F0.a[i & 3][0], F0.a[i & 3][1]), F0.b[i >> 2]);
      if (i < 12) rd_piece(F1, i, wc0, wc1, xc1, 1);
#pragma unroll
      for (int g = 0; g < 6; g++) if ((g * 16 + 4) / 6 == i) {
        if (s == 0) issue(2, ROLE, g);
        else if (ROLE == 1 && s < 6) issue(s + 2, 1, g);
        else if (ROLE == 1) wd_issue(c, wds, 3 * (s - 6) + ((g + 6) >> 2), (g + 6) & 3);
      }
      if (ROLE == 0) {
        if (s == 0 && (i == 12 || i == 14)) ln_store(c, tgp, (i - 12) >> 1);
        if (s == 6 && i == 15) lnflags_wait(c, lf);
        if (s == 7 && i == 6) {
          f4 t = (__builtin_bit_cast(f4, cpr[0]) + __builtin_bit_cast(f4, cpr[1])) + (__builtin_bit_cast(f4, cpr[2]) + __builtin_bit_cast(f4, cpr[3]));
          const float z = swap16_add(swap32_add(t[0], t[1]), swap32_add(t[2], t[3]));
          *LDSP(float, LCS + v * 256 + n * 16 + (((q & 1) << 1) | (q >> 1)) * 4) = z;
          *LDSP(unsigned, LST + (32 * v + (lane & 31)) * 4) = str;
        }
        if (s == 7 && i == 11) u_write(c, ur[0], ur[1]);
      }
      SB();
    }
    if (s < 2) colsum(F0, 2 * s);
    if (ROLE == 0) { if (s == 2 || s == 6) waitv<0>(); else if (s >= 1 && s < 6) waitv<6>(); }
    else if (s < 6) waitv<6>(); else if (s == 6) waitv<12>();
    bar();
    if (s == 0) STAMP(3);
    if (s == 4) STAMP(4);
    SB();
    if (ROLE == 0 && s == 2 && c.w == 0) st_flag(c.ws, c.tag, WS_FLN + blockIdx.x * 8, lane);
#pragma unroll
    for (int i = 0; i < 16; i++) {
      mma(acc[i >> 2][i & 3], cat(F1.a[i & 3][0], F1.a[i & 3][1]), F1.b[i >> 2]);
      if (s < 7 && i < 12) rd_piece(F0, i, wn0, wn1, xn0, 0);
#pragma unroll
      for (int g = 0; g < 6; g++) if ((g * 16 + 4) / 6 == i) {
        if (ROLE == 0 && s < 5) issue(s + 3, 0, g);
        else if (ROLE == 1 && s >= 5 && (s < 7 || g < 4)) wd_issue(c, wds, 3 * (s - 5) + (g >> 2), g & 3);
      }
      if (ROLE == 1 && s == 2 && i == 1 && c.w == 4) touch(wds.srd, lane * 128, blockIdx.x * 7168);
      if (ROLE == 0) {
        if (s == 5 && i == 2) lf = lnflags_load(c);
        if (s == 6 && i < 4) cpr[i] = __builtin_amdgcn_raw_buffer_load_b128(c.ws, q * 1024 + n * 16, WS_CPART + ft * 16384 + i * 4096 + v * 256, SC1);
        if (s == 6 && i == 4) str = __builtin_amdgcn_raw_buffer_load_b32(c.ws, (lane & 31) * 4, WS_STATS + (p * 128 + 32 * v) * 4, SC1);
        if (s == 6 && (i == 6 || i == 10)) ur[(i - 6) >> 2] = u_load(c, (i - 6) >> 2);
        if (s == 7 && i == 0) kfl = ld_flag(c.ws, 0, WS_FK + (c.xc * 32 + c.jj) * 8);
      }
      SB();
    }
    if (s < 2) colsum(F1, 2 * s + 1);
    wc0 = wn0; wc1 = wn1;
  }
  STAMP(5);
}

template <int ROLE>
DEV void geglu(const Ctx& c, f4 (&acc)[4][4]) {
  const int p = (c.xc & 3) * 4 + (c.jj & 3), ft = (c.xc >> 2) * 7 + (c.jj >> 2);
  f4 cv[4]; unsigned st[4];
#pragma unroll
  for (int t = 0; t < 4; t++) { cv[t] = *LDSP(f4, LCS + c.v * 256 + t * 64 + c.q * 16); st[t] = *LDSP(unsigned, LST + (64 * ROLE + 16 * t + c.n) * 4); }
  mma_fence();
#pragma unroll
  for (int i = 0; i < 16; i++) asm volatile("" : "+v"(acc[i >> 2][i & 3]));
  const unsigned hbase = WS_H + uni((2 * p + ROLE) * 56 + 4 * ft + c.v) * 4096;
#pragma unroll
  for (int tt = 0; tt < 4; tt++) {
    const float nm = -blo(st[tt]), sd = bhi(st[tt]), r = __builtin_amdgcn_rcpf(sd);
    const float k0 = (-2.f * 0.7978845608028654f * 1.4426950408889634f) * r, k1 = k0 * r * r * 0.044715f, s2 = sd * sd;
    u4 o;
#pragma unroll
    for (int j = 0; j < 4; j++) {
      const int t = j >> 1, e = (j & 1) * 2;
      const f2 a = fma2(f2{nm, nm}, f2{cv[t][e], cv[t][e + 1]}, f2{acc[tt][t][e], acc[tt][t][e + 1]});
      const f2 b = fma2(f2{nm, nm}, f2{cv[t + 2][e], cv[t + 2][e + 1]}, f2{acc[tt][t + 2][e], acc[tt][t + 2][e + 1]});
      const f2 z = fma2(a * a, f2{k1, k1}, f2{k0, k0}) * a;
      const f2 ex = {__builtin_amdgcn_exp2f(z.x), __builtin_amdgcn_exp2f(z.y)};
      const f2 d = fma2(ex, f2{s2, s2}, f2{s2, s2});
      const f2 rc = {__builtin_amdgcn_rcpf(d.x), __builtin_amdgcn_rcpf(d.y)};
      o[j] = pk(a * b * rc);
    }
    __builtin_amdgcn_raw_buffer_store_b128(o, c.ws, c.lane * 16 + tt * 1024, hbase, SC1);
    SB();
  }
}
template <int ROLE> DEV void h_publish(const Ctx& c) {
  const int p = (c.xc & 3) * 4 + (c.jj & 3), ft = (c.xc >> 2) * 7 + (c.jj >> 2);
  waitv<0>();
  st_flag(c.ws, c.tag, WS_FH + uni((2 * p + ROLE) * 56 + 4 * ft + c.v) * 8, c.lane);
}

struct C2 { f2 re, im; };
DEV C2 cmul(C2 a, C2 b) { C2 r; r.re = fma2(a.re, b.re, -(a.im * b.im)); r.im = fma2(a.re, b.im, a.im * b.re); return r; }
DEV C2 cexp2(f2 al, f2 th) {
  const f2 e = {__builtin_amdgcn_exp2f(al.x), __builtin_amdgcn_exp2f(al.y)};
  C2 r; r.re = e * f2{__builtin_amdgcn_cosf(th.x), __builtin_amdgcn_cosf(th.y)}; r.im = e * f2{__builtin_amdgcn_sinf(th.x), __builtin_amdgcn_sinf(th.y)};
  return r;
}
DEV f2 fract2(f2 v) { return f2{__builtin_amdgcn_fractf(v.x), __builtin_amdgcn_fractf(v.y)}; }
struct KPar { unsigned are, aim, bre, bim, cre, cim; };
DEV void kgen_channel(const Ctx& c, const KPar& k, float ldt, float dsk, int ch) {
  const unsigned T = KGB + c.w * KGW, CC = T + 8704;
  const unsigned wrp = T + c.lane * 8, rdp = T + c.n * 544 + c.q * 16;
  const float dt = rbf(__builtin_amdgcn_exp2f(ldt * 1.4426950408889634f));
  const f2 al = (unpk(k.are) * dt) * 1.4426950408889634f;
  const f2 th = fract2((unpk(k.aim) * dt) * 0.15915494309189535f);
  C2 wv = cmul(C2{unpk(k.cre), unpk(k.cim)}, C2{unpk(k.bre) * dt, unpk(k.bim) * dt});
  auto put = [&](int r, C2 z) { *LDSP(u2, wrp + r * 544) = u2{pk(z.re), pk(z.im)}; };
  auto tile = [&](const bf8 (&B)[8]) {
    bf8 A[8];
#pragma unroll
    for (int i = 0; i < 8; i++) A[i] = *LDSP(bf8, rdp + i * 64);
    f4 a0 = {0.f, 0.f, 0.f, 0.f}, a1 = {0.f, 0.f, 0.f, 0.f};
#pragma unroll
    for (int i = 0; i < 8; i += 2) {
      a0 = __builtin_amdgcn_mfma_f32_16x16x32_bf16(A[i], B[i], a0, 0, 0, 0);
      a1 = __builtin_amdgcn_mfma_f32_16x16x32_bf16(A[i + 1], B[i + 1], a1, 0, 0, 0);
    }
    return a0 + a1;
  };
  C2 z[9];
  z[1] = cexp2(al, th);
  z[2] = cmul(z[1], z[1]); z[3] = cmul(z[2], z[1]); z[4] = cmul(z[2], z[2]); z[5] = cmul(z[4], z[1]);
  z[6] = cmul(z[4], z[2]); z[7] = cmul(z[4], z[3]); z[8] = cmul(z[4], z[4]);
  bf8 B[8];
  {
    C2 b = {wv.re, -wv.im}; const C2 s = {z[8].re, -z[8].im};
#pragma unroll
    for (int j = 0; j < 16; j++) { put(j, b); if (j < 15) b = cmul(b, s); }
#pragma unroll
    for (int i = 0; i < 8; i++) B[i] = *LDSP(bf8, rdp + i * 64);
  }
  const C2 p1 = cexp2(al * 128.f, fract2(th * 128.f));
  put(0, C2{f2{1.f, 1.f}, f2{0.f, 0.f}});
#pragma unroll
  for (int i = 1; i < 8; i++) put(i, z[i]);
  put(8, p1);
#pragma unroll
  for (int i = 1; i < 8; i++) put(8 + i, cmul(p1, z[i]));
  f4 t0 = tile(B);
  C2 pp = p1;
#pragma unroll
  for (int i = 0; i < 2; i++) { pp = cmul(pp, p1); put(4 * i, pp); put(4 * i + 1, cmul(pp, z[2])); put(4 * i + 2, cmul(pp, z[4])); put(4 * i + 3, cmul(pp, z[6])); }
#pragma unroll
  for (int i = 0; i < 4; i++) { pp = cmul(pp, p1); put(8 + 2 * i, pp); put(9 + 2 * i, cmul(pp, z[4])); }
  f4 t1 = tile(B);
#pragma unroll
  for (int i = 0; i < 9; i++) { pp = cmul(pp, p1); put(i, pp); }
  f4 t2 = tile(B);
  if (c.lane == 0) t0[0] += dsk;
  const int n = c.n, q = c.q;
  *LDSP(u2, CC + (128 * (q >> 1) + 4 * (q & 1) + 8 * n) * 2) = u2{pk(f2{t0[0], t0[1]}), pk(f2{t0[2], t0[3]})};
  const unsigned a1 = CC + (q < 2 ? 256 + 64 * q + 4 * n : 256 + 64 * q + 2 * n) * 2;
  *LDSP(unsigned, a1) = pk(f2{t1[0], t1[1]});
  *LDSP(unsigned, a1 + (q < 2 ? 4 : 64)) = pk(f2{t1[2], t1[3]});
#pragma unroll
  for (int i = 0; i < 4; i++) *LDSP(u16, CC + (512 + 64 * q + 16 * i + n) * 2) = (u16)pk(f2{t2[i], t2[i]});
  const unsigned kb = WS_KEXP + ch * 4096;
  const u2 s0 = *LDSP(u2, CC + c.lane * 8);
  unsigned d0[3], d1[3];
#pragma unroll
  for (int i = 0; i < 3; i++) { d0[i] = *LDSP(unsigned, CC + 512 + 256 * i + c.lane * 4); d1[i] = *LDSP(unsigned, CC + 516 + 256 * i + c.lane * 4); }
  auto lo2 = [](unsigned d) { return __builtin_amdgcn_perm(d, d, 0x01000100u); };
  auto hi2 = [](unsigned d) { return __builtin_amdgcn_perm(d, d, 0x03020302u); };
  __builtin_amdgcn_raw_buffer_store_b64(s0, c.ws, c.lane * 8, kb, SC1);
  __builtin_amdgcn_raw_buffer_store_b64(u2{lo2(d0[0]), __builtin_amdgcn_alignbit(d1[0], d0[0], 16)}, c.ws, c.lane * 8, kb + 512, SC1);
  __builtin_amdgcn_raw_buffer_store_b128(u4{lo2(d0[1]), d0[1], hi2(d0[1]), lo2(d1[1])}, c.ws, c.lane * 16, kb + 1024, SC1);
  __builtin_amdgcn_raw_buffer_store_b128(u4{lo2(d0[2]), lo2(d0[2]), d0[2], hi2(d0[2])}, c.ws, c.lane * 32, kb + 2048, SC1);
  __builtin_amdgcn_raw_buffer_store_b128(u4{hi2(d0[2]), hi2(d0[2]), lo2(d1[2]), lo2(d1[2])}, c.ws, c.lane * 32 + 16, kb + 2048, SC1);
}
DEV void kgen_block(Ctx& c, const Params& P) {
  const int tgp = (c.xc & 3) * 64 + (c.xc >> 2) * 32 + c.jj;
  const int pair = c.xc * 32 + (c.jj - 28) * 8 + c.w;
  KPar k[2];
#pragma unroll
  for (int i = 0; i < 2; i++) {
    const int o = (2 * pair + i) * 64 + c.lane;
    k[i].are = ((const unsigned*)P.are)[o]; k[i].aim = ((const unsigned*)P.aim)[o]; k[i].bre = ((const unsigned*)P.bre)[o];
    k[i].bim = ((const unsigned*)P.bim)[o]; k[i].cre = ((const unsigned*)P.cre)[o]; k[i].cim = ((const unsigned*)P.cim)[o];
  }
  const unsigned ldt = ((const unsigned*)P.ldt)[pair], dsk = ((const unsigned*)P.dsk)[pair];
  if (c.w < 4) {
    const rsrc_t rx = mkrs(P.x, L * D * 2);
    const u4 xa_ = __builtin_amdgcn_raw_buffer_load_b128(rx, c.lane * 16, (tgp * 8 + 2 * c.v) * 1024, 0);
    const u4 xb_ = __builtin_amdgcn_raw_buffer_load_b128(rx, c.lane * 16, (tgp * 8 + 2 * c.v + 1) * 1024, 0);
    ln_compute(c, xa_, xb_, tgp);
  }
  STAMP(1);
  asm volatile("" ::"s"(c.tag[0]), "s"(c.tag[1]));
  bar();
  if (c.w < 4) { ln_store(c, tgp, 0); ln_store(c, tgp, 1); }
#pragma unroll 1
  for (int i = 0; i < 2; i++) {
    kgen_channel(c, i ? k[1] : k[0], i ? bhi(ldt) : blo(ldt), i ? bhi(dsk) : blo(dsk), 2 * pair + i);
    waitv<0>();
    if (i == 0) { bar(); if (c.w == 0) st_flag(c.ws, c.tag, WS_FLN + blockIdx.x * 8, c.lane); STAMP(3); }
  }
  st_flag(c.ws, c.tag, WS_FK + pair * 8, c.lane);
  STAMP(4);
}

DEV KRaw k_load(const Ctx& c) {
  const int ch = 2 * (c.xc * 32 + c.jj) + (c.v & 1), kh = c.v >> 1;
  KRaw r;
#pragma unroll
  for (int i = 0; i < 2; i++) {
    r.a[i] = __builtin_amdgcn_raw_buffer_load_b128(c.ws, c.lane * 16, WS_KEXP + ch * 4096 + kh * 1024 + i * 2048, SC1);
    r.p[i] = __builtin_amdgcn_raw_buffer_load_b64(c.ws, c.lane * 16, WS_KEXP - 8 + ch * 4096 + kh * 1024 + i * 2048, SC1);
  }
  return r;
}
DEV void k_write(const Ctx& c, KRaw r) {
  const int kh = c.v >> 1;
  const unsigned base = LKC + (c.v & 1) * KCH;
  if (kh == 0 && c.lane == 0) r.p[0] = u2{0, 0};
#pragma unroll
  for (int i = 0; i < 2; i++) {
    const unsigned a = base + 32 + kh * 1024 + i * 2048 + c.lane * 16;
    const u4 d = r.a[i]; const u2 pv = r.p[i];
    const unsigned e0 = __builtin_amdgcn_alignbit(pv[1], pv[0], 16), e1 = __builtin_amdgcn_alignbit(d[0], pv[1], 16),
                   e2 = __builtin_amdgcn_alignbit(d[1], d[0], 16), e3 = __builtin_amdgcn_alignbit(d[2], d[1], 16), e4 = __builtin_amdgcn_alignbit(d[3], d[2], 16);
    *LDSP(u4, a) = d;
    *LDSP(u4, a + KCS) = u4{e1, e2, e3, e4};
    *LDSP(u4, a + 2 * KCS) = u4{pv[1], d[0], d[1], d[2]};
    *LDSP(u4, a + 3 * KCS) = u4{e0, e1, e2, e3};
  }
  if (kh == 0 && c.lane < 8) *LDSP(u4, base + (c.lane >> 1) * KCS + (c.lane & 1) * 16) = u4{0, 0, 0, 0};
}
struct CFr { bf4 a[8][2]; bf8 b[8]; };
DEV void conv_rd(CFr& Fr, int i, unsigned ka, unsigned ua, int sp) {
  if (i < 2) Fr.a[0][i] = trd(ka + 64 * sp + (i ? 0 : 8));
  else if (i < 10) Fr.b[i - 2] = *LDSP(bf8, ua + 512 * (i - 2) + 192 - 64 * sp);
  else { const int g = (i - 8) >> 1, t = i & 1; Fr.a[g][t] = trd(ka + 512 * g + 64 * sp + (t ? 0 : 8)); }
}
template <bool G1> DEV void conv_publish_h(const Ctx& c) { if (G1) h_publish<0>(c); }
DEV void conv(Ctx& c, bool g1) {
  const int chl = c.v & 1, kh = c.v >> 1, n = c.n, q = c.q;
  unsigned ka = LKC + chl * KCH + (n >> 2) * KCS + 2 * (16 + 4 * (n & 3) + 128 * kh + 16 - 8 * q) - 8;
  unsigned ua = LU + chl * UCH + 2 * (256 + 16 * n - 16 - 128 * kh + 8 * q) - 192;
  asm volatile("" : "+v"(ka), "+v"(ua));
  f4 acc[8];
  CFr Fr[2];
#pragma unroll
  for (int i = 0; i < 24; i++) conv_rd(Fr[0], i, ka, ua, 0);
  SB();
#pragma unroll
  for (int sp = 0; sp < 4; sp++) {
    int i = 0;
#pragma unroll
    for (int g = 0; g < 8; g++)
#pragma unroll
      for (int m = 0; m + g < 8; m++) {
        if (sp == 0 && g == 0) mma0(acc[m], cat(Fr[0].a[0][0], Fr[0].a[0][1]), Fr[0].b[m]);
        else mma(acc[g + m], cat(Fr[sp & 1].a[g][0], Fr[sp & 1].a[g][1]), Fr[sp & 1].b[m]);
        if (sp < 3 && i < 24) conv_rd(Fr[(sp + 1) & 1], i, ka, ua, sp + 1);
        i++;
        SB();
      }
    if (sp == 0 && g1) { h_publish<0>(c); SB(); }
  }
  mma_fence();
#pragma unroll
  for (int i = 0; i < 8; i++) asm volatile("" : "+v"(acc[i]));
  const unsigned xw = LKC + chl * KCH + 32 + (c.lane >> 3) * 512 + (c.lane & 7) * 16;
  f4 own[4];
#pragma unroll
  for (int j = 0; j < 4; j++) { *LDSP(f4, xw + kh * 256 + j * KCS) = kh ? acc[j] : acc[4 + j]; own[j] = kh ? acc[4 + j] : acc[j]; }
  lds_post(LSY + 16 + c.v * 4, c.lane);
  lds_wait(LSY + 16 + (c.v ^ 2) * 4);
  unsigned pkd[4][2];
#pragma unroll
  for (int j = 0; j < 4; j++) { const f4 t = own[j] + *LDSP(f4, xw + (kh ^ 1) * 256 + j * KCS); pkd[j][0] = pk(f2{t[0], t[1]}); pkd[j][1] = pk(f2{t[2], t[3]}); }
  const int ch = 2 * (c.xc * 32 + c.jj) + chl;
#pragma unroll
  for (int j = 0; j < 4; j += 2) {
    swap16(pkd[j][0], pkd[j + 1][0]); swap16(pkd[j][1], pkd[j + 1][1]);
    __builtin_amdgcn_raw_buffer_store_b128(u4{pkd[j][0], pkd[j][1], pkd[j + 1][0], pkd[j + 1][1]}, c.ws,
                                           (q & 1) * 512 + n * 32 + (q >> 1) * 16 + j * 512, WS_YT + ch * 4096 + kh * 2048, SC1);
  }
  STAMP(9);
  waitv<0>();
  st_flag(c.ws, c.tag, WS_FY + (kh * 512 + ch) * 8, c.lane);
}
DEV void y_gather(Ctx& c) {
  const int T64 = c.xc * 4 + (c.jj & 3), ct = c.jj >> 2;
  const int r0 = 32 * (c.v & 1) + 16 * (c.v >> 1);
  const unsigned fo = WS_FY + ((T64 >> 4) * 512 + 64 * ct + r0) * 8;
  wait_flags(c.ws, c.tag, ld_flag(c.ws, c.n * 8, fo), c.n * 8, fo);
  STAMP(10);
#pragma unroll
  for (int i = 0; i < 2; i++) {
    const u4 t = __builtin_amdgcn_raw_buffer_load_b128(c.ws, (c.lane >> 3) * 4096 + (c.lane & 7) * 16, WS_YT + (64 * ct + r0 + 8 * i) * 4096 + T64 * 128, SC1);
    *LDSP(u4, LU + (r0 + 8 * i + (c.lane >> 3)) * 144 + (c.lane & 7) * 16) = t;
  }
}

DEV void gemm2(Ctx& c, const Params& P, const WdSrc& wds, const bf8 (&W1)[7][4], unsigned wra) {
  const int T64 = c.xc * 4 + (c.jj & 3), ct = c.jj >> 2, n = c.n, q = c.q, v = c.v;
  const unsigned hk = uni(T64 * 56);
  const unsigned fo = WS_FH + (hk + 14 * v) * 8, fv = (n < 14 ? n : 13) * 8;
  wait_flags(c.ws, c.tag, ld_flag(c.ws, fv, fo), fv, fo);
  STAMP(8);
  constexpr int WIN = 3;
  u4 H[14][4];
  auto hload = [&](int i, int t) {
    H[i][t] = __builtin_amdgcn_raw_buffer_load_b128(c.ws, c.lane * 16, WS_H + (hk + kstep(c, wds.rot, i)) * 4096 + ((t ^ v) * 1024), SC1);
  };
#pragma unroll
  for (int i = 0; i < WIN; i++)
#pragma unroll
    for (int t = 0; t < 4; t++) hload(i, t);
  SB();
  f4 acc[4][4];
  bf4 W2[7][4][2];
  u2 xr[4];
  unsigned wa[4];
#pragma unroll
  for (int d = 0; d < 4; d++) { wa[d] = wra ^ (d * 32); asm volatile("" : "+v"(wa[d])); }
  const rsrc_t rx = mkrs(P.x, L * D * 2);
#pragma unroll
  for (int i = 0; i < 14; i++) {
    if (i == 10) {
#pragma unroll
      for (int d = 0; d < 4; d++) { wa[d] += 65536; asm volatile("" : "+v"(wa[d])); }
    }
#pragma unroll
    for (int j = 0; j < 16; j++) {
      const int t = j >> 2, d = j & 3;
      if (i == 0) mma0(acc[t][d], W1[0][d], H[0][t]);
      else if (i < 7) mma(acc[t][d], W1[i][d], H[i][t]);
      else mma(acc[t][d], cat(W2[i - 7][d][0], W2[i - 7][d][1]), H[i][t]);
      if (i < 2 && (j & 3) == 3) hload(i + 3, t);
      if (i >= 2 && i < 4 && (j & 1) == 1) hload(2 * i + 1 + (j >> 3), (j >> 1) & 3);
      if (i >= 4 && i + 5 < 14 && (j & 3) == 1) hload(i + 5, t);
      if (i >= 6 && i < 13 && j < 8) W2[i - 6][j >> 1][j & 1] = trd(wa[j >> 1] + ((i - 6) & 3) * 16384 + (j & 1) * 2048);
      if (i == 9 && (j & 3) == 1) xr[t] = __builtin_amdgcn_raw_buffer_load_b64(rx, n * 1024 + q * 8, (T64 * 64 + 16 * v) * 1024 + ct * 128 + t * 32, 0);
      SB();
    }
  }
  STAMP(9);
  mma_fence();
#pragma unroll
  for (int i = 0; i < 16; i++) asm volatile("" : "+v"(acc[i >> 2][i & 3]));
#pragma unroll
  for (int t = 1; t < 4; t++)
#pragma unroll
    for (int d = 0; d < 4; d++) *LDSP(f4, (4 * (t - 1) + v) * 4096 + d * 1024 + c.lane * 16) = acc[t][d];
  bar();
  STAMP(10);
  unsigned o[4][2];
#pragma unroll
  for (int d = 0; d < 4; d++) {
    f4 s = acc[0][d];
#pragma unroll
    for (int t = 1; t < 4; t++) s += *LDSP(f4, (4 * (t - 1) + (v ^ t)) * 4096 + d * 1024 + c.lane * 16);
    const u2 y = __builtin_bit_cast(u2, trd(LU + (16 * d + 4 * q + (n >> 2)) * 144 + (16 * v + 4 * (n & 3)) * 2));
    o[d][0] = pk(f2{s[0], s[1]} + unpk(y[0]) + unpk(xr[d][0]));
    o[d][1] = pk(f2{s[2], s[3]} + unpk(y[1]) + unpk(xr[d][1]));
  }
  const rsrc_t ro = mkrs(P.out, L * D * 2);
#pragma unroll
  for (int d = 0; d < 4; d += 2) {
    swap16(o[d][0], o[d + 1][0]); swap16(o[d][1], o[d + 1][1]);
    __builtin_amdgcn_raw_buffer_store_b128(u4{o[d][0], o[d][1], o[d + 1][0], o[d + 1][1]}, ro,
                                           n * 1024 + (q & 1) * 32 + (q >> 1) * 16 + d * 32, (T64 * 64 + 16 * v) * 1024 + ct * 128, 0);
  }
}

__global__ void __launch_bounds__(512, 2) fused_s4_geglu(Params P) {
  Ctx c;
  STAMP(0);
  c.w = __builtin_amdgcn_readfirstlane(threadIdx.x >> 6);
  c.v = c.w & 3; c.lane = threadIdx.x & 63; c.n = c.lane & 15; c.q = c.lane >> 4;
  c.xc = blockIdx.x & 7; c.jj = blockIdx.x >> 3;
  {
    u64 pc; unsigned len;
    asm volatile("s_getpc_b64 %0\n.Lpc%=:\n\ts_mov_b32 %1, fused_code_end-.Lpc%=" : "=s"(pc), "=s"(len));
    if (c.w == 7) touch(mksrd((const void*)pc, len), c.lane * 128, (c.jj & 7) * 8192);
  }
  c.ws = mkrs(P.ws, WS_BYTES);
  { const u64 e = *(const __attribute__((address_space(4))) u64*)(P.ws + WS_EPOCH) + 1; c.tag = u2{(unsigned)e, (unsigned)(e >> 32)}; }
  if (threadIdx.x < 8) *LDSP(unsigned, LSY + threadIdx.x * 4) = 0u;
  const bool g1 = c.jj < 28;
  if (!g1) kgen_block(c, P);
  if (c.w < 4) {
    f4 acc[4][4]; u2 kfl; WdSrc none;
    const unsigned kfo = WS_FK + (c.xc * 32 + c.jj) * 8;
    if (g1) gemm1<0>(c, P, none, acc, kfl);
    else {
      lnflags_wait(c, lnflags_load(c));
      const u4 r0 = u_load(c, 0), r1 = u_load(c, 1);
      kfl = ld_flag(c.ws, 0, kfo);
      u_write(c, r0, r1);
      asm volatile("" : "+v"(kfl));
      bar();
    }
    wait_flags(c.ws, c.tag, kfl, 0, kfo);
    const KRaw kr = k_load(c);
    SB();
    STAMP(6);
    if (g1) geglu<0>(c, acc);
    STAMP(7);
    k_write(c, kr);
    lds_post(LSY + c.v * 4, c.lane);
    lds_wait(LSY + (c.v ^ 2) * 4);
    STAMP(8);
    conv(c, g1);
    y_gather(c);
    bar();
  } else {
    const WdSrc wds = wd_src(c, P);
    f4 acc[4][4]; u2 kfl;
    if (g1) gemm1<1>(c, P, wds, acc, kfl);
    else {
      bar();
#pragma unroll
      for (int i = 0; i < 28; i++) wd_issue(c, wds, i >> 2, i & 3);
    }
    const unsigned wra = c.v * 4096 + (4 * c.q + (c.n >> 2)) * 128 + (((2 * c.q + (c.n >> 3)) & 3) * 32) + (c.n & 3) * 8;
    bf8 W1[7][4];
    unsigned wa[4];
#pragma unroll
    for (int d = 0; d < 4; d++) { wa[d] = wra ^ (d * 32); asm volatile("" : "+v"(wa[d])); }
    waitv<4>();
#pragma unroll
    for (int i = 0; i < 7; i++) {
      if (i == 4) {
#pragma unroll
        for (int d = 0; d < 4; d++) { wa[d] += 65536; asm volatile("" : "+v"(wa[d])); }
      }
      if (i == 6) waitv<24>();
#pragma unroll
      for (int d = 0; d < 4; d++) W1[i][d] = cat(trd(wa[d] + (i & 3) * 16384), trd(wa[d] + (i & 3) * 16384 + 2048));
      asm volatile("s_waitcnt lgkmcnt(0)" ::: "memory");
#pragma unroll
      for (int r = 0; r < 4; r++) wd_issue(c, wds, 7 + i, r);
      SB();
    }
    STAMP(6);
    if (g1) { __builtin_amdgcn_s_setprio(1); geglu<1>(c, acc); __builtin_amdgcn_s_setprio(0); STAMP(7); h_publish<1>(c); }
    else waitv<0>();
    gemm2(c, P, wds, W1, wra);
  }
  asm volatile("fused_code_end:");
}

extern "C" void kernel_launch(void* const* d_in, const int* in_sizes, int n_in, void* d_out, int out_size, void* d_ws, size_t ws_size,
                              hipStream_t stream) {
  Params P;
  P.x = (const u16*)d_in[0]; P.are = (const u16*)d_in[1]; P.aim = (const u16*)d_in[2]; P.bre = (const u16*)d_in[3];
  P.bim = (const u16*)d_in[4]; P.cre = (const u16*)d_in[5]; P.cim = (const u16*)d_in[6]; P.ldt = (const u16*)d_in[7];
  P.dsk = (const u16*)d_in[8]; P.wg = (const u16*)d_in[10]; P.wu = (const u16*)d_in[11]; P.wd = (const u16*)d_in[12];
  P.out = (u16*)d_out; P.ws = (char*)d_ws;
  (void)hipFuncSetAttribute((const void*)fused_s4_geglu, hipFuncAttributeMaxDynamicSharedMemorySize, LDS_BYTES);
  fused_s4_geglu<<<256, 512, LDS_BYTES, stream>>>(P);
}
```

```cpp
#include <hip/hip_runtime.h>
#include <hip/hip_bf16.h>
#include <stdint.h>
#pragma clang diagnostic ignored "-Wint-to-pointer-cast"

typedef unsigned long long u64;
typedef unsigned short u16;
typedef __bf16 bf2 __attribute__((ext_vector_type(2)));
typedef __bf16 bf4 __attribute__((ext_vector_type(4)));
typedef __bf16 bf8 __attribute__((ext_vector_type(8)));
typedef float f2 __attribute__((ext_vector_type(2)));
typedef float f4 __attribute__((ext_vector_type(4)));
typedef int i4 __attribute__((ext_vector_type(4)));
typedef unsigned u2 __attribute__((ext_vector_type(2)));
typedef unsigned u4 __attribute__((ext_vector_type(4)));
typedef __amdgpu_buffer_rsrc_t rsrc_t;
#define LDSP(T, a) ((__attribute__((address_space(3))) T*)(a))
#define DEV __device__ __forceinline__
#define SB() __builtin_amdgcn_sched_barrier(0)
#define SC1 16

template <int V> struct IC { static constexpr int value = V; };
template <int B, int E, class Fn> DEV void sfor(Fn&& f) { if constexpr (B < E) { f(IC<B>{}); sfor<B + 1, E>(f); } }
#define LAM(I) [&](auto I) __attribute__((always_inline))
#define CV(I) decltype(I)::value

constexpr int L = 2048, D = 512, NS = 128, F = 1792;

constexpr unsigned WS_EPOCH = 0;
constexpr unsigned WS_FLN = 8192;
constexpr unsigned WS_STATS = 16384;
constexpr unsigned WS_CPART = 65536;
constexpr unsigned WS_FC = 327680;
constexpr unsigned WS_FH = 524288;
constexpr unsigned WS_FY = 786432;
constexpr unsigned WS_XNT = 1u << 20;
constexpr unsigned WS_YT = 3u << 20;
constexpr unsigned WS_H = 5u << 20;
constexpr unsigned WS_BYTES = WS_H + 32u * 56u * 4096u;
constexpr unsigned WS_DBG = 16u << 20;

constexpr unsigned SLOT_C = 98816;
constexpr unsigned slot_of(int s) { return s % 3 == 0 ? 0u : s % 3 == 1 ? 49152u : SLOT_C; }
constexpr unsigned wd_base(int i) { return i < 3 ? SLOT_C + i * 16384 : (i - 3) * 16384; }
constexpr unsigned LKC = 65536;
constexpr unsigned KCS = 4160, KCH = 4 * KCS;
constexpr unsigned LKG = 98304;
constexpr unsigned LU = 147968;
constexpr unsigned UCH = 4608;
constexpr unsigned LCS = LU + 9216;
constexpr unsigned LST = LU + 10240;
constexpr unsigned LKT = 160064;
constexpr unsigned LSY = 163136;
constexpr unsigned LJK = 163328;
constexpr unsigned LDS_BYTES = 163840;

struct Params {
  const u16 *x, *wg, *wu;
  char* ws;
  const u16 *are, *aim, *bre, *bim, *cre, *cim, *ldt, *dsk, *wd;
  u16* out;
};

DEV i4 mksrd(const void* p, unsigned bytes) {
  u64 a = (u64)p; i4 r; r[0] = (int)(unsigned)a; r[1] = (int)(unsigned)(a >> 32); r[2] = (int)bytes; r[3] = 0x00020000; return r;
}
DEV rsrc_t mkrs(const void* p, unsigned bytes) { return __builtin_amdgcn_make_buffer_rsrc((void*)p, 0, (int)bytes, 0x00020000); }
DEV void glds(i4 srd, unsigned voff, unsigned soff, unsigned dst) {
  asm volatile("s_mov_b32 m0, %3\n\ts_nop 0\n\tbuffer_load_dwordx4 %1, %0, %2 offen lds" ::"s"(srd), "v"(voff), "s"(soff), "s"(dst) : "memory");
}
DEV void touch(i4 srd, unsigned voff, unsigned soff) {
  asm volatile("s_mov_b32 m0, %3\n\ts_nop 0\n\tbuffer_load_dword %1, %0, %2 offen lds" ::"s"(srd), "v"(voff), "s"(soff), "s"(LJK) : "memory");
}
template <int N> DEV void waitv() { asm volatile("s_waitcnt vmcnt(%0)" ::"i"(N) : "memory"); }
DEV void waitl() { __builtin_amdgcn_s_waitcnt(0xC07F); asm volatile("" ::: "memory"); }
DEV void bar() { asm volatile("" ::: "memory"); __builtin_amdgcn_s_waitcnt(0xC07F); asm volatile("s_barrier" ::: "memory"); }
DEV void set_m0(unsigned base, unsigned imm) { asm volatile("s_add_u32 m0, %0, %1" ::"s"(base), "i"(imm) : "memory", "scc"); }
template <int OFF> DEV void glds_at(i4 srd, unsigned voff, unsigned soff) {
  asm volatile("buffer_load_dwordx4 %1, %0, %2 offen offset:%3 lds" ::"s"(srd), "v"(voff), "s"(soff), "i"(OFF) : "memory");
}
template <class T> DEV void pin(T& x) { asm volatile("" : "+v"(x)); }
template <class T> DEV void spin_(T& x) { asm volatile("" : "+s"(x)); }
DEV bf4 trd(unsigned a) { return __builtin_amdgcn_ds_read_tr16_b64_v4bf16(LDSP(bf4, a)); }
DEV bf8 cat(bf4 a, bf4 b) { return __builtin_shufflevector(a, b, 0, 1, 2, 3, 4, 5, 6, 7); }
template <class TA, class TB> DEV void mma(f4& c, TA a, TB b) { asm volatile("v_mfma_f32_16x16x32_bf16 %0, %1, %2, %0" : "+v"(c) : "v"(a), "v"(b)); }
template <class TA, class TB> DEV void mma0(f4& c, TA a, TB b) { asm volatile("v_mfma_f32_16x16x32_bf16 %0, %1, %2, 0" : "=&v"(c) : "v"(a), "v"(b)); }
DEV void mma_fence() { asm volatile("s_nop 7\n\ts_nop 1" ::: "memory"); }
DEV float blo(unsigned v) { return __uint_as_float(v << 16); }
DEV float bhi(unsigned v) { return __uint_as_float(v & 0xffff0000u); }
DEV f2 unpk(unsigned v) { return f2{blo(v), bhi(v)}; }
DEV unsigned pk(f2 v) { return __builtin_bit_cast(unsigned, __builtin_convertvector(v, bf2)); }
DEV float rbf(float v) { return blo(pk(f2{v, v})); }
DEV f2 fma2(f2 a, f2 b, f2 c) { return __builtin_elementwise_fma(a, b, c); }
template <int CTRL> DEV float dpp_add(float v) {
  return v + __builtin_bit_cast(float, __builtin_amdgcn_update_dpp(0, __builtin_bit_cast(int, v), CTRL, 0xf, 0xf, true));
}
DEV void swap16(unsigned& a, unsigned& b) { auto r = __builtin_amdgcn_permlane16_swap(a, b, false, false); a = r[0]; b = r[1]; }
DEV void swap32(unsigned& a, unsigned& b) { auto r = __builtin_amdgcn_permlane32_swap(a, b, false, false); a = r[0]; b = r[1]; }
DEV float swap16_add(float a, float b) { unsigned x = __float_as_uint(a), y = __float_as_uint(b); swap16(x, y); return __uint_as_float(x) + __uint_as_float(y); }
DEV float swap32_add(float a, float b) { unsigned x = __float_as_uint(a), y = __float_as_uint(b); swap32(x, y); return __uint_as_float(x) + __uint_as_float(y); }
DEV float red2(float a, float b) {
  float s = swap32_add(a, b);
  s = swap16_add(s, s);
  s = dpp_add<0x128>(s); s = dpp_add<0x124>(s); s = dpp_add<0x122>(s); s = dpp_add<0x121>(s);
  return s;
}
DEV float rdl(float v, int l) { return __builtin_bit_cast(float, __builtin_amdgcn_readlane(__builtin_bit_cast(int, v), l)); }
DEV unsigned uni(unsigned v) { return (unsigned)__builtin_amdgcn_readfirstlane((int)v); }
DEV u4 row_load(i4 srd, unsigned voff, unsigned soff) {
  u4 r; asm volatile("buffer_load_dwordx4 %0, %2, %1, %3 offen" : "=v"(r) : "s"(srd), "v"(voff), "s"(soff) : "memory"); return r;
}
constexpr unsigned SPIN_CAP = 1u << 15;
DEV u2 ld_flag(rsrc_t rs, unsigned voff, unsigned soff) { return __builtin_amdgcn_raw_buffer_load_b64(rs, voff, soff, SC1); }
DEV void st_flag(rsrc_t rs, u2 tag, unsigned soff, int lane) { if (lane == 0) __builtin_amdgcn_raw_buffer_store_b64(tag, rs, 0, soff, SC1); }
DEV bool flags_up(u2 tag, u2 v) { return __builtin_amdgcn_ballot_w64(__builtin_bit_cast(u64, v) != __builtin_bit_cast(u64, tag)) == 0; }
template <int NAP> DEV void wait_flags(rsrc_t rs, u2 tag, u2 v, unsigned voff, unsigned soff) {
#pragma nounroll
  for (unsigned spin = 0; spin < SPIN_CAP; ++spin) {
    if (flags_up(tag, v)) break;
    __builtin_amdgcn_s_sleep(NAP);
    asm volatile("" ::: "memory");
    v = ld_flag(rs, voff, soff);
  }
  asm volatile("" : "+v"(v)::"memory");
}
DEV void lds_post(unsigned a, int lane) { asm volatile("" ::: "memory"); if (lane == 0) *LDSP(volatile unsigned, a) = 1u; }
template <int NAP> DEV void lds_wait(unsigned a, unsigned want) {
#pragma nounroll
  for (unsigned spin = 0; spin < (SPIN_CAP << 6); ++spin) {
    if (uni(*LDSP(volatile unsigned, a)) >= want) break;
    if (NAP) __builtin_amdgcn_s_sleep(NAP);
  }
  asm volatile("" ::: "memory");
}
DEV unsigned lds_arrive(unsigned a, int lane) {
  asm volatile("s_waitcnt lgkmcnt(0)" ::: "memory");
  unsigned o = 0;
  if (lane == 0) o = __hip_atomic_fetch_add(LDSP(unsigned, a), 1u, __ATOMIC_RELAXED, __HIP_MEMORY_SCOPE_WORKGROUP);
  return uni(o);
}

#define STAMP(i)
#define FINISH()

#define G2MAP 0
#define TILE_T(c) (G2MAP ? (c.xc >> 1) * 8 + (c.jj & 7) : c.xc * 4 + (c.jj & 3))
#define TILE_C(c) (G2MAP ? (c.xc & 1) * 4 + (c.jj >> 3) : c.jj >> 2)
struct Ctx {
  int xc, jj, w, v, lane, n, q;
  u2 tag;
  rsrc_t ws;
};

struct alignas(16) F1 { float v; };
DEV float red4(float a, float b, float c, float d) {
  float s = swap16_add(swap32_add(a, b), swap32_add(c, d));
  s = dpp_add<0x128>(s); s = dpp_add<0x124>(s); s = dpp_add<0x122>(s); s = dpp_add<0x121>(s);
  return s;
}
DEV int ln_row(int r) { return r * 1088 + (r & 8) * 4; }
DEV void ln_compute(const Ctx& c, const u4* rows, unsigned img, int tok0) {
  f2 x[4][4]; float t[4];
#pragma unroll
  for (int k = 0; k < 4; k++) {
#pragma unroll
    for (int i = 0; i < 4; i++) x[k][i] = unpk(rows[k][i]);
    const f2 s = (x[k][0] + x[k][1]) + (x[k][2] + x[k][3]); t[k] = s.x + s.y;
  }
  float mean = red4(t[0], t[2], t[1], t[3]) * (1.f / D);
#pragma unroll
  for (int k = 0; k < 4; k++) {
    const float m = rdl(mean, 16 * k);
    f2 qq = {0.f, 0.f};
#pragma unroll
    for (int i = 0; i < 4; i++) { x[k][i] -= m; qq = fma2(x[k][i], x[k][i], qq); }
    t[k] = qq.x + qq.y;
  }
  float var = red4(t[0], t[2], t[1], t[3]) * (1.f / D);
  const unsigned st = pk(f2{mean, __builtin_amdgcn_sqrtf(rbf(rbf(var) + 1e-5f))});
  const float r = __builtin_amdgcn_rcpf(bhi(st)), o = (mean - blo(st)) * r;
#pragma unroll
  for (int k = 0; k < 4; k++) {
    const float rk = rdl(r, 16 * k), ok = rdl(o, 16 * k);
    u4 nv;
#pragma unroll
    for (int i = 0; i < 4; i++) nv[i] = pk(fma2(x[k][i], f2{rk, rk}, f2{ok, ok}));
    *LDSP(u4, img + ln_row(4 * c.w + k) + c.lane * 16) = nv;
  }
  __builtin_amdgcn_raw_buffer_store_b32(st, c.ws, c.n ? 0x7ffffff0u : (unsigned)(c.q * 4), WS_STATS + (tok0 + 4 * c.w) * 4, SC1);
}
DEV void ln_store(const Ctx& c, unsigned img, int tok0) {
  const unsigned a = img + ln_row(8 * c.q + (c.n >> 2)) + (64 * c.w + 4 * (c.n & 3)) * 2;
#pragma unroll
  for (int i = 0; i < 4; i++)
    __builtin_amdgcn_raw_buffer_store_b128(__builtin_bit_cast(u4, cat(trd(a + 32 * i), trd(a + 32 * i + 4 * 1088))), c.ws, c.n * 64 + c.q * 16 + i * 1024,
                                           WS_XNT + tok0 * 1024 + c.w * 4096, SC1);
}
DEV unsigned u_off(const Ctx& c) { return (c.lane >> 3) * 32768 + (c.lane & 7) * 16; }
DEV u4 u_load(const Ctx& c, unsigned uv, int i) {
  return __builtin_amdgcn_raw_buffer_load_b128(c.ws, uv, WS_XNT + (16 * c.v + 8 * i) * 32768 + (c.xc * 32 + c.jj) * 128, SC1);
}
DEV void u_write(const Ctx& c, u4 r0, u4 r1) {
  const unsigned a = LU + ((c.lane >> 2) & 1) * UCH + 512 + (16 * c.v + (c.lane >> 3)) * 64 + (c.lane & 3) * 16;
  *LDSP(u4, a) = r0;
  *LDSP(u4, a + 512) = r1;
  if (c.v == 0) *LDSP(u4, LU + (c.lane & 1) * UCH + (c.lane >> 1) * 16) = u4{0, 0, 0, 0};
}
DEV u2 flag_sample(const Params& P, unsigned voff, unsigned soff) {
  u2 r; asm volatile("buffer_load_dwordx2 %0, %2, %1, %3 offen sc1" : "=v"(r) : "s"(mksrd(P.ws, WS_BYTES)), "v"(voff), "s"(soff) : "memory"); return r;
}

struct C2 { f2 re, im; };
DEV C2 cmul(C2 a, C2 b) { C2 r; r.re = fma2(a.re, b.re, -(a.im * b.im)); r.im = fma2(a.re, b.im, a.im * b.re); return r; }
DEV C2 zpow(f2 al, f2 th, float e) {
  const f2 a = al * e, t0 = th * e;
  const f2 t = {__builtin_amdgcn_fractf(t0.x), __builtin_amdgcn_fractf(t0.y)};
  const f2 m = {__builtin_amdgcn_exp2f(a.x), __builtin_amdgcn_exp2f(a.y)};
  C2 r; r.re = m * f2{__builtin_amdgcn_cosf(t.x), __builtin_amdgcn_cosf(t.y)}; r.im = m * f2{__builtin_amdgcn_sinf(t.x), __builtin_amdgcn_sinf(t.y)};
  return r;
}
struct KIn { unsigned are, aim, bre, bim, cre, cim, ldt, dsk; u64 epoch; };
DEV unsigned par_load(const u16* p, unsigned voff) {
  unsigned r; asm volatile("global_load_dword %0, %1, %2" : "=v"(r) : "v"(voff), "s"(p) : "memory"); return r;
}
DEV unsigned spar_load(const u16* p, unsigned off) { unsigned r; asm volatile("s_load_dword %0, %1, %2" : "=s"(r) : "s"(p), "s"(off) : "memory"); return r; }
DEV KIn kgen_load(const Ctx& c, const Params& P) {
  const unsigned pair = c.xc * 32 + c.jj, o = (2 * pair + (c.w & 1)) * 256 + c.lane * 4;
  KIn r;
  r.are = par_load(P.are, o); r.aim = par_load(P.aim, o);
  r.ldt = spar_load(P.ldt, pair * 4); r.dsk = spar_load(P.dsk, pair * 4);
  asm volatile("s_load_dwordx2 %0, %1, 0" : "=s"(r.epoch) : "s"(P.ws + WS_EPOCH) : "memory");
  if (c.w < 2) { r.bre = par_load(P.bre, o); r.bim = par_load(P.bim, o); r.cre = par_load(P.cre, o); r.cim = par_load(P.cim, o); }
  return r;
}
template <int N> DEV void kgen_take(Ctx& c, const Params& P, KIn& k) {
  KIn o; char* ws = P.ws;
  asm volatile("s_waitcnt lgkmcnt(0)\n\ts_mov_b32 %0, %4\n\ts_mov_b32 %1, %5\n\ts_mov_b64 %2, %6"
               : "=&s"(o.ldt), "=&s"(o.dsk), "=&s"(o.epoch), "+s"(ws) : "s"(k.ldt), "s"(k.dsk), "s"(k.epoch) : "memory");
  asm volatile("s_waitcnt vmcnt(%12)\n\tv_mov_b32 %0, %6\n\tv_mov_b32 %1, %7\n\tv_mov_b32 %2, %8\n\tv_mov_b32 %3, %9\n\tv_mov_b32 %4, %10\n\tv_mov_b32 %5, %11"
               : "=&v"(o.are), "=&v"(o.aim), "=&v"(o.bre), "=&v"(o.bim), "=&v"(o.cre), "=&v"(o.cim)
               : "v"(k.are), "v"(k.aim), "v"(k.bre), "v"(k.bim), "v"(k.cre), "v"(k.cim), "i"(N) : "memory");
  k = o;
  c.tag = u2{(unsigned)(o.epoch + 1), (unsigned)((o.epoch + 1) >> 32)};
  c.ws = mkrs(ws, WS_BYTES);
}
struct KPre { float del, g0, g1, rev; unsigned wa, wb, rd, sh, ta, tb, rs, s0, s1; bool sw; };
DEV KPre kgen_pre(const Ctx& c) {
  const int task = c.w >> 1, n = c.n, q = c.q;
  const unsigned T = LKG + c.w * 8672 - (c.w > 4 ? (c.w > 5 ? 2 : 1) * 3808 : 0);
  KPre k;
  k.del = task == 0 ? 8.f : task == 1 ? 1.f : task == 2 ? 2.f : 128.f;
  k.g0 = task == 0 ? 64.f : task == 1 ? 128.f : task == 2 ? 256.f : 512.f;
  k.g1 = task == 2 ? 2048.f : 516.f;
  k.rev = task == 0 ? -0.15915494309189535f : 0.15915494309189535f;
  k.wa = T + c.lane * 8; k.wb = k.wa + (task == 2 ? 4 : 8) * 544; k.rd = T + n * 544 + q * 16;
  k.sh = (c.w & 1) ? 0 : 16;
  k.ta = LKT + (c.w & 1) * 1536 + 2 * (task == 1 ? 128 * (q >> 1) + 4 * (q & 1) + 8 * n : task == 2 ? (q < 2 ? 256 : 512) + 64 * q + 4 * n
                                                                                                    : 384 + 64 * q + (q < 2 ? 2 * n : 31 * (n & 1) + n));
  if (task == 0) k.ta = LJK + c.lane * 8;
  const bool far = task == 3 && q >= 2;
  k.tb = k.ta + (task < 3 ? 4 : q < 2 ? 64 : 32);
  k.s0 = !far ? 0x03020100u : (n & 1) ? 0x01000504u : 0x05040100u;
  k.s1 = k.s0 + (far ? 0x02020202u : 0x04040404u);
  k.sw = far && (n & 1);
  k.rs = task == 3 ? 1088 : 544;
  asm volatile("" : "+v"(k.del), "+v"(k.g0), "+v"(k.g1), "+v"(k.rev), "+v"(k.wa), "+v"(k.wb), "+v"(k.rd), "+v"(k.ta), "+v"(k.tb), "+v"(k.s0), "+v"(k.s1));
  return k;
}
template <int ROLE, class Hook>
DEV void kgen_rows(const Ctx& c, const KPre& p, const KIn& k, bf8 (&A)[8], Hook&& hook) {
  const float dt = rbf(__builtin_amdgcn_exp2f(bhi(k.ldt << p.sh) * 1.4426950408889634f));
  const f2 al = (unpk(k.are) * dt) * 1.4426950408889634f;
  const f2 th = (unpk(k.aim) * dt) * p.rev;
  auto put = [&](unsigned ad, C2 z) __attribute__((always_inline)) { *LDSP(u2, ad) = u2{pk(z.re), pk(z.im)}; };
  C2 st = zpow(al, th, p.del);
  pin(st.re); pin(st.im);
  hook(IC<0>{});
  C2 a, b, e;
  if (ROLE == 0) {
    a = C2{f2{1.f, 1.f}, f2{0.f, 0.f}}; b = zpow(al, th, p.g0);
    if (c.w < 2) { a = cmul(C2{unpk(k.cre), -unpk(k.cim)}, C2{unpk(k.bre) * dt, unpk(k.bim) * -dt}); b = cmul(b, a); }
  } else {
    a = zpow(al, th, p.g0);
    if (c.w >= 6) b = cmul(a, a); else b = zpow(al, th, 384.f);
  }
  pin(b.re); pin(b.im);
  hook(IC<1>{});
  sfor<0, 4>(LAM(I) {
    constexpr int i = CV(I);
    put(p.wa + i * (ROLE ? p.rs : 544), a); put(p.wb + i * 544, b);
    hook(IC<2 + i>{});
    if constexpr (ROLE == 0 || i < 3) a = cmul(a, st); else a = zpow(al, th, p.g1);
    b = cmul(b, st);
  });
  if (ROLE == 1 && c.w < 6) { put(p.wa + 8 * 544, a); hook(IC<6>{}); hook(IC<7>{}); hook(IC<8>{}); }
  else sfor<4, 8>(LAM(I) {
    constexpr int i = CV(I);
    put(p.wa + (ROLE ? 2 * i - 7 : i) * 544, a); put(p.wb + i * 544, b);
    if constexpr (i < 7) { hook(IC<2 + i>{}); a = cmul(a, st); b = cmul(b, st); }
  });
#pragma unroll
  for (int i = 0; i < 8; i++) A[i] = *LDSP(bf8, p.rd + i * 64);
}
DEV void kgen_cols(const Ctx& c, bf8 (&B)[8]) {
#pragma unroll
  for (int i = 0; i < 8; i++) B[i] = *LDSP(bf8, LKG + (c.w & 1) * 8672 + c.n * 544 + c.q * 16 + i * 64);
}
DEV void kgen_mma(f4& t, const bf8 (&A)[8], const bf8 (&B)[8]) {
  mma0(t, A[0], B[0]);
#pragma unroll
  for (int i = 1; i < 8; i++) mma(t, A[i], B[i]);
}
struct alignas(16) U1 { unsigned v; };
struct KTab { f4 t; U1 a, b, p[2]; };
constexpr int KT_OPS = 9;
template <int ROLE, int k> DEV void ktab_op(const Ctx& c, const KPre& p, KTab& z) {
  if constexpr (ROLE == 0) {
    if constexpr (k == 0) { pin(z.t); z.t[0] += __uint_as_float(z.a.v); pin(z.t); }
    if constexpr (k == 1) { z.p[1].v = pk(f2{z.t[2], z.t[3]}); pin(z.p[1].v); }
    if constexpr (k == 2) { z.p[0].v = pk(f2{z.t[0], z.t[1]}); pin(z.p[0].v); }
    if constexpr (k == 3) *LDSP(u2, p.ta) = u2{z.p[0].v, z.p[1].v};
  } else {
    if constexpr (k == 0) { pin(z.t); z.p[0].v = pk(f2{z.t[0], z.t[1]}); pin(z.p[0].v); }
    if constexpr (k == 1) { z.p[1].v = pk(f2{z.t[2], z.t[3]}); pin(z.p[1].v); }
    if constexpr (k == 2) { z.b.v = p.sw ? z.p[0].v : z.p[1].v; pin(z.b.v); }
    if constexpr (k == 3) { z.a.v = p.sw ? z.p[1].v : z.p[0].v; pin(z.a.v); }
    if constexpr (k == 4) { z.b.v = (unsigned)__builtin_amdgcn_update_dpp((int)z.b.v, (int)z.b.v, 0xb1, 0xc, 0xf, false); pin(z.b.v); }
    if constexpr (k == 5) { z.p[0].v = __builtin_amdgcn_perm(z.b.v, z.a.v, p.s0); pin(z.p[0].v); }
    if constexpr (k == 6) { z.p[1].v = __builtin_amdgcn_perm(z.b.v, z.a.v, p.s1); pin(z.p[1].v); }
    if constexpr (k == 7) *LDSP(unsigned, p.ta) = z.p[0].v;
    if constexpr (k == 8) *LDSP(unsigned, p.tb) = z.p[1].v;
  }
}
DEV void ktab_skip(const Ctx& c, const KPre& p, KTab& z, unsigned dsk) { z.a.v = (c.w >= 2 && c.lane == 0) ? (dsk << p.sh) & 0xffff0000u : 0u; pin(z.a.v); }
typedef unsigned u2a __attribute__((ext_vector_type(2), aligned(4)));
struct KExp { U1 a0, a1, a2, a3, s[7], wr, h0, pv[2][2], o[2][4][4], es[3]; u2 e[4]; };
DEV void kexp_pre(const Ctx& c, KExp& z) {
  const int kh = c.v >> 1, l = c.lane;
  const unsigned CC = LKT + (c.v & 1) * 1536;
  const bool up = l >= 32, odd = l & 1;
  z.a0.v = CC + (kh ? 768 + l * 4 : up ? 256 + l * 8 : l * 16);
  z.a1.v = z.a0.v + (kh ? 0 : up ? 4 : 8);
  z.a2.v = CC + 1024 + 128 * kh + (l >> 1) * 4;
  z.a3.v = CC + 764;
  z.s[0].v = (kh || up) ? 0x01000100u : 0x03020100u;
  z.s[1].v = kh ? 0x03020100u : up ? 0x05040302u : 0x07060504u;
  z.s[2].v = kh ? 0x03020302u : z.s[0].v;
  z.s[3].v = kh ? 0x05040504u : z.s[1].v;
  z.s[4].v = odd ? 0x03020302u : 0x01000100u;
  z.s[5].v = odd ? 0x05040504u : 0x03020100u;
  z.s[6].v = odd ? 0x05040504u : 0x03020302u;
  z.wr.v = LKC + (c.v & 1) * KCH + 32 + kh * 1024 + l * 16;
  z.es[0].v = kh ? 0x01000100u : 0x0c0c0c0cu; z.es[1].v = kh ? 0x05040302u : 0x0c0c0c0cu; z.es[2].v = kh ? 0x05040504u : 0x03020302u;
  asm volatile("" : "+s"(z.es[0].v), "+s"(z.es[1].v), "+s"(z.es[2].v));
  asm volatile("" : "+v"(z.a0.v), "+v"(z.a1.v), "+v"(z.a2.v), "+v"(z.a3.v), "+v"(z.s[0].v), "+v"(z.s[1].v), "+v"(z.s[2].v), "+v"(z.s[3].v),
                    "+v"(z.s[4].v), "+v"(z.s[5].v), "+v"(z.s[6].v), "+v"(z.wr.v));
}
constexpr int KX_OPS = 45;
template <int k> DEV void kexp_op(const Ctx& c, KExp& z) {
  auto set = [&](U1& d, unsigned x) __attribute__((always_inline)) { d.v = x; pin(d.v); };
  if constexpr (k == 0) z.e[0] = *LDSP(u2a, z.a0.v);
  else if constexpr (k == 1) z.e[1] = *LDSP(u2a, z.a1.v);
  else if constexpr (k == 2) z.e[2] = *LDSP(u2a, z.a2.v);
  else if constexpr (k == 3) z.e[3] = *LDSP(u2a, z.a3.v);
  else if constexpr (k == 4) z.h0.v = *LDSP(unsigned, z.a3.v + 256);
  else if constexpr (k < 9) { constexpr int i = k - 5; set(z.o[0][0][i], __builtin_amdgcn_perm(z.e[i >> 1][1], z.e[i >> 1][0], z.s[i].v)); }
  else if constexpr (k < 13) { constexpr int i = k - 9;
    if constexpr (i == 1) asm volatile("v_mov_b32 %0, %1" : "=v"(z.o[1][0][1].v) : "v"(z.o[1][0][0].v));
    else set(z.o[1][0][i], __builtin_amdgcn_perm(z.e[2][1], z.e[2][0], z.s[i ? 3 + i : 4].v));
  }
  else if constexpr (k == 13) { z.pv[0][0].v = __builtin_amdgcn_perm(z.e[3][1], z.e[3][0], z.es[0].v); pin(z.pv[0][0].v); }
  else if constexpr (k == 14) { z.pv[0][1].v = __builtin_amdgcn_perm(z.e[3][1], z.e[3][0], z.es[1].v); pin(z.pv[0][1].v); }
  else if constexpr (k == 15) { z.pv[1][0].v = __builtin_amdgcn_perm(z.e[2][0], z.h0.v, z.es[2].v); pin(z.pv[1][0].v); }
  else if constexpr (k == 16) { z.pv[1][1].v = z.o[1][0][0].v; pin(z.pv[1][1].v); }
  else if constexpr (k < 21) {
    constexpr int i = (k - 17) >> 1, j = (k - 17) & 1;
    z.pv[i][j].v = (unsigned)__builtin_amdgcn_update_dpp((int)z.pv[i][j].v, (int)z.o[i][0][2 + j].v, 0x138, 0xf, 0xf, false); pin(z.pv[i][j].v);
  } else {
    constexpr int i = (k - 21) / 12, cp = 1 + (k - 21) % 12 / 4, j = (k - 21) % 4;
    auto dw = [&](int m) __attribute__((always_inline)) { return m < 0 ? z.pv[i][2 + m].v : z.o[i][0][m].v; };
    if constexpr (cp == 2) asm volatile("v_mov_b32 %0, %1" : "=v"(z.o[i][2][j].v) : "v"(dw(j - 1)));
    else asm volatile("v_alignbit_b32 %0, %1, %2, 16" : "=v"(z.o[i][cp][j].v) : "v"(dw(j - (cp >> 1))), "v"(dw(j - 1 - (cp >> 1))));
  }
}
template <int k> DEV void kexp_put(const Ctx& c, const KExp& z) {
  if constexpr (k < 8) *LDSP(u4, z.wr.v + (k >> 2) * 2048 + (k & 3) * KCS) = u4{z.o[k >> 2][k & 3][0].v, z.o[k >> 2][k & 3][1].v, z.o[k >> 2][k & 3][2].v, z.o[k >> 2][k & 3][3].v};
  else *LDSP(unsigned, LKC + (c.lane >> 3) * KCS + (c.lane & 7) * 4) = 0u;
}

struct WdSrc { i4 srd; unsigned voff[4]; };
DEV WdSrc wd_src(const Ctx& c, const Params& P) {
  WdSrc s; s.srd = mksrd(P.wd, F * D * 2);
  const int r = c.lane >> 3;
#pragma unroll
  for (int k = 0; k < 4; k++) { s.voff[k] = (448 * c.v + r) * 1024 + TILE_C(c) * 128 + (((c.lane & 7) ^ (r & 6)) * 16) + k * 7168; pin(s.voff[k]); }
  return s;
}
DEV unsigned wd_raddr(int v, int n, int q) { return v * 4096 + (4 * q + (n >> 2)) * 128 + (((2 * q + (n >> 3)) & 3) * 32) + (n & 3) * 8; }
DEV void wd_ld(const WdSrc& s, int i, int r) {
  if (r == 0) glds_at<0>(s.srd, s.voff[0], i * 32768);
  else if (r == 1) glds_at<1024>(s.srd, s.voff[1], i * 32768);
  else if (r == 2) glds_at<2048>(s.srd, s.voff[2], i * 32768);
  else glds_at<3072>(s.srd, s.voff[3], i * 32768);
}
DEV void wd_chunk(const Ctx& c, const WdSrc& s, int i) {
  set_m0(c.v * 4096, wd_base(i % 7)); asm volatile("s_nop 0");
#pragma unroll
  for (int r = 0; r < 4; r++) wd_ld(s, i, r);
}

struct Frags { bf4 a[4][2]; bf8 b[4]; };
DEV void rd_piece(Frags& Fr, int i, unsigned wa0, unsigned wa1, unsigned xa, int ks) {
  if (i == 0) Fr.b[0] = *LDSP(bf8, xa);
  else if (i < 9) { const int t = (i - 1) >> 1, h = (i - 1) & 1; Fr.a[t][h] = trd(((t & 1) ? wa1 : wa0) + (t >> 1) * 16384 + ks * 8192 + h * 1024); }
  else Fr.b[i - 8] = *LDSP(bf8, xa + (i - 8) * 2048);
}

struct G1Src { i4 sx, sg, su; unsigned vx0, vx1, vw0, vw1; int k0; };
DEV G1Src g1_src(const Ctx& c) {
  const int p = (c.xc & 3) * 4 + (c.jj & 3), ft = (c.xc >> 2) * 7 + (c.jj >> 2);
  G1Src g;
  g.k0 = 2 * (c.xc & 3);
  const int xr = c.lane >> 3;
  g.vx0 = (p * 128 + 16 * c.w + xr) * 1024 + (((c.lane & 7) ^ (xr >> 1)) * 16);
  g.vx1 = (g.vx0 ^ 64) + 8192 - 1024;
  g.vw0 = (8 * c.w + c.q) * 3584 + ft * 256 + ((c.n ^ ((c.q | ((c.w & 1) << 2)) << 1)) * 16);
  g.vw1 = g.vw0 + 4 * 3584 - 1024;
  asm volatile("" : "+v"(g.vx0), "+v"(g.vx1), "+v"(g.vw0), "+v"(g.vw1));
  return g;
}
DEV void g1_m0(const Ctx& c, int s, int i) { set_m0(c.w * 2048, slot_of(s) + (i >> 1) * 16384); }
DEV void g1_ld(const G1Src& g, int s, int i) {
  const int sk = (s + g.k0) & 7;
  if (i == 0) glds_at<0>(g.sx, g.vx0, sk * 128);
  else if (i == 1) glds_at<1024>(g.sx, g.vx1, sk * 128);
  else if (i & 1) glds_at<1024>(i > 3 ? g.su : g.sg, g.vw1, sk * 64 * 3584);
  else glds_at<0>(i > 3 ? g.su : g.sg, g.vw0, sk * 64 * 3584);
}
struct G1Rot { i4 srd[2]; unsigned vo[2], dst[4], so[2]; };
DEV G1Rot g1_rot(const Ctx& c, const G1Src& g) {
  const int mine = c.jj & 3;
  G1Rot r;
#pragma unroll
  for (int k = 0; k < 2; k++) {
    const bool up = ((mine >> 1) ^ k) & 1;
    r.srd[k] = g.sg; r.srd[k][0] = up ? g.su[0] : g.sg[0]; r.srd[k][1] = up ? g.su[1] : g.sg[1];
    r.vo[k] = g.vw0 + ((mine ^ k) & 1) * 4 * 3584;
    r.so[k] = (g.k0 + k) * 64 * 3584;
  }
#pragma unroll
  for (int k = 0; k < 4; k++) r.dst[k] = 16384 + ((mine ^ k) >> 1) * 16384 + c.w * 2048 + ((mine ^ k) & 1) * 1024;
  return r;
}
template <int S, int K> DEV void g1_ld_w(const G1Rot& r) {
  asm volatile("s_add_u32 m0, %3, %4\n\ts_nop 0\n\tbuffer_load_dwordx4 %1, %0, %2 offen lds"
               ::"s"(r.srd[K >> 1]), "v"(r.vo[K & 1]), "s"(r.so[S]), "s"(r.dst[K]), "i"(slot_of(S)) : "memory", "scc");
}

constexpr int feed_half(int e) { return e < 24 ? (e - 12) / 4 : e / 3 - 5; }
constexpr int feed_slot(int e) { return e < 16 ? 5 + 3 * (e - 12) : e < 24 ? 1 + 4 * (e % 4) : 4 + 5 * (e % 3); }
constexpr int feed_at(int h, int i) { for (int e = 12; e < 48; e++) if (feed_half(e) == h && feed_slot(e) == i) return e; return -1; }
constexpr int wd_at(int h, int i) { for (int e = 0; e < 28; e++) if (11 + e / 6 == h && 1 + (e % 6) * 5 / 2 == i) return e; return -1; }

template <int ROLE>
DEV void gemm1(Ctx& c, const Params& P, G1Src gs, WdSrc& wds, KIn kin, f4 (&acc)[4][4], bf4 (&W1)[7][4][2], unsigned (&wa)[4], KExp& kx, u4 (&ur)[2]) {
  gs.sx = mksrd(P.x, L * D * 2); gs.sg = mksrd(P.wg, D * F * 2); gs.su = mksrd(P.wu, D * F * 2);
  const int p = (c.xc & 3) * 4 + (c.jj & 3), ft = (c.xc >> 2) * 7 + (c.jj >> 2);
  const int lane = c.lane, n = c.n, q = c.q, v = c.v;
  G1Rot rot = g1_rot(c, gs);
  g1_ld_w<0, 0>(rot);
  g1_m0(c, 0, 0); asm volatile("s_nop 0"); g1_ld(gs, 0, 0); g1_ld(gs, 0, 1);
  if (ROLE == 0 && threadIdx.x < 12) *LDSP(unsigned, LSY + threadIdx.x * 4) = 0u;
  const KPre kp = kgen_pre(c);
  if (ROLE == 0) kexp_pre(c, kx); else wds = wd_src(c, P);
  unsigned xa[2][2], wr[3][2];
  xa[0][0] = (ROLE * 64 + n) * 128 + ((q ^ (n >> 1)) * 16); xa[0][1] = xa[0][0] ^ 64;
  wr[0][0] = 16384 + (8 * q + (n >> 2)) * 256 + (((2 * v) ^ ((n >> 2) | ((q & 1) << 2))) * 32) + (n & 3) * 8; wr[0][1] = wr[0][0] ^ 32;
#pragma unroll
  for (int k = 0; k < 2; k++) { xa[1][k] = xa[0][k] + SLOT_C; wr[1][k] = wr[0][k] + slot_of(1); wr[2][k] = wr[0][k] + SLOT_C; }
  unsigned kbr = LKG + (c.w & 1) * 8672 + n * 544 + q * 16;
  asm volatile("" : "+v"(xa[0][0]), "+v"(xa[0][1]), "+v"(xa[1][0]), "+v"(xa[1][1]), "+v"(wr[0][0]), "+v"(wr[0][1]), "+v"(wr[1][0]), "+v"(wr[1][1]),
                    "+v"(wr[2][0]), "+v"(wr[2][1]), "+v"(kbr), "+v"(rot.vo[1]));
  asm volatile("" : "+s"(rot.srd[1]), "+s"(rot.so[1]), "+s"(rot.dst[1]), "+s"(rot.dst[2]), "+s"(rot.dst[3]));
  unsigned lfv, cpv, l4, stv, csa, sta, one0, uv; u4 ones;
  if (ROLE == 0) {
    lfv = q == 0 ? WS_FC + ((v * 14 + ft) * 16 + n) * 128 : WS_FLN + (q == 1 ? 16 * v + n : 4 * p + v) * 128;
    cpv = q * 1024 + n * 16; l4 = lane * 4; stv = l4 & 124; sta = LST + 128 * v + stv;
    csa = LCS + v * 256 + n * 16 + (((q & 1) << 1) | (q >> 1)) * 4;
    one0 = lane ? 0x7ffffff0u : 0u; uv = u_off(c);
    ones = u4{0x3f803f80u, 0x3f803f80u, 0x3f803f80u, 0x3f803f80u};
    asm volatile("" : "+v"(lfv), "+v"(cpv), "+v"(l4), "+v"(stv), "+v"(sta), "+v"(csa), "+v"(one0), "+v"(ones), "+v"(uv));
  } else {
#pragma unroll
    for (int d = 0; d < 4; d++) { wa[d] = (wd_raddr(v, n, q) ^ (d * 32)) + SLOT_C; pin(wa[d]); }
  }
  kgen_take<3>(c, P, kin);
  STAMP(16);
  bf8 KA[8], KB[8]; KTab kt;
  kgen_rows<ROLE>(c, kp, kin, KA, LAM(H) {
    constexpr int h = CV(H);
    if constexpr (h < 3) g1_ld_w<0, h + 1>(rot);
    else if constexpr (h == 3) g1_ld_w<1, 0>(rot);
    else if constexpr (h == 4) { g1_m0(c, 1, 0); asm volatile("s_nop 0"); g1_ld(gs, 1, 0); }
    else if constexpr (h == 5) g1_ld(gs, 1, 1);
    else g1_ld_w<1, h - 5>(rot);
    SB();
  });
  u2 tagv = c.tag;
  if (ROLE == 0) { ktab_skip(c, kp, kt, kin.dsk); pin(tagv); }
  waitv<6>();
  bar();
  STAMP(2);
  Frags F0, Fb;
  auto rd = [&](Frags& Fr, int i, int s, int ks) __attribute__((always_inline)) {
    rd_piece(Fr, i, wr[s % 3][0], wr[s % 3][1], xa[s % 3 == 2][ks] + (s % 3 == 1 ? slot_of(1) : 0), ks);
  };
#pragma unroll
  for (int i = 0; i < 12; i++) rd(F0, i, 0, 0);
  SB();
#pragma unroll
  for (int i = 0; i < 8; i++) KB[i] = *LDSP(bf8, kbr + i * 64);
  SB();
  f4 cs[4];
  auto colsum = [&](const Frags& Fr, int rel) __attribute__((always_inline)) {
    if (ROLE == 0 && (c.jj & 3) == rel) {
#pragma unroll
      for (int t = 0; t < 4; t++) asm volatile("v_mfma_f32_16x16x32_bf16 %0, %1, %2, 0" : "+v"(cs[t]) : "v"(ones), "v"(cat(Fr.a[t][0], Fr.a[t][1])));
    }
    SB();
  };
  u2 lf; u4 cpr[4]; unsigned str; F1 cs4[4]; U1 csm;
  auto feed = [&](auto H, auto I) __attribute__((always_inline)) {
    constexpr int h = CV(H), i = CV(I);
    constexpr int e = feed_at(h, i), en = feed_at(h, i + 1);
    if constexpr (e >= 0) g1_ld(gs, e / 6, e % 6);
    if constexpr (en >= 0 && !(en & 1)) g1_m0(c, en / 6, en % 6);
    constexpr int d = ROLE ? wd_at(h, i) : -1, dn = ROLE ? wd_at(h, i + 1) : -1;
    if constexpr (d >= 0) wd_ld(wds, d >> 2, d & 3);
    if constexpr (dn >= 0 && !(dn & 3)) set_m0(v * 4096, wd_base(dn >> 2));
  };
  sfor<0, 8>(LAM(S) {
    constexpr int s = CV(S);
    sfor<0, 16>(LAM(I) {
      constexpr int i = CV(I);
      if constexpr (s == 0) mma0(acc[i >> 2][i & 3], cat(F0.a[i & 3][0], F0.a[i & 3][1]), F0.b[i >> 2]);
      else mma(acc[i >> 2][i & 3], cat(F0.a[i & 3][0], F0.a[i & 3][1]), F0.b[i >> 2]);
      if constexpr (s == 0) {
        if constexpr (i == 3) bar();
        if constexpr (i >= 3 && i < 15) rd(Fb, i - 3, s, 1);
        if constexpr (i == 4) mma0(kt.t, KA[0], KB[0]);
        if constexpr (i > 4 && i < 12) mma(kt.t, KA[i - 4], KB[i - 4]);
      } else if constexpr (i != 4 && i != 9 && i < 14) rd(Fb, i - (i > 4) - (i > 9), s, 1);
      feed(IC<2 * s>{}, I);
      if constexpr (ROLE == 0 && s == 3) kexp_op<16 + i>(c, kx);
      if constexpr (ROLE == 0 && s == 2) {
        if constexpr (i == 5) { pin(cs[0]); pin(cs[1]); pin(cs[2]); pin(cs[3]); csm.v = __float_as_uint(cs[0][0]); }
        if constexpr (i >= 5 && i < 8) { csm.v = (unsigned)__builtin_amdgcn_update_dpp((int)csm.v, (int)__float_as_uint(cs[i - 4][0]), 0xe4, 1 << (i - 4), 0xf, false); pin(csm.v); }
        if constexpr (i == 15) __builtin_amdgcn_raw_buffer_store_b32(csm.v, c.ws, l4, WS_CPART + (ft * 16 + p) * 1024 + v * 256, SC1);
      }
      if constexpr (ROLE == 0) {
        if constexpr (s == 5 && i == 15) lf = flag_sample(P, lfv, 0);
        if constexpr (s == 6 && (i & 3) == 0) cpr[i >> 2] = __builtin_amdgcn_raw_buffer_load_b128(c.ws, cpv, WS_CPART + ft * 16384 + (i >> 2) * 4096 + v * 256, SC1);
        if constexpr (s == 7) {
          if constexpr (i < 4) { cs4[i].v += __uint_as_float(cpr[3][i]); pin(cs4[i].v); }
          if constexpr (i == 4) { cs4[0].v = swap32_add(cs4[0].v, cs4[1].v); pin(cs4[0].v); }
          if constexpr (i == 5) { cs4[2].v = swap32_add(cs4[2].v, cs4[3].v); pin(cs4[2].v); }
          if constexpr (i == 7) *LDSP(float, csa) = swap16_add(cs4[0].v, cs4[2].v);
          if constexpr (i == 9) *LDSP(unsigned, sta) = str;
        }
        if constexpr (s == 6 && i == 14) str = __builtin_amdgcn_raw_buffer_load_b32(c.ws, stv, WS_STATS + (p * 128 + 32 * v) * 4, SC1);
      }
      SB();
    });
    if constexpr (s == 0 && ROLE == 0) { asm volatile("" : "=v"(cs[0]), "=v"(cs[1]), "=v"(cs[2]), "=v"(cs[3])); }
    if constexpr (s < 2) colsum(F0, 2 * s);
    if constexpr (s == 0) waitv<4>();
    else if constexpr (s == 2) waitv<ROLE ? 6 : 7>();
    else if constexpr (s < 5) waitv<6>();
    else if constexpr (s == 5) waitv<ROLE ? 6 : 7>();
    else if constexpr (ROLE == 1) waitv<12>();
    bar();
    if constexpr (s == 0) { STAMP(3); }
    if constexpr (s == 2) { STAMP(4); }
    if constexpr (s == 5) { STAMP(5); }
    if constexpr (s == 6) { STAMP(6); }
    if constexpr (s == 7) { STAMP(1); }
    SB();
    sfor<0, 16>(LAM(I) {
      constexpr int i = CV(I);
      mma(acc[i >> 2][i & 3], cat(Fb.a[i & 3][0], Fb.a[i & 3][1]), Fb.b[i >> 2]);
      if constexpr (s < 7 && i != 4 && i != 9 && i < 14) rd(F0, i - (i > 4) - (i > 9), s + 1, 0);
      feed(IC<2 * s + 1>{}, I);
      if constexpr (s == 1 && i < KT_OPS) ktab_op<ROLE, i>(c, kp, kt);
      if constexpr (ROLE == 0 && s == 3 && i == 0) __builtin_amdgcn_raw_buffer_store_b64(tagv, c.ws, one0, WS_FC + ((v * 14 + ft) * 16 + p) * 128, SC1);
      if constexpr (ROLE == 0 && (s == 2 || s == 3) && 32 * (s - 2) + i < KX_OPS) kexp_op<32 * (s - 2) + i>(c, kx);
      if constexpr (ROLE == 1 && s == 7) W1[i >> 3][(i >> 1) & 3][i & 1] = trd(wa[(i >> 1) & 3] + (i >> 3) * 16384 + (i & 1) * 2048);
      if constexpr (ROLE == 0) {
        if constexpr (s == 5 && i == 15) {
          asm volatile("s_waitcnt vmcnt(0)" : "+v"(lf)::"memory");
          if (!flags_up(c.tag, lf)) wait_flags<4>(c.ws, c.tag, ld_flag(c.ws, lfv, 0), lfv, 0);
        }
        if constexpr (s == 7 && (i == 4 || i == 9)) ur[i >> 3] = u_load(c, uv, i >> 3);
        if constexpr (s == 6 && i >= 8) { constexpr int e = i & 3, k = (i - 8) >> 2; cs4[e].v = (k ? cs4[e].v : __uint_as_float(cpr[0][e])) + __uint_as_float(cpr[k + 1][e]); pin(cs4[e].v); }
      }
      SB();
    });
    if constexpr (s < 2) colsum(Fb, 2 * s + 1);
  });
  STAMP(7);
}

template <int ROLE, class Hook>
DEV void geglu(const Ctx& c, f4 (&acc)[4][4], Hook&& hook) {
  const int p = (c.xc & 3) * 4 + (c.jj & 3), ft = (c.xc >> 2) * 7 + (c.jj >> 2);
  f4 cv[4]; unsigned st[4];
#pragma unroll
  for (int t = 0; t < 4; t++) { cv[t] = *LDSP(f4, LCS + c.v * 256 + t * 64 + c.q * 16); st[t] = *LDSP(unsigned, LST + (64 * ROLE + 16 * t + c.n) * 4); }
  mma_fence();
#pragma unroll
  for (int i = 0; i < 16; i++) asm volatile("" : "+v"(acc[i >> 2][i & 3]));
  const unsigned hbase = WS_H + uni((2 * p + ROLE) * 56 + 4 * ft + c.v) * 4096;
#pragma unroll
  for (int tt = 0; tt < 4; tt++) {
    const float nm = -blo(st[tt]), sd = bhi(st[tt]), r = __builtin_amdgcn_rcpf(sd);
    const float k0 = (-2.f * 0.7978845608028654f * 1.4426950408889634f) * r, k1 = k0 * r * r * 0.044715f, s2 = sd * sd;
    u4 o;
    hook(tt);
#pragma unroll
    for (int j = 0; j < 4; j++) {
      const int t = j >> 1, e = (j & 1) * 2;
      const f2 a = fma2(f2{nm, nm}, f2{cv[t][e], cv[t][e + 1]}, f2{acc[tt][t][e], acc[tt][t][e + 1]});
      const f2 b = fma2(f2{nm, nm}, f2{cv[t + 2][e], cv[t + 2][e + 1]}, f2{acc[tt][t + 2][e], acc[tt][t + 2][e + 1]});
      const f2 z = fma2(a * a, f2{k1, k1}, f2{k0, k0}) * a;
      const f2 ex = {__builtin_amdgcn_exp2f(z.x), __builtin_amdgcn_exp2f(z.y)};
      const f2 d = fma2(ex, f2{s2, s2}, f2{s2, s2});
      const f2 rc = {__builtin_amdgcn_rcpf(d.x), __builtin_amdgcn_rcpf(d.y)};
      o[j] = pk(a * b * rc);
    }
    __builtin_amdgcn_raw_buffer_store_b128(o, c.ws, c.lane * 16 + tt * 1024, hbase, SC1);
    SB();
  }
}
template <int ROLE, int YOUNGER> DEV void h_publish(const Ctx& c) {
  const int p = (c.xc & 3) * 4 + (c.jj & 3), ft = (c.xc >> 2) * 7 + (c.jj >> 2);
  waitv<YOUNGER>();
  st_flag(c.ws, c.tag, WS_FH + uni((2 * p + ROLE) * 56 + 4 * ft + c.v) * 128, c.lane);
}

DEV void ln_block(Ctx& c, const Params& P, KIn kin) {
  const int tok0 = (c.xc & 3) * 512 + (c.xc >> 2) * 256 + (c.jj - 28) * 64;
  const i4 sx = mksrd(P.x, L * D * 2);
  u4 rows[8];
#pragma unroll
  for (int i = 0; i < 8; i++) rows[i] = row_load(sx, c.lane * 16, (tok0 + 32 * (i >> 2) + 4 * c.w + (i & 3)) * 1024);
  if (threadIdx.x < 12) *LDSP(unsigned, LSY + threadIdx.x * 4) = 0u;
  kgen_take<8>(c, P, kin);
  asm volatile("s_waitcnt vmcnt(4)" : "+v"(rows[0]), "+v"(rows[1]), "+v"(rows[2]), "+v"(rows[3])::"memory");
  STAMP(1);
  ln_compute(c, rows, 0, tok0);
  asm volatile("s_waitcnt vmcnt(1)" : "+v"(rows[4]), "+v"(rows[5]), "+v"(rows[6]), "+v"(rows[7])::"memory");
  bar();
  ln_store(c, 0, tok0);
  STAMP(2);
  ln_compute(c, rows + 4, 34816, tok0 + 32);
  waitv<1>();
  bar();
  if (c.w == 0) st_flag(c.ws, c.tag, WS_FLN + (tok0 >> 5) * 128, c.lane);
  ln_store(c, 34816, tok0 + 32);
  STAMP(3);
  waitv<0>();
  if (lds_arrive(LSY + 32, c.lane) == 7) st_flag(c.ws, c.tag, WS_FLN + ((tok0 >> 5) + 1) * 128, c.lane);
  STAMP(4);
  const i4 swd = mksrd(P.wd, F * D * 2);
  const unsigned wq = (c.jj - 28) * 448 * 1024;
  if (c.w < 4) touch(swd, c.lane * 128, wq + (c.xc * 7 + 2 * c.w) * 4096);
  bf8 KA[8], KB[8]; KTab kt;
  const KPre kp = kgen_pre(c);
  auto warm = LAM(H) {
    constexpr int h = CV(H);
    if constexpr (!(h & 1) && h < 8) {
      if (G2MAP) {
        if ((c.w >> 2) == ((h >> 1) & 1) && 2 * h + c.v < 14) { unsigned k = 2 * h + c.v + ((c.xc >> 1) * 7 >> 1); k = k >= 14 ? k - 14 : k;
          touch(swd, (c.lane >> 2) * 1024 + (c.lane & 3) * 128, wq + 229376 + k * 16384 + (c.xc & 1) * 512); }
      } else if (h < 6 || c.w < 4) { unsigned k = 4 * h + c.w + ((c.xc * 7) >> 1); k = k >= 28 ? k - 28 : k; touch(swd, c.lane * 128, wq + 229376 + k * 8192); }
    }
  };
  if (c.w < 4) kgen_rows<0>(c, kp, kin, KA, warm); else kgen_rows<1>(c, kp, kin, KA, warm);
  bar();
  kgen_cols(c, KB); kgen_mma(kt.t, KA, KB); mma_fence();
  if (c.w < 4) { ktab_skip(c, kp, kt, kin.dsk); sfor<0, KT_OPS>(LAM(K) { ktab_op<0, CV(K)>(c, kp, kt); }); }
  else sfor<0, KT_OPS>(LAM(K) { ktab_op<1, CV(K)>(c, kp, kt); });
  bar();
  STAMP(5);
  if (blockIdx.x == 224 && c.w == 0) {
#pragma unroll 1
    for (int i = 0; i < 4; i++) {
      const int b = 64 * i + c.lane;
      const unsigned fv = b < 224 ? WS_FC + b * 128 : WS_FLN + (b - 224) * 256;
      wait_flags<16>(c.ws, c.tag, ld_flag(c.ws, fv, 0), fv, 0);
    }
    st_flag(c.ws, c.tag, WS_EPOCH, c.lane);
  }
  asm volatile("fused_end_a:");
}

struct CFr { bf4 a[8][2]; bf8 b[8]; };
DEV void conv_rd(CFr& Fr, int i, unsigned ka, unsigned ua, int sp) {
  if (i < 2) Fr.a[0][i] = trd(ka + 64 * sp + (i ? 0 : 8));
  else if (i < 10) Fr.b[i - 2] = *LDSP(bf8, ua + 512 * (i - 2) + 192 - 64 * sp);
  else { const int g = (i - 8) >> 1, t = i & 1; Fr.a[g][t] = trd(ka + 512 * g + 64 * sp + (t ? 0 : 8)); }
}
DEV void conv(Ctx& c) {
  const int chl = c.v & 1, kh = c.v >> 1, n = c.n, q = c.q;
  unsigned ka = LKC + chl * KCH + (n >> 2) * KCS + 2 * (16 + 4 * (n & 3) + 128 * kh + 16 - 8 * q) - 8;
  unsigned ua = LU + chl * UCH + 2 * (256 + 16 * n - 16 - 128 * kh + 8 * q) - 192;
  asm volatile("" : "+v"(ka), "+v"(ua));
  f4 acc[8];
  CFr Fr[2];
#pragma unroll
  for (int i = 0; i < 24; i++) conv_rd(Fr[0], i, ka, ua, 0);
  unsigned xw = LKC + chl * KCH + 32 + (c.lane >> 3) * 512 + (c.lane & 7) * 16, yo = (q & 1) * 512 + n * 32 + (q >> 1) * 16;
  asm volatile("" : "+v"(xw), "+v"(yo));
  lds_wait<1>(LSY + 44, 4);
  STAMP(11);
  SB();
#pragma unroll
  for (int sp = 0; sp < 4; sp++) {
    int i = 0;
#pragma unroll
    for (int g = 0; g < 8; g++)
#pragma unroll
      for (int m = 0; m + g < 8; m++) {
        if (sp == 0 && g == 0) mma0(acc[m], cat(Fr[0].a[0][0], Fr[0].a[0][1]), Fr[0].b[m]);
        else mma(acc[g + m], cat(Fr[sp & 1].a[g][0], Fr[sp & 1].a[g][1]), Fr[sp & 1].b[m]);
        if (sp < 3 && i < 24) conv_rd(Fr[(sp + 1) & 1], i, ka, ua, sp + 1);
        i++;
        SB();
      }
  }
  mma_fence();
#pragma unroll
  for (int i = 0; i < 8; i++) asm volatile("" : "+v"(acc[i]));
  unsigned pkd[4][2];
  auto finish = [&](auto KH) __attribute__((always_inline)) {
    constexpr int h = CV(KH);
#pragma unroll
    for (int j = 0; j < 4; j++) *LDSP(f4, xw + h * 256 + j * KCS) = acc[4 * (1 - h) + j];
    lds_post(LSY + 16 + c.v * 4, c.lane);
    STAMP(12);
    lds_wait<0>(LSY + 16 + (c.v ^ 2) * 4, 1);
#pragma unroll
    for (int j = 0; j < 4; j++) { const f4 t = acc[4 * h + j] + *LDSP(f4, xw + (1 - h) * 256 + j * KCS); pkd[j][0] = pk(f2{t[0], t[1]}); pkd[j][1] = pk(f2{t[2], t[3]}); }
  };
  if (kh) finish(IC<1>{}); else finish(IC<0>{});
  const int ch = 2 * (c.xc * 32 + c.jj) + chl;
#pragma unroll
  for (int j = 0; j < 4; j += 2) {
    swap16(pkd[j][0], pkd[j + 1][0]); swap16(pkd[j][1], pkd[j + 1][1]);
    __builtin_amdgcn_raw_buffer_store_b128(u4{pkd[j][0], pkd[j][1], pkd[j + 1][0], pkd[j + 1][1]}, c.ws,
                                           yo + j * 512, WS_YT + ch * 4096 + kh * 2048, SC1);
  }
  STAMP(13);
  waitv<0>();
  STAMP(14);
  st_flag(c.ws, c.tag, WS_FY + (kh * 512 + ch) * 128, c.lane);
}
DEV u4 x_load(const Ctx& c, const Params& P, int role, int l) {
  return __builtin_amdgcn_raw_buffer_load_b128(mkrs(P.x, L * D * 2), (l >> 3) * 1024 + (l & 7) * 16,
                                               (TILE_T(c) * 64 + 16 * c.v + 8 * role) * 1024 + TILE_C(c) * 128, 0);
}
DEV void y_gather(Ctx& c) {
  const int T64 = TILE_T(c), ct = TILE_C(c), l = c.lane;
  const int r0 = 32 * (c.v & 1) + 16 * (c.v >> 1);
  const unsigned fo = WS_FY + ((T64 >> 4) * 512 + 64 * ct + r0) * 128;
  unsigned fv = (l < 16 ? l : 15) * 128, yv = (l >> 3) * 4096 + (l & 7) * 16, ya = LU + (c.v & 1) * (UCH + 64) + (c.v >> 1) * 2080 + l * 16;
  asm volatile("" : "+v"(fv), "+v"(yv), "+v"(ya));
  lds_wait<2>(LSY + 40, 1);
  wait_flags<8>(c.ws, c.tag, ld_flag(c.ws, fv, fo), fv, fo);
  STAMP(15);
#pragma unroll
  for (int i = 0; i < 2; i++)
    *LDSP(u4, ya + i * 1040) = __builtin_amdgcn_raw_buffer_load_b128(c.ws, yv, WS_YT + (64 * ct + r0 + 8 * i) * 4096 + T64 * 128, SC1);
}
struct Fin { unsigned a0, a1, ya, oa; f4 a, b, y0, y1; };
template <int ROLE> DEV Fin finish_pre(const Ctx& c, u4 x, int l) {
  const int t = l >> 3, c8 = l & 7;
  Fin f;
  f.a0 = c.v * 16384 + (8 * ROLE + t) * 256 + ((2 * c8) ^ t) * 16; f.a1 = f.a0 ^ 16;
  f.ya = LU + (c8 >> 2) * (UCH + 64) + (c8 & 3) * 1040 + (16 * c.v + 8 * ROLE + t) * 2;
  f.oa = t * 1024 + c8 * 16;
  f.a = f4{blo(x[0]), bhi(x[0]), blo(x[1]), bhi(x[1])}; f.b = f4{blo(x[2]), bhi(x[2]), blo(x[3]), bhi(x[3])};
  f.y0 = f4{0.f, 0.f, 0.f, 0.f}; f.y1 = f.y0;
  asm volatile("" : "+v"(f.a0), "+v"(f.a1), "+v"(f.ya), "+v"(f.oa), "+v"(f.a), "+v"(f.b), "+v"(f.y0), "+v"(f.y1));
  return f;
}
template <int ROLE> DEV void finish_tile(Ctx& c, const Params& P, Fin f) {
  const int T64 = TILE_T(c), ct = TILE_C(c);
  const rsrc_t ro = mkrs(P.out, L * D * 2);
  bar();
  if (ROLE == 1) { STAMP(15); }
  float y[8] = {f.y0[0], f.y0[1], f.y0[2], f.y0[3], f.y1[0], f.y1[1], f.y1[2], f.y1[3]};
  asm volatile("ds_read_u16_d16_hi %0, %8\n\tds_read_u16_d16_hi %1, %8 offset:128\n\tds_read_u16_d16_hi %2, %8 offset:256\n\tds_read_u16_d16_hi %3, %8 offset:384\n\t"
               "ds_read_u16_d16_hi %4, %8 offset:512\n\tds_read_u16_d16_hi %5, %8 offset:640\n\tds_read_u16_d16_hi %6, %8 offset:768\n\tds_read_u16_d16_hi %7, %8 offset:896"
               : "+v"(y[0]), "+v"(y[1]), "+v"(y[2]), "+v"(y[3]), "+v"(y[4]), "+v"(y[5]), "+v"(y[6]), "+v"(y[7]) : "v"(f.ya) : "memory");
  f4 pa[4], pb[4];
#pragma unroll
  for (int k = 0; k < 4; k++) { pa[k] = *LDSP(f4, f.a0 + k * 4096); pb[k] = *LDSP(f4, f.a1 + k * 4096); }
  SB();
#pragma unroll
  for (int k = 0; k < 4; k++) { f.a += pa[k]; f.b += pb[k]; }
  asm volatile("s_waitcnt lgkmcnt(0)" : "+v"(y[0]), "+v"(y[1]), "+v"(y[2]), "+v"(y[3]), "+v"(y[4]), "+v"(y[5]), "+v"(y[6]), "+v"(y[7])::"memory");
  f.a += f4{y[0], y[1], y[2], y[3]}; f.b += f4{y[4], y[5], y[6], y[7]};
  __builtin_amdgcn_raw_buffer_store_b128(u4{pk(f2{f.a[0], f.a[1]}), pk(f2{f.a[2], f.a[3]}), pk(f2{f.b[0], f.b[1]}), pk(f2{f.b[2], f.b[3]})}, ro, f.oa,
                                         (T64 * 64 + 16 * c.v + 8 * ROLE) * 1024 + ct * 128, SC1);
}

DEV void gemm2(Ctx& c, const Params& P, const bf4 (&W1)[7][4][2]) {
  const int T64 = TILE_T(c), v = c.v;
  int n, q;
  unsigned l16 = c.lane * 16;
  asm volatile("" : "+v"(l16));
  const unsigned hk = uni(T64 * 56 + 14 * v);
  const unsigned fo = WS_FH + hk * 128, fv = (l16 < 224 ? l16 : 208) * 8;
  wait_flags<2>(c.ws, c.tag, ld_flag(c.ws, fv, fo), fv, fo);
  STAMP(12);
#define GWIN 3
  constexpr int WIN = GWIN;
  u4 H[14][4];
  auto hload = [&](int i, int t) {
    H[i][t] = __builtin_amdgcn_raw_buffer_load_b128(c.ws, l16 + t * 1024, WS_H + (hk + i) * 4096, SC1);
  };
#pragma unroll
  for (int i = 0; i < WIN; i++)
#pragma unroll
    for (int t = 0; t < 4; t++) hload(i, t);
  SB();
  f4 acc[4][4];
  bf4 W2[7][4][2];
  unsigned wa[4], pw[4]; u4 xr; Fin fin;
#pragma unroll
  for (int i = 0; i < 14; i++) {
    if (i == 5) {
      asm volatile("" : "+v"(l16));
      n = (l16 >> 4) & 15; q = l16 >> 8;
#pragma unroll
      for (int d = 0; d < 4; d++) { wa[d] = (wd_raddr(v, n, q) ^ (d * 32)) + SLOT_C; pin(wa[d]); }
      SB();
    }
    if (i == 9) {
#pragma unroll
      for (int d = 0; d < 4; d++) { wa[d] -= SLOT_C; pin(wa[d]); }
    }
#pragma unroll
    for (int j = 0; j < 16; j++) {
      const int t = j >> 2, d = j & 3;
      if (i == 0) mma0(acc[t][d], cat(W1[0][d][0], W1[0][d][1]), H[0][t]);
      else if (i < 7) mma(acc[t][d], cat(W1[i][d][0], W1[i][d][1]), H[i][t]);
      else mma(acc[t][d], cat(W2[i - 7][d][0], W2[i - 7][d][1]), H[i][t]);
      if (i + WIN < 14 && (j & 3) == 3) hload(i + WIN, t);
      if (i >= 6 && i < 13 && (j & 1) == 0) W2[i - 6][j >> 2][(j >> 1) & 1] = trd(wa[j >> 2] + (i < 9 ? i - 6 : i - 9) * 16384 + ((j >> 1) & 1) * 2048);
#define YSIG 8
      if (i == YSIG && j == 14) *LDSP(volatile unsigned, LSY + 40) = 1u;
      if (i == 6 && j == 1) xr = x_load(c, P, 1, l16 >> 4);
      if (i == 10 && j == 15) fin = finish_pre<1>(c, xr, l16 >> 4);
      if (i == 11 && (j & 3) == 3) { pw[t] = (v * 4096 + n * 256 + (q ^ (n & 7)) * 16) ^ (t * 64); pin(pw[t]); }
      if (i == 13 && j >= 8) { pin(acc[t - 2][d]); *LDSP(f4, pw[d] + (t - 2) * 16384) = acc[t - 2][d]; }
      SB();
    }
  }
  STAMP(13);
  asm volatile("s_nop 1");
#pragma unroll
  for (int j = 8; j < 16; j++) { pin(acc[j >> 2][j & 3]); *LDSP(f4, pw[j & 3] + (j >> 2) * 16384) = acc[j >> 2][j & 3]; if (j == 11) asm volatile("s_nop 3"); SB(); }
  STAMP(14);
  finish_tile<1>(c, P, fin);
}

__global__ void __launch_bounds__(512, 2) fused_s4_geglu(Params P) {
  Ctx c;
  STAMP(0);
  c.w = __builtin_amdgcn_readfirstlane(threadIdx.x >> 6);
  c.v = c.w & 3; c.lane = threadIdx.x & 63; c.n = c.lane & 15; c.q = c.lane >> 4;
  c.xc = blockIdx.x & 7; c.jj = blockIdx.x >> 3;
  {
    u64 pc; unsigned len;
    asm volatile("s_getpc_b64 %0\n.Lpc%=:\n\ts_mov_b32 %1, fused_end_a-.Lpc%=\n\ts_max_u32 %1, %1, fused_end_b-.Lpc%=" : "=s"(pc), "=s"(len)::"scc");
    if (c.w == 7) touch(mksrd((const void*)pc, len), c.lane * 128, (c.jj & 7) * 8192);
  }
  const bool g1 = c.jj < 28;
  const G1Src gs = g1_src(c);
  const KIn kin = kgen_load(c, P);
  if (__builtin_expect(!g1, 0)) ln_block(c, P, kin);
  if (c.w >= 4) {
    WdSrc wds;
    f4 acc[4][4]; bf4 W1[7][4][2]; unsigned wa[4];
    if (__builtin_expect(g1, 1)) { KExp kx; u4 ur[2]; gemm1<1>(c, P, gs, wds, kin, acc, W1, wa, kx, ur); }
    else {
      wds = wd_src(c, P);
#pragma unroll
      for (int i = 0; i < 7; i++) wd_chunk(c, wds, i);
#pragma unroll
      for (int d = 0; d < 4; d++) { wa[d] = (wd_raddr(c.v, c.n, c.q) ^ (d * 32)) + SLOT_C; pin(wa[d]); }
      waitv<16>();
#pragma unroll
      for (int i = 0; i < 16; i++) W1[i >> 3][(i >> 1) & 3][i & 1] = trd(wa[(i >> 1) & 3] + (i >> 3) * 16384 + (i & 1) * 2048);
      waitl();
    }
#pragma unroll
    for (int i = 2; i < 7; i++) {
      if (i == 3) {
#pragma unroll
        for (int d = 0; d < 4; d++) { wa[d] -= SLOT_C; pin(wa[d]); }
        waitv<4>();
      }
      if (i == 6) waitv<0>();
#pragma unroll
      for (int k = 0; k < 8; k++) W1[i][k >> 1][k & 1] = trd(wa[k >> 1] + (i < 3 ? i : i - 3) * 16384 + (k & 1) * 2048);
      SB();
    }
    STAMP(8);
    waitl();
#define NEARLY 0
#define NPUB 2
    if (g1) geglu<1>(c, acc, [&](int tt) __attribute__((always_inline)) { if (tt < NEARLY) wd_chunk(c, wds, 7 + tt); });
    else {
#pragma unroll
      for (int i = 7; i < 7 + NEARLY; i++) wd_chunk(c, wds, i);
    }
    lds_arrive(LSY + 44, c.lane);
    STAMP(9);
#pragma unroll
    for (int i = 7 + NEARLY; i < 7 + NEARLY + NPUB; i++) wd_chunk(c, wds, i);
    if (g1) h_publish<1, 4 * NPUB>(c);
    STAMP(10);
#pragma unroll
    for (int i = 7 + NEARLY + NPUB; i < 14; i++) wd_chunk(c, wds, i);
    STAMP(11);
    gemm2(c, P, W1);
    asm volatile("fused_end_b:");
  } else {
    if (__builtin_expect(g1, 1)) {
      f4 acc[4][4]; WdSrc none; bf4 W1[7][4][2]; unsigned wa[4]; KExp kx; u4 ur[2];
      gemm1<0>(c, P, gs, none, kin, acc, W1, wa, kx, ur);
      geglu<0>(c, acc, [](int) {});
      STAMP(8);
      sfor<0, 9>(LAM(K) { kexp_put<CV(K)>(c, kx); });
      u_write(c, ur[0], ur[1]);
    } else {
      const unsigned fv = (16 * c.v + (c.lane >> 2)) * 128;
      wait_flags<8>(c.ws, c.tag, ld_flag(c.ws, fv, WS_FLN), fv, WS_FLN);
      const u4 r0 = u_load(c, u_off(c), 0), r1 = u_load(c, u_off(c), 1);
      KExp kx;
      kexp_pre(c, kx);
      sfor<0, KX_OPS>(LAM(K) { kexp_op<CV(K)>(c, kx); });
      sfor<0, 9>(LAM(K) { kexp_put<CV(K)>(c, kx); });
      u_write(c, r0, r1);
    }
    lds_arrive(LSY, c.lane);
    STAMP(9);
    if (g1) h_publish<0, 0>(c);
    STAMP(10);
    lds_wait<1>(LSY, 4);
    const u4 xr = x_load(c, P, 0, c.lane);
    conv(c);
    const Fin fin = finish_pre<0>(c, xr, c.lane);
    y_gather(c);
    finish_tile<0>(c, P, fin);
  }
  FINISH();
}

extern "C" void kernel_launch(void* const* d_in, const int* in_sizes, int n_in, void* d_out, int out_size, void* d_ws, size_t ws_size,
                              hipStream_t stream) {
  Params P;
  P.x = (const u16*)d_in[0]; P.are = (const u16*)d_in[1]; P.aim = (const u16*)d_in[2]; P.bre = (const u16*)d_in[3];
  P.bim = (const u16*)d_in[4]; P.cre = (const u16*)d_in[5]; P.cim = (const u16*)d_in[6]; P.ldt = (const u16*)d_in[7];
  P.dsk = (const u16*)d_in[8]; P.wg = (const u16*)d_in[10]; P.wu = (const u16*)d_in[11]; P.wd = (const u16*)d_in[12];
  P.out = (u16*)d_out; P.ws = (char*)d_ws;
  (void)hipFuncSetAttribute((const void*)fused_s4_geglu, hipFuncAttributeMaxDynamicSharedMemorySize, LDS_BYTES);
  fused_s4_geglu<<<256, 512, LDS_BYTES, stream>>>(P);
}
```

```cpp
#include <hip/hip_runtime.h>
#include <hip/hip_bf16.h>
#include <stdint.h>
#pragma clang diagnostic ignored "-Wint-to-pointer-cast"

typedef unsigned long long u64;
typedef unsigned short u16;
typedef __bf16 bf2 __attribute__((ext_vector_type(2)));
typedef __bf16 bf4 __attribute__((ext_vector_type(4)));
typedef __bf16 bf8 __attribute__((ext_vector_type(8)));
typedef float f2 __attribute__((ext_vector_type(2)));
typedef float f4 __attribute__((ext_vector_type(4)));
typedef int i4 __attribute__((ext_vector_type(4)));
typedef unsigned u2 __attribute__((ext_vector_type(2)));
typedef unsigned u4 __attribute__((ext_vector_type(4)));
typedef __amdgpu_buffer_rsrc_t rsrc_t;
#define LDSP(T, a) ((__attribute__((address_space(3))) T*)(a))
#define DEV __device__ __forceinline__
#define SB() __builtin_amdgcn_sched_barrier(0)
#define SC1 16

template <int V> struct IC { static constexpr int value = V; };
template <int B, int E, class Fn> DEV void sfor(Fn&& f) { if constexpr (B < E) { f(IC<B>{}); sfor<B + 1, E>(f); } }
#define LAM(I) [&](auto I) __attribute__((always_inline))
#define CV(I) decltype(I)::value

constexpr int L = 2048, D = 512, NS = 128, F = 1792;

constexpr unsigned WS_EPOCH = 0;
constexpr unsigned WS_FLN = 8192;
constexpr unsigned WS_STATS = 16384;
constexpr unsigned WS_CPART = 65536;
constexpr unsigned WS_FC = 327680;
constexpr unsigned WS_FH = 524288;
constexpr unsigned WS_FY = 786432;
constexpr unsigned WS_XNT = 1u << 20;
constexpr unsigned WS_YT = 3u << 20;
constexpr unsigned WS_H = 5u << 20;
constexpr unsigned WS_BYTES = WS_H + 32u * 56u * 4096u;
constexpr unsigned WS_DBG = 16u << 20;

constexpr unsigned SLOT_C = 98816;
constexpr unsigned slot_of(int s) { return s % 3 == 0 ? 0u : s % 3 == 1 ? 49152u : SLOT_C; }
constexpr unsigned wd_base(int i) { return i < 3 ? SLOT_C + i * 16384 : (i - 3) * 16384; }
constexpr unsigned LKC = 65536;
constexpr unsigned KCS = 4160, KCH = 4 * KCS;
constexpr unsigned LKG = 98304;
constexpr unsigned LU = 147968;
constexpr unsigned UCH = 4608;
constexpr unsigned LCS = LU + 9216;
constexpr unsigned LST = LU + 10240;
constexpr unsigned LKT = 160064;
constexpr unsigned LSY = 163136;
constexpr unsigned LJK = 163328;
constexpr unsigned LDS_BYTES = 163840;

struct Params {
  const u16 *are, *aim, *ldt, *dsk, *bre, *bim, *cre, *cim;
  char* ws;
  const u16 *x, *wg, *wu, *wd;
  u16* out;
};

DEV i4 mksrd(const void* p, unsigned bytes) {
  u64 a = (u64)p; i4 r; r[0] = (int)(unsigned)a; r[1] = (int)(unsigned)(a >> 32); r[2] = (int)bytes; r[3] = 0x00020000; return r;
}
DEV rsrc_t mkrs(const void* p, unsigned bytes) { return __builtin_amdgcn_make_buffer_rsrc((void*)p, 0, (int)bytes, 0x00020000); }
DEV void glds(i4 srd, unsigned voff, unsigned soff, unsigned dst) {
  asm volatile("s_mov_b32 m0, %3\n\ts_nop 0\n\tbuffer_load_dwordx4 %1, %0, %2 offen lds" ::"s"(srd), "v"(voff), "s"(soff), "s"(dst) : "memory");
}
DEV void touch(i4 srd, unsigned voff, unsigned soff) {
  asm volatile("s_mov_b32 m0, %3\n\ts_nop 0\n\tbuffer_load_dword %1, %0, %2 offen lds" ::"s"(srd), "v"(voff), "s"(soff), "s"(LJK) : "memory");
}
template <int N> DEV void waitv() { asm volatile("s_waitcnt vmcnt(%0)" ::"i"(N) : "memory"); }
DEV void waitl() { __builtin_amdgcn_s_waitcnt(0xC07F); asm volatile("" ::: "memory"); }
DEV void bar() { asm volatile("" ::: "memory"); __builtin_amdgcn_s_waitcnt(0xC07F); asm volatile("s_barrier" ::: "memory"); }
DEV void set_m0(unsigned base, unsigned imm) { asm volatile("s_add_u32 m0, %0, %1" ::"s"(base), "i"(imm) : "memory", "scc"); }
template <int OFF> DEV void glds_at(i4 srd, unsigned voff, unsigned soff) {
  asm volatile("buffer_load_dwordx4 %1, %0, %2 offen offset:%3 lds" ::"s"(srd), "v"(voff), "s"(soff), "i"(OFF) : "memory");
}
template <class T> DEV void pin(T& x) { asm volatile("" : "+v"(x)); }
template <class T> DEV void spin_(T& x) { asm volatile("" : "+s"(x)); }
DEV bf4 trd(unsigned a) { return __builtin_amdgcn_ds_read_tr16_b64_v4bf16(LDSP(bf4, a)); }
DEV bf8 cat(bf4 a, bf4 b) { return __builtin_shufflevector(a, b, 0, 1, 2, 3, 4, 5, 6, 7); }
template <class TA, class TB> DEV void mma(f4& c, TA a, TB b) { asm volatile("v_mfma_f32_16x16x32_bf16 %0, %1, %2, %0" : "+v"(c) : "v"(a), "v"(b)); }
template <class TA, class TB> DEV void mma0(f4& c, TA a, TB b) { asm volatile("v_mfma_f32_16x16x32_bf16 %0, %1, %2, 0" : "=&v"(c) : "v"(a), "v"(b)); }
DEV void mma_fence() { asm volatile("s_nop 7\n\ts_nop 1" ::: "memory"); }
DEV float blo(unsigned v) { return __uint_as_float(v << 16); }
DEV float bhi(unsigned v) { return __uint_as_float(v & 0xffff0000u); }
DEV f2 unpk(unsigned v) { return f2{blo(v), bhi(v)}; }
DEV unsigned pk(f2 v) { return __builtin_bit_cast(unsigned, __builtin_convertvector(v, bf2)); }
DEV float rbf(float v) { return blo(pk(f2{v, v})); }
DEV f2 fma2(f2 a, f2 b, f2 c) { return __builtin_elementwise_fma(a, b, c); }
template <int CTRL> DEV float dpp_add(float v) {
  return v + __builtin_bit_cast(float, __builtin_amdgcn_update_dpp(0, __builtin_bit_cast(int, v), CTRL, 0xf, 0xf, true));
}
DEV void swap16(unsigned& a, unsigned& b) { auto r = __builtin_amdgcn_permlane16_swap(a, b, false, false); a = r[0]; b = r[1]; }
DEV void swap32(unsigned& a, unsigned& b) { auto r = __builtin_amdgcn_permlane32_swap(a, b, false, false); a = r[0]; b = r[1]; }
DEV float swap16_add(float a, float b) { unsigned x = __float_as_uint(a), y = __float_as_uint(b); swap16(x, y); return __uint_as_float(x) + __uint_as_float(y); }
DEV float swap32_add(float a, float b) { unsigned x = __float_as_uint(a), y = __float_as_uint(b); swap32(x, y); return __uint_as_float(x) + __uint_as_float(y); }
DEV float red2(float a, float b) {
  float s = swap32_add(a, b);
  s = swap16_add(s, s);
  s = dpp_add<0x128>(s); s = dpp_add<0x124>(s); s = dpp_add<0x122>(s); s = dpp_add<0x121>(s);
  return s;
}
DEV float rdl(float v, int l) { return __builtin_bit_cast(float, __builtin_amdgcn_readlane(__builtin_bit_cast(int, v), l)); }
DEV unsigned uni(unsigned v) { return (unsigned)__builtin_amdgcn_readfirstlane((int)v); }
DEV u4 row_load(i4 srd, unsigned voff, unsigned soff) {
  u4 r; asm volatile("buffer_load_dwordx4 %0, %2, %1, %3 offen" : "=v"(r) : "s"(srd), "v"(voff), "s"(soff) : "memory"); return r;
}
constexpr unsigned SPIN_CAP = 1u << 15;
DEV u2 ld_flag(rsrc_t rs, unsigned voff, unsigned soff) { return __builtin_amdgcn_raw_buffer_load_b64(rs, voff, soff, SC1); }
DEV unsigned flag_off(int lane) { return lane < 16 ? lane * 8 : 0x7ffffff0u; }
DEV void st_flag(rsrc_t rs, u2 tag, unsigned soff, int lane) { __builtin_amdgcn_raw_buffer_store_b64(tag, rs, flag_off(lane), soff, SC1); }
DEV bool flags_up(u2 tag, u2 v) { return __builtin_amdgcn_ballot_w64(__builtin_bit_cast(u64, v) != __builtin_bit_cast(u64, tag)) == 0; }
template <int NAP> DEV void wait_flags(rsrc_t rs, u2 tag, u2 v, unsigned voff, unsigned soff) {
#pragma nounroll
  for (unsigned spin = 0; spin < SPIN_CAP; ++spin) {
    if (flags_up(tag, v)) break;
    __builtin_amdgcn_s_sleep(NAP);
    asm volatile("" ::: "memory");
    v = ld_flag(rs, voff, soff);
  }
  asm volatile("" : "+v"(v)::"memory");
}
DEV void lds_post(unsigned a, int lane) { asm volatile("" ::: "memory"); if (lane == 0) *LDSP(volatile unsigned, a) = 1u; }
template <int NAP> DEV void lds_wait(unsigned a, unsigned want) {
#pragma nounroll
  for (unsigned spin = 0; spin < (SPIN_CAP << 6); ++spin) {
    if (uni(*LDSP(volatile unsigned, a)) >= want) break;
    if (NAP) __builtin_amdgcn_s_sleep(NAP);
  }
  asm volatile("" ::: "memory");
}
DEV unsigned lds_arrive(unsigned a, int lane) {
  asm volatile("s_waitcnt lgkmcnt(0)" ::: "memory");
  unsigned o = 0;
  if (lane == 0) o = __hip_atomic_fetch_add(LDSP(unsigned, a), 1u, __ATOMIC_RELAXED, __HIP_MEMORY_SCOPE_WORKGROUP);
  return uni(o);
}

#define STAMP(i)
#define FINISH()

#define TILE_P(c) (c.xc & 1)
#define TILE_T(c) ((c.xc >> 1) * 8 + 2 * (c.jj & 3) + TILE_P(c))
#define TILE_C(c) (c.jj >> 2)
#define GPRIO 0
#define CPF 2
struct Ctx {
  int xc, jj, w, v, lane, n, q;
  u2 tag;
  rsrc_t ws;
};

struct alignas(16) F1 { float v; };
DEV float red4(float a, float b, float c, float d) {
  float s = swap16_add(swap32_add(a, b), swap32_add(c, d));
  s = dpp_add<0x128>(s); s = dpp_add<0x124>(s); s = dpp_add<0x122>(s); s = dpp_add<0x121>(s);
  return s;
}
DEV int ln_row(int r) { return r * 1088 + (r & 8) * 4; }
DEV void ln_compute(const Ctx& c, const u4* rows, unsigned img, int tok0) {
  f2 x[4][4]; float t[4];
#pragma unroll
  for (int k = 0; k < 4; k++) {
#pragma unroll
    for (int i = 0; i < 4; i++) x[k][i] = unpk(rows[k][i]);
    const f2 s = (x[k][0] + x[k][1]) + (x[k][2] + x[k][3]); t[k] = s.x + s.y;
  }
  float mean = red4(t[0], t[2], t[1], t[3]) * (1.f / D);
#pragma unroll
  for (int k = 0; k < 4; k++) {
    const float m = rdl(mean, 16 * k);
    f2 qq = {0.f, 0.f};
#pragma unroll
    for (int i = 0; i < 4; i++) { x[k][i] -= m; qq = fma2(x[k][i], x[k][i], qq); }
    t[k] = qq.x + qq.y;
  }
  float var = red4(t[0], t[2], t[1], t[3]) * (1.f / D);
  const unsigned st = pk(f2{mean, __builtin_amdgcn_sqrtf(rbf(rbf(var) + 1e-5f))});
  const float r = __builtin_amdgcn_rcpf(bhi(st)), o = (mean - blo(st)) * r;
#pragma unroll
  for (int k = 0; k < 4; k++) {
    const float rk = rdl(r, 16 * k), ok = rdl(o, 16 * k);
    u4 nv;
#pragma unroll
    for (int i = 0; i < 4; i++) nv[i] = pk(fma2(x[k][i], f2{rk, rk}, f2{ok, ok}));
    *LDSP(u4, img + ln_row(4 * c.w + k) + c.lane * 16) = nv;
  }
  if (c.n == 0) *LDSP(unsigned, img + 1024 + (c.w >> 1) * 1088 + (4 * (c.w & 1) + c.q) * 4) = st;
}
DEV void ln_store(const Ctx& c, unsigned img, int tok0) {
  const unsigned a = img + ln_row(8 * c.q + (c.n >> 2)) + (64 * c.w + 4 * (c.n & 3)) * 2;
  if (c.w == 7) __builtin_amdgcn_raw_buffer_store_b32(*LDSP(unsigned, img + 1024 + ((c.lane >> 3) & 3) * 1088 + (c.lane & 7) * 4), c.ws,
                                                      c.lane < 32 ? c.lane * 4 : 0x7ffffff0u, WS_STATS + tok0 * 4, SC1);
#pragma unroll
  for (int i = 0; i < 4; i++)
    __builtin_amdgcn_raw_buffer_store_b128(__builtin_bit_cast(u4, cat(trd(a + 32 * i), trd(a + 32 * i + 4 * 1088))), c.ws, c.n * 64 + c.q * 16 + i * 1024,
                                           WS_XNT + tok0 * 1024 + c.w * 4096, SC1);
}
DEV unsigned u_off(const Ctx& c) { return (c.lane >> 3) * 32768 + (c.lane & 7) * 16; }
DEV u4 u_load(const Ctx& c, unsigned uv, int i) {
  return __builtin_amdgcn_raw_buffer_load_b128(c.ws, uv, WS_XNT + (16 * c.v + 8 * i) * 32768 + (c.xc * 32 + c.jj) * 128, SC1);
}
DEV void u_write(const Ctx& c, u4 r0, u4 r1) {
  const unsigned a = LU + ((c.lane >> 2) & 1) * UCH + 512 + (16 * c.v + (c.lane >> 3)) * 64 + (c.lane & 3) * 16;
  *LDSP(u4, a) = r0;
  *LDSP(u4, a + 512) = r1;
  if (c.v == 0) *LDSP(u4, LU + (c.lane & 1) * UCH + (c.lane >> 1) * 16) = u4{0, 0, 0, 0};
}
DEV u2 flag_sample(const Params& P, unsigned voff, unsigned soff) {
  u2 r; asm volatile("buffer_load_dwordx2 %0, %2, %1, %3 offen sc1" : "=v"(r) : "s"(mksrd(P.ws, WS_BYTES)), "v"(voff), "s"(soff) : "memory"); return r;
}

struct C2 { f2 re, im; };
DEV C2 cmul(C2 a, C2 b) { C2 r; r.re = fma2(a.re, b.re, -(a.im * b.im)); r.im = fma2(a.re, b.im, a.im * b.re); return r; }
DEV C2 zpow(f2 al, f2 th, float e) {
  const f2 a = al * e, t0 = th * e;
  const f2 t = {__builtin_amdgcn_fractf(t0.x), __builtin_amdgcn_fractf(t0.y)};
  const f2 m = {__builtin_amdgcn_exp2f(a.x), __builtin_amdgcn_exp2f(a.y)};
  C2 r; r.re = m * f2{__builtin_amdgcn_cosf(t.x), __builtin_amdgcn_cosf(t.y)}; r.im = m * f2{__builtin_amdgcn_sinf(t.x), __builtin_amdgcn_sinf(t.y)};
  return r;
}
struct KIn { unsigned are, aim, bre, bim, cre, cim, ldt, dsk; u64 epoch; };
DEV unsigned par_load(const u16* p, unsigned voff) {
  unsigned r; asm volatile("global_load_dword %0, %1, %2" : "=v"(r) : "v"(voff), "s"(p) : "memory"); return r;
}
DEV unsigned spar_load(const u16* p, unsigned off) { unsigned r; asm volatile("s_load_dword %0, %1, %2" : "=s"(r) : "s"(p), "s"(off) : "memory"); return r; }
template <int ROLE> DEV KIn kgen_load(const Ctx& c, const Params& P) {
  const unsigned pair = c.xc * 32 + c.jj, o = (2 * pair + (c.w & 1)) * 256 + c.lane * 4;
  KIn r;
  r.are = par_load(P.are, o); r.aim = par_load(P.aim, o);
  r.ldt = spar_load(P.ldt, pair * 4); r.dsk = spar_load(P.dsk, pair * 4);
  asm volatile("s_load_dwordx2 %0, %1, 0" : "=s"(r.epoch) : "s"(P.ws + WS_EPOCH) : "memory");
  if (ROLE == 0 && c.w < 2) { r.bre = par_load(P.bre, o); r.bim = par_load(P.bim, o); r.cre = par_load(P.cre, o); r.cim = par_load(P.cim, o); }
  return r;
}
template <int ROLE, int N> DEV void kgen_take(Ctx& c, const Params& P, KIn& k) {
  KIn o; char* ws = P.ws;
  asm volatile("s_waitcnt lgkmcnt(0)\n\ts_mov_b32 %0, %4\n\ts_mov_b32 %1, %5\n\ts_mov_b64 %2, %6"
               : "=&s"(o.ldt), "=&s"(o.dsk), "=&s"(o.epoch), "+s"(ws) : "s"(k.ldt), "s"(k.dsk), "s"(k.epoch) : "memory");
  asm volatile("s_waitcnt vmcnt(%4)\n\tv_mov_b32 %0, %2\n\tv_mov_b32 %1, %3" : "=&v"(o.are), "=&v"(o.aim) : "v"(k.are), "v"(k.aim), "i"(N) : "memory");
  if (ROLE == 0) asm volatile("v_mov_b32 %0, %4\n\tv_mov_b32 %1, %5\n\tv_mov_b32 %2, %6\n\tv_mov_b32 %3, %7"
               : "=&v"(o.bre), "=&v"(o.bim), "=&v"(o.cre), "=&v"(o.cim) : "v"(k.bre), "v"(k.bim), "v"(k.cre), "v"(k.cim) : "memory");
  k = o;
  c.tag = u2{(unsigned)(o.epoch + 1), (unsigned)((o.epoch + 1) >> 32)};
  c.ws = mkrs(ws, WS_BYTES);
}
constexpr unsigned LKB = LKG + 44416;
struct KPre { float del, g0, g1, rev; unsigned wa, wb, rd, sh, rs, ta, tb, s0, s1; bool sw; };
template <int ROLE> DEV void kgen_pre(const Ctx& c, KPre& k) {
  const bool hi = c.v >= 2;
  const int ch = c.w & 1;
  const unsigned T = ROLE == 0 ? (hi ? LKG : LKB) + ch * 8672 : (hi ? LKG + 17344 + ch * 8672 : LKG + 34688 + ch * 4864);
  k.del = ROLE == 0 ? (hi ? 1.f : 8.f) : (hi ? 128.f : 2.f);
  k.g0 = ROLE == 0 ? (hi ? 128.f : 64.f) : (hi ? 512.f : 256.f);
  k.g1 = hi ? 516.f : 2048.f;
  k.rev = (ROLE == 0 && !hi) ? -0.15915494309189535f : 0.15915494309189535f;
  k.wa = T + c.lane * 8; k.wb = k.wa + ((ROLE == 1 && !hi) ? 4 : 8) * 544; k.rd = T + c.n * 544 + c.q * 16;
  k.sh = ch ? 0 : 16;
  k.rs = hi ? 1088 : 544;
  pin(k.del); pin(k.g0); pin(k.wa); pin(k.rd);
  if (ROLE == 0) pin(k.rev); else { pin(k.g1); pin(k.wb); }
}
template <int ROLE> DEV void ktab_pre(const Ctx& c, KPre& k) {
  const bool hi = c.v >= 2;
  const int n = c.n, q = c.q;
  const unsigned tb = LKT + (c.w & 1) * 1536;
  if (ROLE == 0) { k.ta = hi ? tb + 256 * (q >> 1) + 8 * (q & 1) + 16 * n : LJK + c.lane * 8; pin(k.ta); }
  else {
    const bool far = hi && q >= 2;
    k.ta = tb + 128 * q + (!hi ? (q < 2 ? 512 : 1024) + 8 * n : 768 + (q < 2 ? 4 * n : 62 * (n & 1) + 2 * n));
    k.tb = k.ta + (!hi ? 4 : q < 2 ? 64 : 32);
    k.s0 = !far ? 0x03020100u : (n & 1) ? 0x01000504u : 0x05040100u;
    k.s1 = k.s0 + (far ? 0x02020202u : 0x04040404u);
    k.sw = far && (n & 1);
    pin(k.ta); pin(k.tb); pin(k.s0); pin(k.s1);
  }
}
template <int ROLE, class Hook>
DEV void kgen_rows(const Ctx& c, const KPre& p, const KIn& k, bf8 (&A)[8], Hook&& hook) {
  const float dt = rbf(__builtin_amdgcn_exp2f(bhi(k.ldt << p.sh) * 1.4426950408889634f));
  const f2 al = (unpk(k.are) * dt) * 1.4426950408889634f;
  const f2 th = (unpk(k.aim) * dt) * p.rev;
  auto put = [&](unsigned ad, C2 z) __attribute__((always_inline)) { *LDSP(u2, ad) = u2{pk(z.re), pk(z.im)}; };
  C2 st = zpow(al, th, p.del);
  pin(st.re); pin(st.im);
  hook(IC<0>{});
  C2 a, b, e;
  if (ROLE == 0) {
    a = C2{f2{1.f, 1.f}, f2{0.f, 0.f}}; b = zpow(al, th, p.g0);
    if (c.w < 2) { a = cmul(C2{unpk(k.cre), -unpk(k.cim)}, C2{unpk(k.bre) * dt, unpk(k.bim) * -dt}); b = cmul(b, a); }
  } else {
    a = zpow(al, th, p.g0);
    if (c.w >= 6) b = cmul(a, a); else b = zpow(al, th, 384.f);
  }
  pin(b.re); pin(b.im);
  hook(IC<1>{});
  sfor<0, 4>(LAM(I) {
    constexpr int i = CV(I);
    put(p.wa + i * (ROLE ? p.rs : 544), a); put(p.wb + i * 544, b);
    hook(IC<2 + i>{});
    if constexpr (ROLE == 0 || i < 3) a = cmul(a, st); else a = zpow(al, th, p.g1);
    b = cmul(b, st);
  });
  if (ROLE == 1 && c.w < 6) { put(p.wa + 8 * 544, a); hook(IC<6>{}); hook(IC<7>{}); hook(IC<8>{}); }
  else sfor<4, 8>(LAM(I) {
    constexpr int i = CV(I);
    put(p.wa + (ROLE ? 2 * i - 7 : i) * 544, a); put(p.wb + i * 544, b);
    if constexpr (i < 7) { hook(IC<2 + i>{}); a = cmul(a, st); b = cmul(b, st); }
  });
#pragma unroll
  for (int i = 0; i < 8; i++) A[i] = *LDSP(bf8, p.rd + i * 64);
}
DEV void kgen_cols(const Ctx& c, bf8 (&B)[8]) {
#pragma unroll
  for (int i = 0; i < 8; i++) B[i] = *LDSP(bf8, LKB + (c.w & 1) * 8672 + c.n * 544 + c.q * 16 + i * 64);
}
DEV void kgen_mma(f4& t, const bf8 (&A)[8], const bf8 (&B)[8]) {
  mma0(t, A[0], B[0]);
#pragma unroll
  for (int i = 1; i < 8; i++) mma(t, A[i], B[i]);
}
struct alignas(16) U1 { unsigned v; };
struct KTab { f4 t; U1 a, b, p[2]; };
constexpr int KT_OPS = 9;
template <int ROLE, int k> DEV void ktab_op(const Ctx& c, const KPre& p, KTab& z) {
  if constexpr (ROLE == 0) {
    if constexpr (k == 0) { pin(z.t); z.t[0] += __uint_as_float(z.a.v); pin(z.t); }
    if constexpr (k == 1) { z.p[1].v = pk(f2{z.t[2], z.t[3]}); pin(z.p[1].v); }
    if constexpr (k == 2) { z.p[0].v = pk(f2{z.t[0], z.t[1]}); pin(z.p[0].v); }
    if constexpr (k == 3) *LDSP(u2, p.ta) = u2{z.p[0].v, z.p[1].v};
  } else {
    if constexpr (k == 0) { pin(z.t); z.p[0].v = pk(f2{z.t[0], z.t[1]}); pin(z.p[0].v); }
    if constexpr (k == 1) { z.p[1].v = pk(f2{z.t[2], z.t[3]}); pin(z.p[1].v); }
    if constexpr (k == 2) { z.b.v = p.sw ? z.p[0].v : z.p[1].v; pin(z.b.v); }
    if constexpr (k == 3) { z.a.v = p.sw ? z.p[1].v : z.p[0].v; pin(z.a.v); }
    if constexpr (k == 4) { z.b.v = (unsigned)__builtin_amdgcn_update_dpp((int)z.b.v, (int)z.b.v, 0xb1, 0xc, 0xf, false); pin(z.b.v); }
    if constexpr (k == 5) { z.p[0].v = __builtin_amdgcn_perm(z.b.v, z.a.v, p.s0); pin(z.p[0].v); }
    if constexpr (k == 6) { z.p[1].v = __builtin_amdgcn_perm(z.b.v, z.a.v, p.s1); pin(z.p[1].v); }
    if constexpr (k == 7) *LDSP(unsigned, p.ta) = z.p[0].v;
    if constexpr (k == 8) *LDSP(unsigned, p.tb) = z.p[1].v;
  }
}
DEV void ktab_skip(const Ctx& c, const KPre& p, KTab& z, unsigned dsk) { z.a.v = (c.w >= 2 && c.lane == 0) ? (dsk << p.sh) & 0xffff0000u : 0u; pin(z.a.v); }
typedef unsigned u2a __attribute__((ext_vector_type(2), aligned(4)));
struct KExp { U1 a0, a1, a2, a3, s[7], wr, h0, pv[2][2], o[2][4][4], es[3]; u2 e[4]; };
DEV void kexp_pre(const Ctx& c, KExp& z) {
  const int kh = c.v >> 1, l = c.lane;
  const unsigned CC = LKT + (c.v & 1) * 1536;
  const bool up = l >= 32, odd = l & 1;
  z.a0.v = CC + (kh ? 768 + l * 4 : up ? 256 + l * 8 : l * 16);
  z.a1.v = z.a0.v + (kh ? 0 : up ? 4 : 8);
  z.a2.v = CC + 1024 + 128 * kh + (l >> 1) * 4;
  z.a3.v = CC + 764;
  z.s[0].v = (kh || up) ? 0x01000100u : 0x03020100u;
  z.s[1].v = kh ? 0x03020100u : up ? 0x05040302u : 0x07060504u;
  z.s[2].v = kh ? 0x03020302u : z.s[0].v;
  z.s[3].v = kh ? 0x05040504u : z.s[1].v;
  z.s[4].v = odd ? 0x03020302u : 0x01000100u;
  z.s[5].v = odd ? 0x05040504u : 0x03020100u;
  z.s[6].v = odd ? 0x05040504u : 0x03020302u;
  z.wr.v = LKC + (c.v & 1) * KCH + 32 + kh * 1024 + l * 16;
  z.es[0].v = kh ? 0x01000100u : 0x0c0c0c0cu; z.es[1].v = kh ? 0x05040302u : 0x0c0c0c0cu; z.es[2].v = kh ? 0x05040504u : 0x03020302u;
  asm volatile("" : "+s"(z.es[0].v), "+s"(z.es[1].v), "+s"(z.es[2].v));
  asm volatile("" : "+v"(z.a0.v), "+v"(z.a1.v), "+v"(z.a2.v), "+v"(z.a3.v), "+v"(z.s[0].v), "+v"(z.s[1].v), "+v"(z.s[2].v), "+v"(z.s[3].v),
                    "+v"(z.s[4].v), "+v"(z.s[5].v), "+v"(z.s[6].v), "+v"(z.wr.v));
}
constexpr int KX_OPS = 45;
template <int k> DEV void kexp_op(const Ctx& c, KExp& z) {
  auto set = [&](U1& d, unsigned x) __attribute__((always_inline)) { d.v = x; pin(d.v); };
  if constexpr (k == 0) z.e[0] = *LDSP(u2a, z.a0.v);
  else if constexpr (k == 1) z.e[1] = *LDSP(u2a, z.a1.v);
  else if constexpr (k == 2) z.e[2] = *LDSP(u2a, z.a2.v);
  else if constexpr (k == 3) z.e[3] = *LDSP(u2a, z.a3.v);
  else if constexpr (k == 4) z.h0.v = *LDSP(unsigned, z.a3.v + 256);
  else if constexpr (k < 9) { constexpr int i = k - 5; set(z.o[0][0][i], __builtin_amdgcn_perm(z.e[i >> 1][1], z.e[i >> 1][0], z.s[i].v)); }
  else if constexpr (k < 13) { constexpr int i = k - 9;
    if constexpr (i == 1) asm volatile("v_mov_b32 %0, %1" : "=v"(z.o[1][0][1].v) : "v"(z.o[1][0][0].v));
    else set(z.o[1][0][i], __builtin_amdgcn_perm(z.e[2][1], z.e[2][0], z.s[i ? 3 + i : 4].v));
  }
  else if constexpr (k == 13) { z.pv[0][0].v = __builtin_amdgcn_perm(z.e[3][1], z.e[3][0], z.es[0].v); pin(z.pv[0][0].v); }
  else if constexpr (k == 14) { z.pv[0][1].v = __builtin_amdgcn_perm(z.e[3][1], z.e[3][0], z.es[1].v); pin(z.pv[0][1].v); }
  else if constexpr (k == 15) { z.pv[1][0].v = __builtin_amdgcn_perm(z.e[2][0], z.h0.v, z.es[2].v); pin(z.pv[1][0].v); }
  else if constexpr (k == 16) { z.pv[1][1].v = z.o[1][0][0].v; pin(z.pv[1][1].v); }
  else if constexpr (k < 21) {
    constexpr int i = (k - 17) >> 1, j = (k - 17) & 1;
    z.pv[i][j].v = (unsigned)__builtin_amdgcn_update_dpp((int)z.pv[i][j].v, (int)z.o[i][0][2 + j].v, 0x138, 0xf, 0xf, false); pin(z.pv[i][j].v);
  } else {
    constexpr int i = (k - 21) / 12, cp = 1 + (k - 21) % 12 / 4, j = (k - 21) % 4;
    auto dw = [&](int m) __attribute__((always_inline)) { return m < 0 ? z.pv[i][2 + m].v : z.o[i][0][m].v; };
    if constexpr (cp == 2) asm volatile("v_mov_b32 %0, %1" : "=v"(z.o[i][2][j].v) : "v"(dw(j - 1)));
    else asm volatile("v_alignbit_b32 %0, %1, %2, 16" : "=v"(z.o[i][cp][j].v) : "v"(dw(j - (cp >> 1))), "v"(dw(j - 1 - (cp >> 1))));
  }
}
template <int k> DEV void kexp_put(const Ctx& c, const KExp& z) {
  if constexpr (k < 8) *LDSP(u4, z.wr.v + (k >> 2) * 2048 + (k & 3) * KCS) = u4{z.o[k >> 2][k & 3][0].v, z.o[k >> 2][k & 3][1].v, z.o[k >> 2][k & 3][2].v, z.o[k >> 2][k & 3][3].v};
  else *LDSP(unsigned, LKC + (c.lane >> 3) * KCS + (c.lane & 7) * 4) = 0u;
}

struct WdSrc { i4 srd; unsigned voff[4], par; };
DEV WdSrc wd_src(const Ctx& c, const Params& P) {
  WdSrc s; s.srd = mksrd(P.wd, F * D * 2); s.par = TILE_P(c) * 32768;
  const int r = c.lane >> 3;
#pragma unroll
  for (int k = 0; k < 4; k++) { s.voff[k] = (448 * c.v + r) * 1024 + TILE_C(c) * 128 + (((c.lane & 7) ^ (r & 6)) * 16) + k * 7168; pin(s.voff[k]); }
  return s;
}
DEV unsigned wd_raddr(int v, int n, int q) { return v * 4096 + (4 * q + (n >> 2)) * 128 + (((2 * q + (n >> 3)) & 3) * 32) + (n & 3) * 8; }
DEV void wd_ld(const WdSrc& s, int i, int r) {
  const unsigned so = s.par ^ ((i % 7) * 65536 + (i / 7) * 32768);
  if (r == 0) glds_at<0>(s.srd, s.voff[0], so);
  else if (r == 1) glds_at<1024>(s.srd, s.voff[1], so);
  else if (r == 2) glds_at<2048>(s.srd, s.voff[2], so);
  else glds_at<3072>(s.srd, s.voff[3], so);
}
DEV void wd_chunk(const Ctx& c, const WdSrc& s, int i) {
  set_m0(c.v * 4096, wd_base(i % 7)); asm volatile("s_nop 0");
#pragma unroll
  for (int r = 0; r < 4; r++) wd_ld(s, i, r);
}

struct Frags { bf4 a[4][2]; bf8 b[4]; };
DEV void rd_piece(Frags& Fr, int i, unsigned wa0, unsigned wa1, unsigned xa, int ks) {
  if (i == 0) Fr.b[0] = *LDSP(bf8, xa);
  else if (i < 9) { const int t = (i - 1) >> 1, h = (i - 1) & 1; Fr.a[t][h] = trd(((t & 1) ? wa1 : wa0) + (t >> 1) * 16384 + ks * 8192 + h * 1024); }
  else Fr.b[i - 8] = *LDSP(bf8, xa + (i - 8) * 2048);
}

struct G1Src { i4 sx, sg, su; unsigned vx0, vx1, vw0, vw1; int k0; };
DEV G1Src g1_src(const Ctx& c) {
  const int p = (c.xc & 3) * 4 + (c.jj & 3), ft = (c.xc >> 2) * 7 + (c.jj >> 2);
  G1Src g;
  g.k0 = 2 * (c.xc & 3);
  const int xr = c.lane >> 3;
  g.vx0 = (p * 128 + 16 * c.w + xr) * 1024 + (((c.lane & 7) ^ (xr >> 1)) * 16);
  g.vx1 = (g.vx0 ^ 64) + 8192 - 1024;
  g.vw0 = (8 * c.w + c.q) * 3584 + ft * 256 + ((c.n ^ ((c.q | ((c.w & 1) << 2)) << 1)) * 16);
  g.vw1 = g.vw0 + 4 * 3584 - 1024;
  asm volatile("" : "+v"(g.vx0), "+v"(g.vx1), "+v"(g.vw0), "+v"(g.vw1));
  return g;
}
DEV void g1_m0(const Ctx& c, int s, int i) { set_m0(c.w * 2048, slot_of(s) + (i >> 1) * 16384); }
DEV void g1_ld(const G1Src& g, int s, int i) {
  const int sk = (s + g.k0) & 7;
  if (i == 0) glds_at<0>(g.sx, g.vx0, sk * 128);
  else if (i == 1) glds_at<1024>(g.sx, g.vx1, sk * 128);
  else if (i & 1) glds_at<1024>(i > 3 ? g.su : g.sg, g.vw1, sk * 64 * 3584);
  else glds_at<0>(i > 3 ? g.su : g.sg, g.vw0, sk * 64 * 3584);
}
struct G1Rot { i4 srd[2]; unsigned vo[2], dst[4], so[2]; };
DEV G1Rot g1_rot(const Ctx& c, const G1Src& g) {
  const int mine = c.jj & 3;
  G1Rot r;
#pragma unroll
  for (int k = 0; k < 2; k++) {
    const bool up = ((mine >> 1) ^ k) & 1;
    r.srd[k] = g.sg; r.srd[k][0] = up ? g.su[0] : g.sg[0]; r.srd[k][1] = up ? g.su[1] : g.sg[1];
    r.vo[k] = g.vw0 + ((mine ^ k) & 1) * 4 * 3584;
    r.so[k] = (g.k0 + k) * 64 * 3584;
  }
#pragma unroll
  for (int k = 0; k < 4; k++) r.dst[k] = 16384 + ((mine ^ k) >> 1) * 16384 + c.w * 2048 + ((mine ^ k) & 1) * 1024;
  return r;
}
template <int S, int K> DEV void g1_ld_w(const G1Rot& r) {
  asm volatile("s_add_u32 m0, %3, %4\n\ts_nop 0\n\tbuffer_load_dwordx4 %1, %0, %2 offen lds"
               ::"s"(r.srd[K >> 1]), "v"(r.vo[K & 1]), "s"(r.so[S]), "s"(r.dst[K]), "i"(slot_of(S)) : "memory", "scc");
}

#define SCH 3, 4, 4, 4, 3, 3, 3, 3, 3, 3, 3
constexpr int FEED_N[11] = {SCH};
constexpr int feed_done(int h) { int e = 12; for (int k = 0; k <= h && k < 11; k++) e += FEED_N[k]; return e; }
constexpr int feed_at(int h, int i) {
  if (h > 10) return -1;
  const int nn = FEED_N[h], e0 = feed_done(h) - nn;
  for (int k = 0; k < nn; k++) if ((h == 0 ? 14 - 4 * (nn - 1 - k) : nn == 4 ? 1 + 4 * k : 4 + 5 * k) == i) return e0 + k;
  return -1;
}
constexpr int feed_younger(int s) { return feed_done(2 * s) - 6 * (s + 2); }
constexpr bool feed_ok() {
  for (int s = 0; s < 6; s++) if (feed_younger(s) < 0 || feed_done(2 * s) > 6 * (s + 3)) return false;
  return feed_done(10) == 48 && FEED_N[0] <= 3;
}
static_assert(feed_ok(), "load schedule");
constexpr int wd_at(int h, int i) { for (int e = 0; e < 28; e++) if (11 + e / 6 == h && 1 + (e % 6) * 5 / 2 == i) return e; return -1; }

template <int ROLE>
DEV void gemm1(Ctx& c, const Params& P, G1Src gs, KIn kin, WdSrc& wds, f4 (&acc)[4][4], bf4 (&W1)[7][4][2], unsigned (&wa)[4], KExp& kx, u4 (&ur)[2], f4 (&cv)[4], f4 (&tc)[4]) {
  const int p = (c.xc & 3) * 4 + (c.jj & 3), ft = (c.xc >> 2) * 7 + (c.jj >> 2);
  const int lane = c.lane, n = c.n, q = c.q, v = c.v;
  gs.sx = mksrd(P.x, L * D * 2); gs.sg = mksrd(P.wg, D * F * 2); gs.su = mksrd(P.wu, D * F * 2);
  G1Rot rot = g1_rot(c, gs);
  g1_ld_w<0, 0>(rot);
  g1_m0(c, 0, 0); asm volatile("s_nop 0"); g1_ld(gs, 0, 0);
  if (ROLE == 0) g1_ld(gs, 0, 1); else __builtin_amdgcn_s_setprio(1);
  SB();
  KPre kp;
  kgen_pre<ROLE>(c, kp);
  unsigned xa[2][2], wr[3][2];
  xa[0][0] = (((ROLE ^ v) & 1) * 64 + n) * 128 + ((q ^ (n >> 1)) * 16); xa[0][1] = xa[0][0] ^ 64;
  wr[0][0] = 16384 + (8 * q + (n >> 2)) * 256 + (((2 * v) ^ ((n >> 2) | ((q & 1) << 2))) * 32) + (n & 3) * 8; wr[0][1] = wr[0][0] ^ 32;
  unsigned kbr = LKB + (c.w & 1) * 8672 + n * 544 + q * 16, xtv = (lane < 5 ? lane : 4) * 1024;
  asm volatile("" : "+v"(xa[0][0]), "+v"(xa[0][1]), "+v"(wr[0][0]), "+v"(wr[0][1]), "+v"(kbr), "+v"(rot.vo[1]));
  asm volatile("" : "+s"(rot.srd[1]), "+s"(rot.so[1]), "+s"(rot.dst[1]), "+s"(rot.dst[2]), "+s"(rot.dst[3]));
  unsigned lfv, cpv, l4, stv, csa, sta, one0, uv; u4 ones;
  if (ROLE == 0) {
    if (threadIdx.x < 12) *LDSP(unsigned, LSY + threadIdx.x * 4) = 0u;
    kexp_pre(c, kx); ktab_pre<0>(c, kp);
    lfv = q == 0 ? WS_FC + ((v * 14 + ft) * 16 + n) * 128 : WS_FLN + (q == 1 ? 16 * v + n : 4 * p + v) * 128;
    cpv = q * 1024 + n * 16; l4 = lane * 4; stv = l4 & 124; sta = LST + 512 * v + 4 * stv;
    csa = LCS + v * 256 + n * 16 + (((q & 1) << 1) | (q >> 1)) * 4;
    one0 = flag_off(lane); uv = u_off(c);
    ones = u4{0x3f803f80u, 0x3f803f80u, 0x3f803f80u, 0x3f803f80u};
    asm volatile("" : "+v"(lfv), "+v"(cpv), "+v"(l4), "+v"(stv), "+v"(sta), "+v"(csa), "+v"(one0), "+v"(ones), "+v"(uv));
  } else pin(xtv);
  kgen_take<ROLE, 3 - ROLE>(c, P, kin);
  bf8 KA[8], KB[8]; KTab kt;
  kgen_rows<ROLE>(c, kp, kin, KA, LAM(H) {
    constexpr int h = CV(H);
#define HK 0
    if constexpr (ROLE == 1 && h == 0) g1_ld(gs, 0, 1);
    if constexpr (ROLE == 1 && h == 1) touch(gs.sx, xtv, (p * 128 + (c.jj >> 2) * 18 + 5 * v) * 1024 + gs.k0 * 128 + 128);
    if constexpr (HK == 0) {
      if constexpr (h >= 2 && h < 5) g1_ld_w<0, h - 1>(rot);
      if constexpr (h == 5) g1_ld_w<1, 0>(rot);
    } else {
      if constexpr (h == 1) g1_ld_w<1, 0>(rot);
      if constexpr (h >= 3 && h < 6) g1_ld_w<0, h - 2>(rot);
    }
    if constexpr (h == 6) { g1_m0(c, 1, 0); asm volatile("s_nop 0"); g1_ld(gs, 1, 0); }
    if constexpr (h == 7) g1_ld(gs, 1, 1);
    SB();
  });
  if (ROLE == 1) { ktab_pre<1>(c, kp); __builtin_amdgcn_s_setprio(GPRIO); }
  u2 tagv = c.tag;
  if (ROLE == 0) { ktab_skip(c, kp, kt, kin.dsk); pin(tagv); }
  waitv<HK ? 2 : 3>();
  bar();
  STAMP(2);
  g1_ld_w<1, 1>(rot);
  Frags F0, Fb;
  auto rd = [&](Frags& Fr, int i, int s, int ks) __attribute__((always_inline)) {
    rd_piece(Fr, i, wr[s % 3][0], wr[s % 3][1], xa[s % 3 == 2][ks] + (s % 3 == 1 ? slot_of(1) : 0), ks);
  };
#pragma unroll
  for (int i = 0; i < 12; i++) rd(F0, i, 0, 0);
  SB();
  f4 cs[4];
  auto colsum = [&](const Frags& Fr, int rel) __attribute__((always_inline)) {
    if (ROLE == 0 && (c.jj & 3) == rel) {
#pragma unroll
      for (int t = 0; t < 4; t++) asm volatile("v_mfma_f32_16x16x32_bf16 %0, %1, %2, 0" : "+v"(cs[t]) : "v"(ones), "v"(cat(Fr.a[t][0], Fr.a[t][1])));
    }
    SB();
  };
  u2 lf; u4 cpr[4]; unsigned str; F1 cs4[4], tk[4], tr; U1 csm, t0, t1, ga, gb;
  auto feed = [&](auto H, auto I) __attribute__((always_inline)) {
    constexpr int h = CV(H), i = CV(I);
    constexpr int e = feed_at(h, i), en = feed_at(h, i + 1);
    if constexpr (e >= 0) g1_ld(gs, e / 6, e % 6);
    if constexpr (en >= 0 && !(en & 1)) g1_m0(c, en / 6, en % 6);
    constexpr int d = ROLE ? wd_at(h, i) : -1, dn = ROLE ? wd_at(h, i + 1) : -1;
    if constexpr (d >= 0) wd_ld(wds, d >> 2, d & 3);
    if constexpr (dn >= 0 && !(dn & 3)) set_m0(v * 4096, wd_base(dn >> 2));
  };
  sfor<0, 8>(LAM(S) {
    constexpr int s = CV(S);
    sfor<0, 16>(LAM(I) {
      constexpr int i = CV(I);
      if constexpr (s == 0) mma0(acc[i >> 2][i & 3], cat(F0.a[i & 3][0], F0.a[i & 3][1]), F0.b[i >> 2]);
      else mma(acc[i >> 2][i & 3], cat(F0.a[i & 3][0], F0.a[i & 3][1]), F0.b[i >> 2]);
      if constexpr (s == 0) {
        if constexpr (i == 0 || i == 2) g1_ld_w<1, 2 + (i >> 1)>(rot);
        if constexpr (i == 14 || i == 15) { wr[1][i & 1] = wr[0][i & 1] + slot_of(1); pin(wr[1][i & 1]); }
        if constexpr (i < 12) rd(Fb, i, s, 1);
        if constexpr (i >= 6 && i < 14) KB[i - 6] = *LDSP(bf8, kbr + (i - 6) * 64);
      } else if constexpr (i != 4 && i != 9 && i < 14) rd(Fb, i - (i > 4) - (i > 9), s, 1);
      feed(IC<2 * s>{}, I);
      if constexpr (s == 1 && (i == 2 || i == 3)) { wr[2][i & 1] = wr[0][i & 1] + SLOT_C; pin(wr[2][i & 1]); }
      if constexpr (s == 1 && (i == 6 || i == 7)) { xa[1][i & 1] = xa[0][i & 1] + SLOT_C; pin(xa[1][i & 1]); }
      if constexpr (ROLE == 1 && s == 3) {
        if constexpr (i == 0) { t0.v = lane >> 3; pin(t0.v); }
        if constexpr (i == 1) { t1.v = (lane ^ (t0.v & 6)) & 7; pin(t1.v); }
        if constexpr (i == 3) { t0.v = t0.v * 1024 + (448 * v * 1024 + TILE_C(c) * 128); pin(t0.v); }
        if constexpr (i == 5) { wds.voff[0] = t1.v * 16 + t0.v; pin(wds.voff[0]); wds.srd = mksrd(P.wd, F * D * 2); wds.par = TILE_P(c) * 32768; }
        if constexpr (i >= 6 && i < 9) { wds.voff[i - 5] = wds.voff[0] + (i - 5) * 7168; pin(wds.voff[i - 5]); }
      }
      if constexpr (ROLE == 1 && s == 4) {
        if constexpr (i == 0) { t0.v = lane >> 2; pin(t0.v); }
        if constexpr (i == 1) { t0.v = t0.v * 128 + (v * 4096 + SLOT_C); pin(t0.v); }
        if constexpr (i == 2) { t1.v = lane & 3; pin(t1.v); }
        if constexpr (i == 3) { t0.v = t1.v * 8 + t0.v; pin(t0.v); }
        if constexpr (i == 5) { t1.v = lane & 24; pin(t1.v); }
        if constexpr (i == 6) { wa[0] = t1.v * 4 + t0.v; pin(wa[0]); }
        if constexpr (i >= 10 && i < 13) { wa[i - 9] = wa[0] ^ ((i - 9) * 32); pin(wa[i - 9]); }
      }
      if constexpr (ROLE == 0 && s == 3) kexp_op<16 + i>(c, kx);
      if constexpr (ROLE == 0 && s == 2) {
        if constexpr (i == 5) { pin(cs[0]); pin(cs[1]); pin(cs[2]); pin(cs[3]); csm.v = __float_as_uint(cs[0][0]); }
        if constexpr (i >= 5 && i < 8) { csm.v = (unsigned)__builtin_amdgcn_update_dpp((int)csm.v, (int)__float_as_uint(cs[i - 4][0]), 0xe4, 1 << (i - 4), 0xf, false); pin(csm.v); }
        if constexpr (i == 15) __builtin_amdgcn_raw_buffer_store_b32(csm.v, c.ws, l4, WS_CPART + (ft * 16 + p) * 1024 + v * 256, SC1);
      }
      if constexpr (ROLE == 0) {
        if constexpr (s == 5 && i == 15) lf = flag_sample(P, lfv, 0);
        if constexpr (s == 6 && (i & 3) == 0) cpr[i >> 2] = __builtin_amdgcn_raw_buffer_load_b128(c.ws, cpv, WS_CPART + ft * 16384 + (i >> 2) * 4096 + v * 256, SC1);
        if constexpr (s == 7) {
          if constexpr (i < 4) { cs4[i].v += __uint_as_float(cpr[3][i]); pin(cs4[i].v); }
          if constexpr (i == 4) { cs4[0].v = swap32_add(cs4[0].v, cs4[1].v); pin(cs4[0].v); }
          if constexpr (i == 5) { cs4[2].v = swap32_add(cs4[2].v, cs4[3].v); pin(cs4[2].v); }
          if constexpr (i == 7) *LDSP(float, csa) = swap16_add(cs4[0].v, cs4[2].v);
          if constexpr (i == 9) *LDSP(f4, sta) = f4{tk[0].v, tk[1].v, tk[2].v, tk[3].v};
        }
        if constexpr (s == 6 && i == 2) str = __builtin_amdgcn_raw_buffer_load_b32(c.ws, stv, WS_STATS + (p * 128 + 32 * v) * 4, SC1);
      }
      SB();
    });
    if constexpr (s == 0 && ROLE == 0) { asm volatile("" : "=v"(cs[0]), "=v"(cs[1]), "=v"(cs[2]), "=v"(cs[3])); }
    if constexpr (s < 2) colsum(F0, 2 * s);
    if constexpr (s == 3 && ROLE == 0) waitv<(feed_younger(s) < FEED_N[5] + FEED_N[6] ? feed_younger(s) : FEED_N[5] + FEED_N[6])>();
    else if constexpr (s < 6) waitv<feed_younger(s) + (ROLE == 0 && (s == 2 || s == 5))>();
    else if constexpr (ROLE == 1) waitv<12>();
    bar();
    if constexpr (s == 0) { STAMP(3); }
    if constexpr (s == 2) { STAMP(4); }
    if constexpr (s == 5) { STAMP(5); }
    if constexpr (s == 6) { STAMP(6); }
    if constexpr (s == 7) { STAMP(1); }
    SB();
#define PRI 0x60
    if constexpr (ROLE == 1) __builtin_amdgcn_s_setprio((PRI >> s) & 1);
    sfor<0, 16>(LAM(I) {
      constexpr int i = CV(I);
      mma(acc[i >> 2][i & 3], cat(Fb.a[i & 3][0], Fb.a[i & 3][1]), Fb.b[i >> 2]);
      if constexpr (s < 7 && i != 4 && i != 9 && i < 14) rd(F0, i - (i > 4) - (i > 9), s + 1, 0);
      feed(IC<2 * s + 1>{}, I);
      if constexpr (s == 0 && i == 0) mma0(kt.t, KA[0], KB[0]);
      if constexpr (s == 0 && i > 0 && !(i & 1)) mma(kt.t, KA[i >> 1], KB[i >> 1]);
      if constexpr (s == 1 && i < KT_OPS) ktab_op<ROLE, i>(c, kp, kt);
      if constexpr (ROLE == 0 && s == 3 && i == 0) __builtin_amdgcn_raw_buffer_store_b64(tagv, c.ws, one0, WS_FC + ((v * 14 + ft) * 16 + p) * 128, SC1);
      if constexpr (ROLE == 0 && (s == 2 || s == 3) && 32 * (s - 2) + i < KX_OPS) kexp_op<32 * (s - 2) + i>(c, kx);
      if constexpr (s == 4 && i == 0) { ga.v = q * 16 + (LCS + v * 256); pin(ga.v); }
      if constexpr (s == 4 && i == 2) { gb.v = n * 16 + (LST + ((ROLE ^ v) & 1) * 1024); pin(gb.v); }
      if constexpr (s == 7 && (i & 1)) { if constexpr (i & 2) tc[i >> 2] = *LDSP(f4, gb.v + (i >> 2) * 256); else cv[i >> 2] = *LDSP(f4, ga.v + (i >> 2) * 64); }
      if constexpr (ROLE == 1 && s == 7) W1[i >> 3][(i >> 1) & 3][i & 1] = trd(wa[(i >> 1) & 3] + (i >> 3) * 16384 + (i & 1) * 2048);
      if constexpr (ROLE == 0) {
        if constexpr (s == 5 && i == 15) {
          asm volatile("s_waitcnt vmcnt(0)" : "+v"(lf)::"memory");
          if (!flags_up(c.tag, lf)) wait_flags<4>(c.ws, c.tag, ld_flag(c.ws, lfv, 0), lfv, 0);
        }
        if constexpr (s == 7 && (i == 0 || i == 2)) ur[i >> 1] = u_load(c, uv, i >> 1);
        if constexpr (s == 6 && i < 8) {
          if constexpr (i == 0) { tk[3].v = bhi(str); pin(tk[3].v); }
          if constexpr (i == 1) { tr.v = __builtin_amdgcn_rcpf(tk[3].v); pin(tr.v); }
          if constexpr (i == 2) { tk[0].v = blo(str); pin(tk[0].v); }
          if constexpr (i == 3) { tk[1].v = (-2.f * 0.7978845608028654f * 1.4426950408889634f) * tr.v; pin(tk[1].v); }
          if constexpr (i == 4) { tr.v *= tr.v; pin(tr.v); }
          if constexpr (i == 5) { tk[3].v *= tk[3].v; pin(tk[3].v); }
          if constexpr (i == 6) { tr.v *= 0.044715f; pin(tr.v); }
          if constexpr (i == 7) { tk[2].v = tr.v * tk[1].v; pin(tk[2].v); }
        }
        if constexpr (s == 6 && i >= 8) { constexpr int e = i & 3, k = (i - 8) >> 2; cs4[e].v = (k ? cs4[e].v : __uint_as_float(cpr[0][e])) + __uint_as_float(cpr[k + 1][e]); pin(cs4[e].v); }
      }
      SB();
    });
    if constexpr (s < 2) colsum(Fb, 2 * s + 1);
  });
  if (ROLE == 1) __builtin_amdgcn_s_setprio(0);
  STAMP(7);
}

template <int ROLE>
DEV void geglu(const Ctx& c, f4 (&acc)[4][4], const f4 (&cv)[4], const f4 (&tc)[4]) {
  const int p = (c.xc & 3) * 4 + (c.jj & 3), ft = (c.xc >> 2) * 7 + (c.jj >> 2);
  const unsigned hbase = WS_H + uni((2 * p + ((ROLE ^ c.v) & 1)) * 56 + 4 * ft + c.v) * 4096;
#pragma unroll
  for (int i = 0; i < 16; i++) pin(acc[i >> 2][i & 3]);
#pragma unroll
  for (int tt = 0; tt < 4; tt++) {
    const f2 nm = -f2{tc[tt][0], tc[tt][0]}, k0 = {tc[tt][1], tc[tt][1]}, k1 = {tc[tt][2], tc[tt][2]}, s2 = {tc[tt][3], tc[tt][3]};
    f2 a[4], b[4], z[4];
    auto half = [](const f4& x, int e) { return f2{x[2 * e], x[2 * e + 1]}; };
#define STEP(X) _Pragma("unroll") for (int j = 0; j < 4; j++) { X; } SB();
    STEP(a[j] = fma2(nm, half(cv[j >> 1], j & 1), half(acc[tt][j >> 1], j & 1)))
    STEP(z[j] = a[j] * a[j])
    STEP(z[j] = fma2(z[j], k1, k0))
    STEP(z[j] *= a[j])
    STEP(z[j] = (f2{__builtin_amdgcn_exp2f(z[j].x), __builtin_amdgcn_exp2f(z[j].y)}))
    STEP(b[j] = fma2(nm, half(cv[2 + (j >> 1)], j & 1), half(acc[tt][2 + (j >> 1)], j & 1)))
    STEP(z[j] = fma2(z[j], s2, s2))
    STEP(z[j] = (f2{__builtin_amdgcn_rcpf(z[j].x), __builtin_amdgcn_rcpf(z[j].y)}))
    STEP(a[j] *= b[j])
    STEP(a[j] *= z[j])
#undef STEP
    __builtin_amdgcn_raw_buffer_store_b128(u4{pk(a[0]), pk(a[1]), pk(a[2]), pk(a[3])}, c.ws, c.lane * 16 + tt * 1024, hbase, SC1);
    SB();
  }
}
template <int ROLE, int YOUNGER> DEV void h_publish(const Ctx& c) {
  const int p = (c.xc & 3) * 4 + (c.jj & 3), ft = (c.xc >> 2) * 7 + (c.jj >> 2);
  waitv<YOUNGER>();
  st_flag(c.ws, c.tag, WS_FH + uni((2 * p + ((ROLE ^ c.v) & 1)) * 56 + 4 * ft + c.v) * 128, c.lane);
}

DEV void ln_block(Ctx& c, const Params& P, KIn kin) {
  const int tok0 = (c.xc & 3) * 512 + (c.xc >> 2) * 256 + (c.jj - 28) * 64;
  const i4 sx = mksrd(P.x, L * D * 2);
  u4 rows[8];
#pragma unroll
  for (int i = 0; i < 8; i++) rows[i] = row_load(sx, c.lane * 16, (tok0 + 32 * (i >> 2) + 4 * c.w + (i & 3)) * 1024);
  if (threadIdx.x < 12) *LDSP(unsigned, LSY + threadIdx.x * 4) = 0u;
  kgen_take<0, 8>(c, P, kin);
  asm volatile("s_waitcnt vmcnt(4)" : "+v"(rows[0]), "+v"(rows[1]), "+v"(rows[2]), "+v"(rows[3])::"memory");
  STAMP(1);
  ln_compute(c, rows, 0, tok0);
  asm volatile("s_waitcnt vmcnt(0)" : "+v"(rows[4]), "+v"(rows[5]), "+v"(rows[6]), "+v"(rows[7])::"memory");
  bar();
  ln_store(c, 0, tok0);
  STAMP(2);
  const i4 swd = mksrd(P.wd, F * D * 2);
  const unsigned wq = (c.jj - 28) * 448 * 1024;
  auto walk = [&](int k) __attribute__((always_inline)) {
    touch(swd, c.lane * 128, wq + (1 - TILE_P(c)) * 32768 + ((k + (c.xc >> 1)) & 3) * 65536 + c.v * 8192);
  };
  if (c.w >= 4) walk(0);
  ln_compute(c, rows + 4, 34816, tok0 + 32);
  if (c.w >= 4) waitv<1>(); else waitv<0>();
  bar();
  if (c.w == 0) st_flag(c.ws, c.tag, WS_FLN + (tok0 >> 5) * 128, c.lane);
  ln_store(c, 34816, tok0 + 32);
  STAMP(3);
  if (c.w >= 5) { touch(swd, c.lane * 128, wq + 262144 + (c.xc * 3 + c.w - 5) * 8192); waitv<1>(); } else waitv<0>();
  bar();
  if (c.w == 0) st_flag(c.ws, c.tag, WS_FLN + ((tok0 >> 5) + 1) * 128, c.lane);
  STAMP(4);
  bf8 KA[8], KB[8]; KTab kt;
  KPre kp;
  auto warm = LAM(H) { constexpr int h = CV(H); if constexpr (h == 0 || h == 3 || h == 6) { if ((c.w >> 2) == (h & 1)) walk(1 + h / 3); } };
  if (c.w < 4) { kgen_pre<0>(c, kp); kgen_rows<0>(c, kp, kin, KA, warm); } else { kgen_pre<1>(c, kp); kgen_rows<1>(c, kp, kin, KA, warm); }
  bar();
  kgen_cols(c, KB); kgen_mma(kt.t, KA, KB); mma_fence();
  if (c.w < 4) { ktab_pre<0>(c, kp); ktab_skip(c, kp, kt, kin.dsk); sfor<0, KT_OPS>(LAM(K) { ktab_op<0, CV(K)>(c, kp, kt); }); }
  else { ktab_pre<1>(c, kp); sfor<0, KT_OPS>(LAM(K) { ktab_op<1, CV(K)>(c, kp, kt); }); }
  bar();
  STAMP(5);
  asm volatile("fused_end_a:");
}

struct CFr { bf4 a[8][2]; bf8 b[8]; };
DEV void conv_rd(CFr& Fr, int i, unsigned ka, unsigned ua, int sp) {
  if (i < 2) Fr.a[0][i] = trd(ka + 64 * sp + (i ? 0 : 8));
  else if (i < 10) Fr.b[i - 2] = *LDSP(bf8, ua + 512 * (i - 2) + 192 - 64 * sp);
  else { const int g = (i - 8) >> 1, t = i & 1; Fr.a[g][t] = trd(ka + 512 * g + 64 * sp + (t ? 0 : 8)); }
}
DEV void conv(Ctx& c) {
  const int chl = c.v & 1, kh = c.v >> 1, n = c.n, q = c.q;
  unsigned ka = LKC + chl * KCH + (n >> 2) * KCS + 2 * (16 + 4 * (n & 3) + 128 * kh + 16 - 8 * q) - 8;
  unsigned ua = LU + chl * UCH + 2 * (256 + 16 * n - 16 - 128 * kh + 8 * q) - 192;
  asm volatile("" : "+v"(ka), "+v"(ua));
  f4 acc[8];
  CFr Fr[2];
#pragma unroll
  for (int i = 0; i < 24; i++) conv_rd(Fr[0], i, ka, ua, 0);
  unsigned xw = LKC + chl * KCH + 32 + (c.lane >> 3) * 512 + (c.lane & 7) * 16, yo = (q & 1) * 512 + n * 32 + (q >> 1) * 16;
  asm volatile("" : "+v"(xw), "+v"(yo));
  lds_wait<1>(LSY + 44, 4);
  STAMP(11);
  SB();
#pragma unroll
  for (int sp = 0; sp < 4; sp++) {
    int i = 0;
#pragma unroll
    for (int g = 0; g < 8; g++)
#pragma unroll
      for (int m = 0; m + g < 8; m++) {
        if (sp == 0 && g == 0) mma0(acc[m], cat(Fr[0].a[0][0], Fr[0].a[0][1]), Fr[0].b[m]);
        else mma(acc[g + m], cat(Fr[sp & 1].a[g][0], Fr[sp & 1].a[g][1]), Fr[sp & 1].b[m]);
        if (sp < 3 && i < 24) conv_rd(Fr[(sp + 1) & 1], i, ka, ua, sp + 1);
        i++;
        SB();
      }
  }
  mma_fence();
#pragma unroll
  for (int i = 0; i < 8; i++) asm volatile("" : "+v"(acc[i]));
  unsigned pkd[4][2];
  auto finish = [&](auto KH) __attribute__((always_inline)) {
    constexpr int h = CV(KH);
#pragma unroll
    for (int j = 0; j < 4; j++) *LDSP(f4, xw + h * 256 + j * KCS) = acc[4 * (1 - h) + j];
    lds_post(LSY + 16 + c.v * 4, c.lane);
    STAMP(12);
    lds_wait<0>(LSY + 16 + (c.v ^ 2) * 4, 1);
#pragma unroll
    for (int j = 0; j < 4; j++) { const f4 t = acc[4 * h + j] + *LDSP(f4, xw + (1 - h) * 256 + j * KCS); pkd[j][0] = pk(f2{t[0], t[1]}); pkd[j][1] = pk(f2{t[2], t[3]}); }
  };
  if (kh) finish(IC<1>{}); else finish(IC<0>{});
  const int ch = 2 * (c.xc * 32 + c.jj) + chl;
#pragma unroll
  for (int j = 0; j < 4; j += 2) {
    swap16(pkd[j][0], pkd[j + 1][0]); swap16(pkd[j][1], pkd[j + 1][1]);
    __builtin_amdgcn_raw_buffer_store_b128(u4{pkd[j][0], pkd[j][1], pkd[j + 1][0], pkd[j + 1][1]}, c.ws,
                                           yo + j * 512, WS_YT + ch * 4096 + kh * 2048, SC1);
  }
  STAMP(13);
  waitv<0>();
  STAMP(14);
  st_flag(c.ws, c.tag, WS_FY + (kh * 512 + ch) * 128, c.lane);
}
DEV u4 x_load(const Ctx& c, const Params& P, int role, int l) {
  return __builtin_amdgcn_raw_buffer_load_b128(mkrs(P.x, L * D * 2), (l >> 3) * 1024 + (l & 7) * 16,
                                               (TILE_T(c) * 64 + 16 * c.v + 8 * role) * 1024 + TILE_C(c) * 128, 0);
}
DEV void y_gather(Ctx& c) {
  const int T64 = TILE_T(c), ct = TILE_C(c), l = c.lane;
  const int r0 = 32 * (c.v & 1) + 16 * (c.v >> 1);
  const unsigned fo = WS_FY + ((T64 >> 4) * 512 + 64 * ct + r0) * 128;
  unsigned fv = (l < 16 ? l : 15) * 128, yv = (l >> 3) * 4096 + (l & 7) * 16, ya = LU + (c.v & 1) * (UCH + 64) + (c.v >> 1) * 2080 + l * 16;
  asm volatile("" : "+v"(fv), "+v"(yv), "+v"(ya));
  lds_wait<2>(LSY + 40, 1);
  wait_flags<8>(c.ws, c.tag, ld_flag(c.ws, fv, fo), fv, fo);
  STAMP(15);
#pragma unroll
  for (int i = 0; i < 2; i++)
    *LDSP(u4, ya + i * 1040) = __builtin_amdgcn_raw_buffer_load_b128(c.ws, yv, WS_YT + (64 * ct + r0 + 8 * i) * 4096 + T64 * 128, SC1);
}
struct Fin { unsigned a0, a1, ya, oa; f4 a, b, y0, y1; };
template <int ROLE> DEV Fin finish_pre(const Ctx& c, u4 x, int l) {
  const int t = l >> 3, c8 = l & 7;
  Fin f;
  f.a0 = c.v * 16384 + (8 * ROLE + t) * 256 + ((2 * c8) ^ t) * 16; f.a1 = f.a0 ^ 16;
  f.ya = LU + (c8 >> 2) * (UCH + 64) + (c8 & 3) * 1040 + (16 * c.v + 8 * ROLE + t) * 2;
  f.oa = t * 1024 + c8 * 16;
  f.a = f4{blo(x[0]), bhi(x[0]), blo(x[1]), bhi(x[1])}; f.b = f4{blo(x[2]), bhi(x[2]), blo(x[3]), bhi(x[3])};
  f.y0 = f4{0.f, 0.f, 0.f, 0.f}; f.y1 = f.y0;
  asm volatile("" : "+v"(f.a0), "+v"(f.a1), "+v"(f.ya), "+v"(f.oa), "+v"(f.a), "+v"(f.b), "+v"(f.y0), "+v"(f.y1));
  return f;
}
template <int ROLE> DEV void finish_tile(Ctx& c, const Params& P, Fin f) {
  const int T64 = TILE_T(c), ct = TILE_C(c);
  const rsrc_t ro = mkrs(P.out, L * D * 2);
  bar();
  if (ROLE == 1) { STAMP(15); }
  float y[8] = {f.y0[0], f.y0[1], f.y0[2], f.y0[3], f.y1[0], f.y1[1], f.y1[2], f.y1[3]};
  asm volatile("ds_read_u16_d16_hi %0, %8\n\tds_read_u16_d16_hi %1, %8 offset:128\n\tds_read_u16_d16_hi %2, %8 offset:256\n\tds_read_u16_d16_hi %3, %8 offset:384\n\t"
               "ds_read_u16_d16_hi %4, %8 offset:512\n\tds_read_u16_d16_hi %5, %8 offset:640\n\tds_read_u16_d16_hi %6, %8 offset:768\n\tds_read_u16_d16_hi %7, %8 offset:896"
               : "+v"(y[0]), "+v"(y[1]), "+v"(y[2]), "+v"(y[3]), "+v"(y[4]), "+v"(y[5]), "+v"(y[6]), "+v"(y[7]) : "v"(f.ya) : "memory");
  f4 pa[4], pb[4];
#pragma unroll
  for (int k = 0; k < 4; k++) { pa[k] = *LDSP(f4, f.a0 + k * 4096); pb[k] = *LDSP(f4, f.a1 + k * 4096); }
  SB();
#pragma unroll
  for (int k = 0; k < 4; k++) { f.a += pa[k]; f.b += pb[k]; }
  asm volatile("s_waitcnt lgkmcnt(0)" : "+v"(y[0]), "+v"(y[1]), "+v"(y[2]), "+v"(y[3]), "+v"(y[4]), "+v"(y[5]), "+v"(y[6]), "+v"(y[7])::"memory");
  f.a += f4{y[0], y[1], y[2], y[3]}; f.b += f4{y[4], y[5], y[6], y[7]};
  __builtin_amdgcn_raw_buffer_store_b128(u4{pk(f2{f.a[0], f.a[1]}), pk(f2{f.a[2], f.a[3]}), pk(f2{f.b[0], f.b[1]}), pk(f2{f.b[2], f.b[3]})}, ro, f.oa,
                                         (T64 * 64 + 16 * c.v + 8 * ROLE) * 1024 + ct * 128, SC1);
}

DEV u2 hflags_sample(const Ctx& c, const Params& P) {
  return flag_sample(P, (TILE_T(c) * 56 + 14 * c.v + TILE_P(c) + 2 * (c.lane < 7 ? c.lane : 6)) * 128, WS_FH);
}
DEV void gemm2(Ctx& c, const Params& P, const bf4 (&W1)[7][4][2], u2 hfl) {
  const int T64 = TILE_T(c), v = c.v;
  int n, q;
  unsigned l16 = c.lane * 16;
  asm volatile("" : "+v"(l16));
  const unsigned hk = uni(T64 * 56 + 14 * v + TILE_P(c));
  const unsigned fo = WS_FH + hk * 128, fo2 = WS_FH + (hk ^ 1) * 128, fv = (l16 < 112 ? l16 : 96) * 16;
  if (!flags_up(c.tag, hfl)) wait_flags<2>(c.ws, c.tag, ld_flag(c.ws, fv, fo), fv, fo);
  STAMP(12);
#define GWIN 3
  constexpr int WIN = GWIN;
  u4 H[14][4];
  auto hload = [&](int i, int t) __attribute__((always_inline)) {
    H[i][t] = __builtin_amdgcn_raw_buffer_load_b128(c.ws, l16 + t * 1024, WS_H + ((i < 7 ? hk : hk ^ 1) + 2 * (i % 7)) * 4096, SC1);
  };
#pragma unroll
  for (int i = 0; i < WIN; i++)
#pragma unroll
    for (int t = 0; t < 4; t++) hload(i, t);
  SB();
  f4 acc[4][4];
  bf4 W2[7][4][2];
  unsigned wa[4], pw[4]; u4 xr; Fin fin;
  sfor<0, 14>(LAM(I) {
    constexpr int i = CV(I);
    if constexpr (i == 5) {
      asm volatile("" : "+v"(l16));
      n = (l16 >> 4) & 15; q = l16 >> 8;
#pragma unroll
      for (int d = 0; d < 4; d++) { wa[d] = (wd_raddr(v, n, q) ^ (d * 32)) + SLOT_C; pin(wa[d]); }
      SB();
    }
    if constexpr (i == 9) {
#pragma unroll
      for (int d = 0; d < 4; d++) { wa[d] -= SLOT_C; pin(wa[d]); }
    }
    sfor<0, 16>(LAM(J) {
      constexpr int j = CV(J), t = j >> 2, d = j & 3;
      if constexpr (i == 0) mma0(acc[t][d], cat(W1[0][d][0], W1[0][d][1]), H[0][t]);
      else if constexpr (i < 7) mma(acc[t][d], cat(W1[i][d][0], W1[i][d][1]), H[i][t]);
      else mma(acc[t][d], cat(W2[i - 7][d][0], W2[i - 7][d][1]), H[i][t]);
#define HS2 17
      if constexpr (i * 16 + j == HS2) hfl = ld_flag(c.ws, fv, fo2);
      if constexpr (i == 7 - WIN && j == 2) { if (!flags_up(c.tag, hfl)) wait_flags<2>(c.ws, c.tag, ld_flag(c.ws, fv, fo2), fv, fo2); STAMP(16); }
      if constexpr (i + WIN < 14 && (j & 3) == 3) hload(i + WIN, t);
      if constexpr (i >= 6 && i < 13 && (j & 1) == 0) W2[i - 6][j >> 2][(j >> 1) & 1] = trd(wa[j >> 2] + (i < 9 ? i - 6 : i - 9) * 16384 + ((j >> 1) & 1) * 2048);
#define YSIG 10
      if constexpr (i == YSIG && j == 14) *LDSP(volatile unsigned, LSY + 40) = 1u;
      if constexpr (i == 6 && j == 1) xr = x_load(c, P, 1, l16 >> 4);
      if constexpr (i == 10 && j == 15) fin = finish_pre<1>(c, xr, l16 >> 4);
      if constexpr (i == 11 && (j & 3) == 3) { pw[t] = (v * 4096 + n * 256 + (q ^ (n & 7)) * 16) ^ (t * 64); pin(pw[t]); }
      if constexpr (i == 13 && j >= 8) { pin(acc[t - 2][d]); *LDSP(f4, pw[d] + (t - 2) * 16384) = acc[t - 2][d]; }
      SB();
    });
  });
  STAMP(13);
  asm volatile("s_nop 1");
#pragma unroll
  for (int j = 8; j < 16; j++) { pin(acc[j >> 2][j & 3]); *LDSP(f4, pw[j & 3] + (j >> 2) * 16384) = acc[j >> 2][j & 3]; if (j == 11) asm volatile("s_nop 3"); SB(); }
  STAMP(14);
  finish_tile<1>(c, P, fin);
}

__global__ void __launch_bounds__(512, 2) fused_s4_geglu(Params P) {
  Ctx c;
  STAMP(0);
  c.w = __builtin_amdgcn_readfirstlane(threadIdx.x >> 6);
  c.v = c.w & 3; c.lane = threadIdx.x & 63; c.n = c.lane & 15; c.q = c.lane >> 4;
  c.xc = blockIdx.x & 7; c.jj = blockIdx.x >> 3;
  {
    u64 pc; unsigned len;
    asm volatile("s_getpc_b64 %0\n.Lpc%=:\n\ts_mov_b32 %1, fused_end_a-.Lpc%=\n\ts_max_u32 %1, %1, fused_end_b-.Lpc%=" : "=s"(pc), "=s"(len)::"scc");
    if (c.w == 3) touch(mksrd((const void*)pc, len), (c.lane >> CPF) * 128, (c.jj & ((8 << CPF) - 1)) * (8192 >> CPF));
  }
  const bool g1 = c.jj < 28;
  const G1Src gs = g1_src(c);
  const KIn kin = kgen_load<0>(c, P);
  if (__builtin_expect(!g1, 0)) ln_block(c, P, kin);
  if (c.w >= 4) {
    WdSrc wds;
    f4 acc[4][4], cv[4], tc[4]; bf4 W1[7][4][2]; unsigned wa[4];
    if (__builtin_expect(g1, 1)) { KExp kx; u4 ur[2]; gemm1<1>(c, P, gs, kin, wds, acc, W1, wa, kx, ur, cv, tc); }
    else {
      wds = wd_src(c, P);
      lds_arrive(LSY + 44, c.lane);
#pragma unroll
      for (int i = 0; i < 7; i++) wd_chunk(c, wds, i);
#pragma unroll
      for (int d = 0; d < 4; d++) { wa[d] = (wd_raddr(c.v, c.n, c.q) ^ (d * 32)) + SLOT_C; pin(wa[d]); }
      waitv<16>();
#pragma unroll
      for (int i = 0; i < 16; i++) W1[i >> 3][(i >> 1) & 3][i & 1] = trd(wa[(i >> 1) & 3] + (i >> 3) * 16384 + (i & 1) * 2048);
      waitl();
    }
#define NPRE 2
#define NPUB 2
    auto w1_read = [&](int i) __attribute__((always_inline)) {
      if (i == 3) {
#pragma unroll
        for (int d = 0; d < 4; d++) { wa[d] -= SLOT_C; pin(wa[d]); }
      }
#pragma unroll
      for (int k = 0; k < 8; k++) W1[i][k >> 1][k & 1] = trd(wa[k >> 1] + (i < 3 ? i : i - 3) * 16384 + (k & 1) * 2048);
      SB();
    };
#pragma unroll
    for (int i = 2; i < NPRE; i++) { if (i == 3) waitv<4>(); if (i == 6) waitv<0>(); w1_read(i); }
    STAMP(8);
    if (g1) { geglu<1>(c, acc, cv, tc); waitv<4>(); } else waitv<0>();
    lds_arrive(LSY + 44, c.lane);
    STAMP(9);
    u2 hfl;
#pragma unroll
    for (int i = 0; i < 7; i++) {
      if (i + 2 >= NPRE && i + 2 < 7) w1_read(i + 2);
      if (i >= NPRE) { if (i < 5) asm volatile("s_waitcnt lgkmcnt(15)" ::: "memory"); else if (i == 5) asm volatile("s_waitcnt lgkmcnt(8)" ::: "memory"); else waitl(); }
      if (i == 5) {
        if (!g1) lds_wait<4>(LSY + 16, 1);
        hfl = hflags_sample(c, P);
      }
      wd_chunk(c, wds, 7 + i);
      if (i == NPUB - 1) { if (g1) h_publish<1, 4 * NPUB>(c); STAMP(10); }
      SB();
    }
    asm volatile("s_waitcnt vmcnt(8)" : "+v"(hfl)::"memory");
    STAMP(11);
    gemm2(c, P, W1, hfl);
    asm volatile("fused_end_b:");
  } else {
    if (__builtin_expect(g1, 1)) {
      f4 acc[4][4], cv[4], tc[4]; WdSrc none; bf4 W1[7][4][2]; unsigned wa[4]; KExp kx; u4 ur[2];
      gemm1<0>(c, P, gs, kin, none, acc, W1, wa, kx, ur, cv, tc);
      geglu<0>(c, acc, cv, tc);
      STAMP(8);
      sfor<0, 9>(LAM(K) { kexp_put<CV(K)>(c, kx); });
      u_write(c, ur[0], ur[1]);
    } else {
      const unsigned fv = (16 * c.v + (c.lane >> 2)) * 128;
      wait_flags<8>(c.ws, c.tag, ld_flag(c.ws, fv, WS_FLN), fv, WS_FLN);
      const u4 r0 = u_load(c, u_off(c), 0), r1 = u_load(c, u_off(c), 1);
      KExp kx;
      kexp_pre(c, kx);
      sfor<0, KX_OPS>(LAM(K) { kexp_op<CV(K)>(c, kx); });
      sfor<0, 9>(LAM(K) { kexp_put<CV(K)>(c, kx); });
      u_write(c, r0, r1);
    }
    lds_arrive(LSY, c.lane);
    STAMP(9);
    if (g1) h_publish<0, 0>(c);
    STAMP(10);
    lds_wait<1>(LSY, 4);
    const u4 xr = x_load(c, P, 0, c.lane);
    conv(c);
    if (__builtin_expect(blockIdx.x == 224 && c.w == 0, 0)) {
#pragma unroll 1
      for (int i = 0; i < 4; i++) {
        const int b = 64 * i + c.lane;
        const unsigned fv = b < 224 ? WS_FC + b * 128 : WS_FLN + (b - 224) * 256;
        wait_flags<16>(c.ws, c.tag, ld_flag(c.ws, fv, 0), fv, 0);
      }
      st_flag(c.ws, c.tag, WS_EPOCH, c.lane);
    }
    const Fin fin = finish_pre<0>(c, xr, c.lane);
    y_gather(c);
    finish_tile<0>(c, P, fin);
  }
  FINISH();
}

extern "C" void kernel_launch(void* const* d_in, const int* in_sizes, int n_in, void* d_out, int out_size, void* d_ws, size_t ws_size,
                              hipStream_t stream) {
  Params P;
  P.x = (const u16*)d_in[0]; P.are = (const u16*)d_in[1]; P.aim = (const u16*)d_in[2]; P.bre = (const u16*)d_in[3];
  P.bim = (const u16*)d_in[4]; P.cre = (const u16*)d_in[5]; P.cim = (const u16*)d_in[6]; P.ldt = (const u16*)d_in[7];
  P.dsk = (const u16*)d_in[8]; P.wg = (const u16*)d_in[10]; P.wu = (const u16*)d_in[11]; P.wd = (const u16*)d_in[12];
  P.out = (u16*)d_out; P.ws = (char*)d_ws;
  (void)hipFuncSetAttribute((const void*)fused_s4_geglu, hipFuncAttributeMaxDynamicSharedMemorySize, LDS_BYTES);
  fused_s4_geglu<<<256, 512, LDS_BYTES, stream>>>(P);
}
```

```cpp
#include <hip/hip_runtime.h>
#include <hip/hip_bf16.h>
#include <stdint.h>
#pragma clang diagnostic ignored "-Wint-to-pointer-cast"

typedef unsigned long long u64;
typedef unsigned short u16;
typedef __bf16 bf2 __attribute__((ext_vector_type(2)));
typedef __bf16 bf4 __attribute__((ext_vector_type(4)));
typedef __bf16 bf8 __attribute__((ext_vector_type(8)));
typedef float f2 __attribute__((ext_vector_type(2)));
typedef float f4 __attribute__((ext_vector_type(4)));
typedef int i4 __attribute__((ext_vector_type(4)));
typedef unsigned u2 __attribute__((ext_vector_type(2)));
typedef unsigned u4 __attribute__((ext_vector_type(4)));
typedef __amdgpu_buffer_rsrc_t rsrc_t;
#define LDSP(T, a) ((__attribute__((address_space(3))) T*)(a))
#define DEV __device__ __forceinline__
#define SB() __builtin_amdgcn_sched_barrier(0)
#define SC1 16

template <int V> struct IC { static constexpr int value = V; };
template <int B, int E, class Fn> DEV void sfor(Fn&& f) { if constexpr (B < E) { f(IC<B>{}); sfor<B + 1, E>(f); } }
#define LAM(I) [&](auto I) __attribute__((always_inline))
#define CV(I) decltype(I)::value

constexpr int L = 2048, D = 512, NS = 128, F = 1792;

constexpr unsigned WS_EPOCH = 0;
constexpr unsigned WS_FLN = 8192;
constexpr unsigned WS_FG = 4096;
constexpr unsigned WS_STATS = 16384;
constexpr unsigned WS_CPART = 65536;
constexpr unsigned WS_FC = 327680;
constexpr unsigned WS_FH = 524288;
constexpr unsigned WS_FY = 786432;
constexpr unsigned WS_XNT = 1u << 20;
constexpr unsigned WS_YT = 3u << 20;
constexpr unsigned WS_H = 5u << 20;
constexpr unsigned WS_BYTES = WS_H + 32u * 56u * 4096u;
constexpr unsigned WS_DBG = 16u << 20;

constexpr unsigned SLOT_C = 98816;
constexpr unsigned slot_of(int s) { return s % 3 == 0 ? 0u : s % 3 == 1 ? 49152u : SLOT_C; }
constexpr unsigned wd_base(int i) { return i < 3 ? SLOT_C + i * 16384 : (i - 3) * 16384; }
constexpr unsigned LKC = 65536;
constexpr unsigned KCS = 4160, KCH = 4 * KCS;
constexpr unsigned LKG = 98304;
constexpr unsigned LU = 147968;
constexpr unsigned UCH = 4608;
constexpr unsigned LCS = LU + 9216;
constexpr unsigned LST = LU + 10240;
constexpr unsigned LKT = 160064;
constexpr unsigned LSY = 163136;
constexpr unsigned LJK = 163328;
constexpr unsigned LDS_BYTES = 163840;

struct Params {
  const u16 *are, *aim, *ldt, *dsk, *bre, *bim, *cre, *cim;
  char* ws;
  const u16 *x, *wg, *wu, *wd;
  u16* out;
};

DEV i4 mksrd(const void* p, unsigned bytes) {
  u64 a = (u64)p; i4 r; r[0] = (int)(unsigned)a; r[1] = (int)(unsigned)(a >> 32); r[2] = (int)bytes; r[3] = 0x00020000; return r;
}
DEV rsrc_t mkrs(const void* p, unsigned bytes) { return __builtin_amdgcn_make_buffer_rsrc((void*)p, 0, (int)bytes, 0x00020000); }
DEV void glds(i4 srd, unsigned voff, unsigned soff, unsigned dst) {
  asm volatile("s_mov_b32 m0, %3\n\ts_nop 0\n\tbuffer_load_dwordx4 %1, %0, %2 offen lds" ::"s"(srd), "v"(voff), "s"(soff), "s"(dst) : "memory");
}
DEV void touch(i4 srd, unsigned voff, unsigned soff) {
  asm volatile("s_mov_b32 m0, %3\n\ts_nop 0\n\tbuffer_load_dword %1, %0, %2 offen lds" ::"s"(srd), "v"(voff), "s"(soff), "s"(LJK) : "memory");
}
DEV void st128(u4 v, rsrc_t rs, unsigned voff, unsigned soff) {
  __builtin_amdgcn_raw_buffer_store_b128(v, rs, voff, soff, SC1);
  asm volatile("s_nop 1" ::"v"(v) : "memory");
}
template <int N> DEV void waitv() { asm volatile("s_waitcnt vmcnt(%0)" ::"i"(N) : "memory"); }
DEV void waitl() { __builtin_amdgcn_s_waitcnt(0xC07F); asm volatile("" ::: "memory"); }
DEV void bar() { asm volatile("" ::: "memory"); __builtin_amdgcn_s_waitcnt(0xC07F); asm volatile("s_barrier" ::: "memory"); }
DEV void set_m0(unsigned base, unsigned imm) { asm volatile("s_add_u32 m0, %0, %1" ::"s"(base), "i"(imm) : "memory", "scc"); }
template <int OFF> DEV void glds_at(i4 srd, unsigned voff, unsigned soff) {
  asm volatile("buffer_load_dwordx4 %1, %0, %2 offen offset:%3 lds" ::"s"(srd), "v"(voff), "s"(soff), "i"(OFF) : "memory");
}
template <class T> DEV void pin(T& x) { asm volatile("" : "+v"(x)); }
template <class T> DEV void spin_(T& x) { asm volatile("" : "+s"(x)); }
DEV bf4 trd(unsigned a) { return __builtin_amdgcn_ds_read_tr16_b64_v4bf16(LDSP(bf4, a)); }
DEV bf8 cat(bf4 a, bf4 b) { return __builtin_shufflevector(a, b, 0, 1, 2, 3, 4, 5, 6, 7); }
template <class TA, class TB> DEV void mma(f4& c, TA a, TB b) { asm volatile("v_mfma_f32_16x16x32_bf16 %0, %1, %2, %0" : "+v"(c) : "v"(a), "v"(b)); }
template <class TA, class TB> DEV void mma0(f4& c, TA a, TB b) { asm volatile("v_mfma_f32_16x16x32_bf16 %0, %1, %2, 0" : "=&v"(c) : "v"(a), "v"(b)); }
DEV void mma_fence() { asm volatile("s_nop 7\n\ts_nop 1" ::: "memory"); }
DEV float blo(unsigned v) { return __uint_as_float(v << 16); }
DEV float bhi(unsigned v) { return __uint_as_float(v & 0xffff0000u); }
DEV f2 unpk(unsigned v) { return f2{blo(v), bhi(v)}; }
DEV unsigned pk(f2 v) { return __builtin_bit_cast(unsigned, __builtin_convertvector(v, bf2)); }
DEV float rbf(float v) { return blo(pk(f2{v, v})); }
DEV f2 fma2(f2 a, f2 b, f2 c) { return __builtin_elementwise_fma(a, b, c); }
template <int CTRL> DEV float dpp_add(float v) {
  return v + __builtin_bit_cast(float, __builtin_amdgcn_update_dpp(0, __builtin_bit_cast(int, v), CTRL, 0xf, 0xf, true));
}
DEV void swap16(unsigned& a, unsigned& b) { auto r = __builtin_amdgcn_permlane16_swap(a, b, false, false); a = r[0]; b = r[1]; }
DEV void swap32(unsigned& a, unsigned& b) { auto r = __builtin_amdgcn_permlane32_swap(a, b, false, false); a = r[0]; b = r[1]; }
DEV float swap16_add(float a, float b) { unsigned x = __float_as_uint(a), y = __float_as_uint(b); swap16(x, y); return __uint_as_float(x) + __uint_as_float(y); }
DEV float swap32_add(float a, float b) { unsigned x = __float_as_uint(a), y = __float_as_uint(b); swap32(x, y); return __uint_as_float(x) + __uint_as_float(y); }
DEV float red2(float a, float b) {
  float s = swap32_add(a, b);
  s = swap16_add(s, s);
  s = dpp_add<0x128>(s); s = dpp_add<0x124>(s); s = dpp_add<0x122>(s); s = dpp_add<0x121>(s);
  return s;
}
DEV float rdl(float v, int l) { return __builtin_bit_cast(float, __builtin_amdgcn_readlane(__builtin_bit_cast(int, v), l)); }
DEV unsigned uni(unsigned v) { return (unsigned)__builtin_amdgcn_readfirstlane((int)v); }
DEV u4 row_load(i4 srd, unsigned voff, unsigned soff) {
  u4 r; asm volatile("buffer_load_dwordx4 %0, %2, %1, %3 offen" : "=v"(r) : "s"(srd), "v"(voff), "s"(soff) : "memory"); return r;
}
constexpr unsigned SPIN_CAP = 1u << 15;
DEV u2 ld_flag(rsrc_t rs, unsigned voff, unsigned soff) { return __builtin_amdgcn_raw_buffer_load_b64(rs, voff, soff, SC1); }
DEV unsigned flag_off(int lane) { return lane < 16 ? lane * 8 : 0x7ffffff0u; }
DEV void st_flag(rsrc_t rs, u2 tag, unsigned soff, int lane) { __builtin_amdgcn_raw_buffer_store_b64(tag, rs, flag_off(lane), soff, SC1); }
DEV bool flags_up(u2 tag, u2 v) { return __builtin_amdgcn_ballot_w64(__builtin_bit_cast(u64, v) != __builtin_bit_cast(u64, tag)) == 0; }
template <int NAP> DEV void wait_flags(rsrc_t rs, u2 tag, u2 v, unsigned voff, unsigned soff) {
#pragma nounroll
  for (unsigned spin = 0; spin < SPIN_CAP; ++spin) {
    if (flags_up(tag, v)) break;
    __builtin_amdgcn_s_sleep(NAP);
    asm volatile("" ::: "memory");
    v = ld_flag(rs, voff, soff);
  }
  asm volatile("" : "+v"(v)::"memory");
}
DEV void lds_post(unsigned a, int lane) { asm volatile("" ::: "memory"); if (lane == 0) *LDSP(volatile unsigned, a) = 1u; }
template <int NAP> DEV void lds_wait(unsigned a, unsigned want) {
#pragma nounroll
  for (unsigned spin = 0; spin < (SPIN_CAP << 6); ++spin) {
    if (uni(*LDSP(volatile unsigned, a)) >= want) break;
    if (NAP) __builtin_amdgcn_s_sleep(NAP);
  }
  asm volatile("" ::: "memory");
}
DEV unsigned lds_arrive(unsigned a, int lane) {
  asm volatile("s_waitcnt lgkmcnt(0)" ::: "memory");
  unsigned o = 0;
  if (lane == 0) o = __hip_atomic_fetch_add(LDSP(unsigned, a), 1u, __ATOMIC_RELAXED, __HIP_MEMORY_SCOPE_WORKGROUP);
  return uni(o);
}

#define STAMP(i)
#define FINISH()

#define TILE_P(c) (c.xc & 1)
#define TILE_T(c) ((c.xc >> 1) * 8 + 2 * (c.jj & 3) + TILE_P(c))
#define TILE_C(c) (c.jj >> 2)
#define GPRIO 0
#define CPF 2
struct Ctx {
  int xc, jj, w, v, lane, n, q;
  u2 tag;
  rsrc_t ws;
};

struct alignas(16) F1 { float v; };
DEV float red4(float a, float b, float c, float d) {
  float s = swap16_add(swap32_add(a, b), swap32_add(c, d));
  s = dpp_add<0x128>(s); s = dpp_add<0x124>(s); s = dpp_add<0x122>(s); s = dpp_add<0x121>(s);
  return s;
}
DEV int ln_row(int r) { return r * 1088 + (r & 8) * 4; }
DEV void ln_compute(const Ctx& c, const u4* rows, unsigned img, int tok0) {
  f2 x[4][4]; float t[4];
#pragma unroll
  for (int k = 0; k < 4; k++) {
#pragma unroll
    for (int i = 0; i < 4; i++) x[k][i] = unpk(rows[k][i]);
    const f2 s = (x[k][0] + x[k][1]) + (x[k][2] + x[k][3]); t[k] = s.x + s.y;
  }
  float mean = red4(t[0], t[2], t[1], t[3]) * (1.f / D);
#pragma unroll
  for (int k = 0; k < 4; k++) {
    const float m = rdl(mean, 16 * k);
    f2 qq = {0.f, 0.f};
#pragma unroll
    for (int i = 0; i < 4; i++) { x[k][i] -= m; qq = fma2(x[k][i], x[k][i], qq); }
    t[k] = qq.x + qq.y;
  }
  float var = red4(t[0], t[2], t[1], t[3]) * (1.f / D);
  const unsigned st = pk(f2{mean, __builtin_amdgcn_sqrtf(rbf(rbf(var) + 1e-5f))});
  const float r = __builtin_amdgcn_rcpf(bhi(st)), o = (mean - blo(st)) * r;
#pragma unroll
  for (int k = 0; k < 4; k++) {
    const float rk = rdl(r, 16 * k), ok = rdl(o, 16 * k);
    u4 nv;
#pragma unroll
    for (int i = 0; i < 4; i++) nv[i] = pk(fma2(x[k][i], f2{rk, rk}, f2{ok, ok}));
    *LDSP(u4, img + ln_row(4 * c.w + k) + c.lane * 16) = nv;
  }
  if (c.n == 0) *LDSP(unsigned, img + 1024 + (c.w >> 1) * 1088 + (4 * (c.w & 1) + c.q) * 4) = st;
}
DEV void ln_store(const Ctx& c, unsigned img, int tok0) {
  const unsigned a = img + ln_row(8 * c.q + (c.n >> 2)) + (64 * c.w + 4 * (c.n & 3)) * 2;
  if (c.w == 7) __builtin_amdgcn_raw_buffer_store_b32(*LDSP(unsigned, img + 1024 + ((c.lane >> 3) & 3) * 1088 + (c.lane & 7) * 4), c.ws,
                                                      c.lane < 32 ? c.lane * 4 : 0x7ffffff0u, WS_STATS + tok0 * 4, SC1);
  bf8 t[4];
#pragma unroll
  for (int i = 0; i < 4; i++) t[i] = cat(trd(a + 32 * i), trd(a + 32 * i + 4 * 1088));
#pragma unroll
  for (int i = 0; i < 4; i++) st128(__builtin_bit_cast(u4, t[i]), c.ws, c.n * 64 + c.q * 16 + i * 1024, WS_XNT + tok0 * 1024 + c.w * 4096);
}
DEV unsigned u_off(const Ctx& c) { return (c.lane >> 3) * 32768 + (c.lane & 7) * 16; }
DEV u4 u_load(const Ctx& c, unsigned uv, int i) {
  return __builtin_amdgcn_raw_buffer_load_b128(c.ws, uv, WS_XNT + (16 * c.v + 8 * i) * 32768 + (c.xc * 32 + c.jj) * 128, SC1);
}
DEV void u_write(const Ctx& c, u4 r0, u4 r1) {
  const unsigned a = LU + ((c.lane >> 2) & 1) * UCH + 512 + (16 * c.v + (c.lane >> 3)) * 64 + (c.lane & 3) * 16;
  *LDSP(u4, a) = r0;
  *LDSP(u4, a + 512) = r1;
  if (c.v == 0) *LDSP(u4, LU + (c.lane & 1) * UCH + (c.lane >> 1) * 16) = u4{0, 0, 0, 0};
}
DEV u2 flag_sample(const Params& P, unsigned voff, unsigned soff) {
  u2 r; asm volatile("buffer_load_dwordx2 %0, %2, %1, %3 offen sc1" : "=v"(r) : "s"(mksrd(P.ws, WS_BYTES)), "v"(voff), "s"(soff) : "memory"); return r;
}

struct C2 { f2 re, im; };
DEV C2 cmul(C2 a, C2 b) { C2 r; r.re = fma2(a.re, b.re, -(a.im * b.im)); r.im = fma2(a.re, b.im, a.im * b.re); return r; }
DEV C2 zpow(f2 al, f2 th, float e) {
  const f2 a = al * e, t0 = th * e;
  const f2 t = {__builtin_amdgcn_fractf(t0.x), __builtin_amdgcn_fractf(t0.y)};
  const f2 m = {__builtin_amdgcn_exp2f(a.x), __builtin_amdgcn_exp2f(a.y)};
  C2 r; r.re = m * f2{__builtin_amdgcn_cosf(t.x), __builtin_amdgcn_cosf(t.y)}; r.im = m * f2{__builtin_amdgcn_sinf(t.x), __builtin_amdgcn_sinf(t.y)};
  return r;
}
struct KIn { unsigned are, aim, bre, bim, cre, cim, ldt, dsk; u64 epoch; };
DEV unsigned par_load(const u16* p, unsigned voff) {
  unsigned r; asm volatile("global_load_dword %0, %1, %2" : "=v"(r) : "v"(voff), "s"(p) : "memory"); return r;
}
DEV unsigned spar_load(const u16* p, unsigned off) { unsigned r; asm volatile("s_load_dword %0, %1, %2" : "=s"(r) : "s"(p), "s"(off) : "memory"); return r; }
template <int ROLE> DEV KIn kgen_load(const Ctx& c, const Params& P) {
  const unsigned pair = c.xc * 32 + c.jj, o = (2 * pair + (c.w & 1)) * 256 + c.lane * 4;
  KIn r;
  r.are = par_load(P.are, o); r.aim = par_load(P.aim, o);
  r.ldt = spar_load(P.ldt, pair * 4); r.dsk = spar_load(P.dsk, pair * 4);
  asm volatile("s_load_dwordx2 %0, %1, 0" : "=s"(r.epoch) : "s"(P.ws + WS_EPOCH) : "memory");
  if (ROLE == 0 && c.w < 2) { r.bre = par_load(P.bre, o); r.bim = par_load(P.bim, o); r.cre = par_load(P.cre, o); r.cim = par_load(P.cim, o); }
  return r;
}
template <int ROLE, int N> DEV void kgen_take(Ctx& c, const Params& P, KIn& k) {
  KIn o; char* ws = P.ws;
  asm volatile("s_waitcnt lgkmcnt(0)\n\ts_mov_b32 %0, %4\n\ts_mov_b32 %1, %5\n\ts_mov_b64 %2, %6"
               : "=&s"(o.ldt), "=&s"(o.dsk), "=&s"(o.epoch), "+s"(ws) : "s"(k.ldt), "s"(k.dsk), "s"(k.epoch) : "memory");
  asm volatile("s_waitcnt vmcnt(%4)\n\tv_mov_b32 %0, %2\n\tv_mov_b32 %1, %3" : "=&v"(o.are), "=&v"(o.aim) : "v"(k.are), "v"(k.aim), "i"(N) : "memory");
  if (ROLE == 0) asm volatile("v_mov_b32 %0, %4\n\tv_mov_b32 %1, %5\n\tv_mov_b32 %2, %6\n\tv_mov_b32 %3, %7"
               : "=&v"(o.bre), "=&v"(o.bim), "=&v"(o.cre), "=&v"(o.cim) : "v"(k.bre), "v"(k.bim), "v"(k.cre), "v"(k.cim) : "memory");
  k = o;
  c.tag = u2{(unsigned)(o.epoch + 1), (unsigned)((o.epoch + 1) >> 32)};
  c.ws = mkrs(ws, WS_BYTES);
}
constexpr unsigned LKB = LKG + 44416;
struct KPre { float del, g0, g1, rev; unsigned wa, wb, rd, sh, rs, ta, tb, s0, s1; bool sw; };
template <int ROLE> DEV void kgen_pre(const Ctx& c, KPre& k) {
  const bool hi = c.v >= 2;
  const int ch = c.w & 1;
  const unsigned T = ROLE == 0 ? (hi ? LKG : LKB) + ch * 8672 : (hi ? LKG + 17344 + ch * 8672 : LKG + 34688 + ch * 4864);
  k.del = ROLE == 0 ? (hi ? 1.f : 8.f) : (hi ? 128.f : 2.f);
  k.g0 = ROLE == 0 ? (hi ? 128.f : 64.f) : (hi ? 512.f : 256.f);
  k.g1 = hi ? 516.f : 2048.f;
  k.rev = (ROLE == 0 && !hi) ? -0.15915494309189535f : 0.15915494309189535f;
  k.wa = T + c.lane * 8; k.wb = k.wa + ((ROLE == 1 && !hi) ? 4 : 8) * 544; k.rd = T + c.n * 544 + c.q * 16;
  k.sh = ch ? 0 : 16;
  k.rs = hi ? 1088 : 544;
  pin(k.del); pin(k.g0); pin(k.wa); pin(k.rd);
  if (ROLE == 0) pin(k.rev); else { pin(k.g1); pin(k.wb); }
}
template <int ROLE> DEV void ktab_pre(const Ctx& c, KPre& k) {
  const bool hi = c.v >= 2;
  const int n = c.n, q = c.q;
  const unsigned tb = LKT + (c.w & 1) * 1536;
  if (ROLE == 0) { k.ta = hi ? tb + 256 * (q >> 1) + 8 * (q & 1) + 16 * n : LJK + c.lane * 8; pin(k.ta); }
  else {
    const bool far = hi && q >= 2;
    k.ta = tb + 128 * q + (!hi ? (q < 2 ? 512 : 1024) + 8 * n : 768 + (q < 2 ? 4 * n : 62 * (n & 1) + 2 * n));
    k.tb = k.ta + (!hi ? 4 : q < 2 ? 64 : 32);
    k.s0 = !far ? 0x03020100u : (n & 1) ? 0x01000504u : 0x05040100u;
    k.s1 = k.s0 + (far ? 0x02020202u : 0x04040404u);
    k.sw = far && (n & 1);
    pin(k.ta); pin(k.tb); pin(k.s0); pin(k.s1);
  }
}
template <int ROLE, class Hook>
DEV void kgen_rows(const Ctx& c, const KPre& p, const KIn& k, bf8 (&A)[8], Hook&& hook) {
  const float dt = rbf(__builtin_amdgcn_exp2f(bhi(k.ldt << p.sh) * 1.4426950408889634f));
  const f2 al = (unpk(k.are) * dt) * 1.4426950408889634f;
  const f2 th = (unpk(k.aim) * dt) * p.rev;
  auto put = [&](unsigned ad, C2 z) __attribute__((always_inline)) { *LDSP(u2, ad) = u2{pk(z.re), pk(z.im)}; };
  C2 st = zpow(al, th, p.del);
  pin(st.re); pin(st.im);
  hook(IC<0>{});
  C2 a, b, e;
  if (ROLE == 0) {
    a = C2{f2{1.f, 1.f}, f2{0.f, 0.f}}; b = zpow(al, th, p.g0);
    if (c.w < 2) { a = cmul(C2{unpk(k.cre), -unpk(k.cim)}, C2{unpk(k.bre) * dt, unpk(k.bim) * -dt}); b = cmul(b, a); }
  } else {
    a = zpow(al, th, p.g0);
    if (c.w >= 6) b = cmul(a, a); else b = zpow(al, th, 384.f);
  }
  pin(b.re); pin(b.im);
  hook(IC<1>{});
  sfor<0, 4>(LAM(I) {
    constexpr int i = CV(I);
    put(p.wa + i * (ROLE ? p.rs : 544), a); put(p.wb + i * 544, b);
    hook(IC<2 + i>{});
    if constexpr (ROLE == 0 || i < 3) a = cmul(a, st); else a = zpow(al, th, p.g1);
    b = cmul(b, st);
  });
  if (ROLE == 1 && c.w < 6) { put(p.wa + 8 * 544, a); hook(IC<6>{}); hook(IC<7>{}); hook(IC<8>{}); }
  else sfor<4, 8>(LAM(I) {
    constexpr int i = CV(I);
    put(p.wa + (ROLE ? 2 * i - 7 : i) * 544, a); put(p.wb + i * 544, b);
    if constexpr (i < 7) { hook(IC<2 + i>{}); a = cmul(a, st); b = cmul(b, st); }
  });
#pragma unroll
  for (int i = 0; i < 8; i++) A[i] = *LDSP(bf8, p.rd + i * 64);
}
DEV void kgen_cols(const Ctx& c, bf8 (&B)[8]) {
#pragma unroll
  for (int i = 0; i < 8; i++) B[i] = *LDSP(bf8, LKB + (c.w & 1) * 8672 + c.n * 544 + c.q * 16 + i * 64);
}
DEV void kgen_mma(f4& t, const bf8 (&A)[8], const bf8 (&B)[8]) {
  mma0(t, A[0], B[0]);
#pragma unroll
  for (int i = 1; i < 8; i++) mma(t, A[i], B[i]);
}
struct alignas(16) U1 { unsigned v; };
struct KTab { f4 t; U1 a, b, p[2]; };
constexpr int KT_OPS = 9;
template <int ROLE, int k> DEV void ktab_op(const Ctx& c, const KPre& p, KTab& z) {
  if constexpr (ROLE == 0) {
    if constexpr (k == 0) { pin(z.t); z.t[0] += __uint_as_float(z.a.v); pin(z.t); }
    if constexpr (k == 1) { z.p[1].v = pk(f2{z.t[2], z.t[3]}); pin(z.p[1].v); }
    if constexpr (k == 2) { z.p[0].v = pk(f2{z.t[0], z.t[1]}); pin(z.p[0].v); }
    if constexpr (k == 3) *LDSP(u2, p.ta) = u2{z.p[0].v, z.p[1].v};
  } else {
    if constexpr (k == 0) { pin(z.t); z.p[0].v = pk(f2{z.t[0], z.t[1]}); pin(z.p[0].v); }
    if constexpr (k == 1) { z.p[1].v = pk(f2{z.t[2], z.t[3]}); pin(z.p[1].v); }
    if constexpr (k == 2) { z.b.v = p.sw ? z.p[0].v : z.p[1].v; pin(z.b.v); }
    if constexpr (k == 3) { z.a.v = p.sw ? z.p[1].v : z.p[0].v; pin(z.a.v); }
    if constexpr (k == 4) { z.b.v = (unsigned)__builtin_amdgcn_update_dpp((int)z.b.v, (int)z.b.v, 0xb1, 0xc, 0xf, false); pin(z.b.v); }
    if constexpr (k == 5) { z.p[0].v = __builtin_amdgcn_perm(z.b.v, z.a.v, p.s0); pin(z.p[0].v); }
    if constexpr (k == 6) { z.p[1].v = __builtin_amdgcn_perm(z.b.v, z.a.v, p.s1); pin(z.p[1].v); }
    if constexpr (k == 7) *LDSP(unsigned, p.ta) = z.p[0].v;
    if constexpr (k == 8) *LDSP(unsigned, p.tb) = z.p[1].v;
  }
}
DEV void ktab_skip(const Ctx& c, const KPre& p, KTab& z, unsigned dsk) { z.a.v = (c.w >= 2 && c.lane == 0) ? (dsk << p.sh) & 0xffff0000u : 0u; pin(z.a.v); }
typedef unsigned u2a __attribute__((ext_vector_type(2), aligned(4)));
struct KExp { U1 a0, a1, a2, a3, s[7], wr, h0, pv[2][2], o[2][4][4], es[3]; u2 e[4]; };
template <int PART> DEV void kexp_pre(const Ctx& c, KExp& z) {
  const int kh = c.v >> 1, l = c.lane;
  const unsigned CC = LKT + (c.v & 1) * 1536;
  const bool up = l >= 32, odd = l & 1;
  if (PART == 0) {
    z.s[0].v = (kh || up) ? 0x01000100u : 0x03020100u;
    z.s[1].v = kh ? 0x03020100u : up ? 0x05040302u : 0x07060504u;
    z.s[2].v = kh ? 0x03020302u : z.s[0].v;
    z.s[3].v = kh ? 0x05040504u : z.s[1].v;
    z.s[4].v = odd ? 0x03020302u : 0x01000100u;
    z.s[5].v = odd ? 0x05040504u : 0x03020100u;
    z.s[6].v = odd ? 0x05040504u : 0x03020302u;
    z.es[0].v = kh ? 0x01000100u : 0x0c0c0c0cu; z.es[1].v = kh ? 0x05040302u : 0x0c0c0c0cu; z.es[2].v = kh ? 0x05040504u : 0x03020302u;
    asm volatile("" : "+s"(z.es[0].v), "+s"(z.es[1].v), "+s"(z.es[2].v));
    asm volatile("" : "+v"(z.s[0].v), "+v"(z.s[1].v), "+v"(z.s[2].v), "+v"(z.s[3].v), "+v"(z.s[4].v), "+v"(z.s[5].v), "+v"(z.s[6].v));
  } else {
    z.a0.v = CC + (kh ? 768 + l * 4 : up ? 256 + l * 8 : l * 16);
    z.a1.v = z.a0.v + (kh ? 0 : up ? 4 : 8);
    z.a2.v = CC + 1024 + 128 * kh + (l >> 1) * 4;
    z.a3.v = CC + 764;
    z.wr.v = LKC + (c.v & 1) * KCH + 32 + kh * 1024 + l * 16;
    asm volatile("" : "+v"(z.a0.v), "+v"(z.a1.v), "+v"(z.a2.v), "+v"(z.a3.v), "+v"(z.wr.v));
  }
}
constexpr int KX_OPS = 45;
template <int k> DEV void kexp_op(const Ctx& c, KExp& z) {
  auto set = [&](U1& d, unsigned x) __attribute__((always_inline)) { d.v = x; pin(d.v); };
  if constexpr (k == 0) z.e[0] = *LDSP(u2a, z.a0.v);
  else if constexpr (k == 1) z.e[1] = *LDSP(u2a, z.a1.v);
  else if constexpr (k == 2) z.e[2] = *LDSP(u2a, z.a2.v);
  else if constexpr (k == 3) z.e[3] = *LDSP(u2a, z.a3.v);
  else if constexpr (k == 4) z.h0.v = *LDSP(unsigned, z.a3.v + 256);
  else if constexpr (k < 9) { constexpr int i = k - 5; set(z.o[0][0][i], __builtin_amdgcn_perm(z.e[i >> 1][1], z.e[i >> 1][0], z.s[i].v)); }
  else if constexpr (k < 13) { constexpr int i = k - 9;
    if constexpr (i == 1) asm volatile("v_mov_b32 %0, %1" : "=v"(z.o[1][0][1].v) : "v"(z.o[1][0][0].v));
    else set(z.o[1][0][i], __builtin_amdgcn_perm(z.e[2][1], z.e[2][0], z.s[i ? 3 + i : 4].v));
  }
  else if constexpr (k == 13) { z.pv[0][0].v = __builtin_amdgcn_perm(z.e[3][1], z.e[3][0], z.es[0].v); pin(z.pv[0][0].v); }
  else if constexpr (k == 14) { z.pv[0][1].v = __builtin_amdgcn_perm(z.e[3][1], z.e[3][0], z.es[1].v); pin(z.pv[0][1].v); }
  else if constexpr (k == 15) { z.pv[1][0].v = __builtin_amdgcn_perm(z.e[2][0], z.h0.v, z.es[2].v); pin(z.pv[1][0].v); }
  else if constexpr (k == 16) { z.pv[1][1].v = z.o[1][0][0].v; pin(z.pv[1][1].v); }
  else if constexpr (k < 21) {
    constexpr int i = (k - 17) >> 1, j = (k - 17) & 1;
    z.pv[i][j].v = (unsigned)__builtin_amdgcn_update_dpp((int)z.pv[i][j].v, (int)z.o[i][0][2 + j].v, 0x138, 0xf, 0xf, false); pin(z.pv[i][j].v);
  } else {
    constexpr int i = (k - 21) / 12, cp = 1 + (k - 21) % 12 / 4, j = (k - 21) % 4;
    auto dw = [&](int m) __attribute__((always_inline)) { return m < 0 ? z.pv[i][2 + m].v : z.o[i][0][m].v; };
    if constexpr (cp == 2) asm volatile("v_mov_b32 %0, %1" : "=v"(z.o[i][2][j].v) : "v"(dw(j - 1)));
    else asm volatile("v_alignbit_b32 %0, %1, %2, 16" : "=v"(z.o[i][cp][j].v) : "v"(dw(j - (cp >> 1))), "v"(dw(j - 1 - (cp >> 1))));
  }
}
template <int k> DEV void kexp_put(const Ctx& c, const KExp& z) {
  if constexpr (k < 8) *LDSP(u4, z.wr.v + (k >> 2) * 2048 + (k & 3) * KCS) = u4{z.o[k >> 2][k & 3][0].v, z.o[k >> 2][k & 3][1].v, z.o[k >> 2][k & 3][2].v, z.o[k >> 2][k & 3][3].v};
  else *LDSP(unsigned, LKC + (c.lane >> 3) * KCS + (c.lane & 7) * 4) = 0u;
}

struct WdSrc { i4 srd; unsigned voff[4], par; };
DEV WdSrc wd_src(const Ctx& c, const Params& P) {
  WdSrc s; s.srd = mksrd(P.wd, F * D * 2); s.par = TILE_P(c) * 32768;
  const int r = c.lane >> 3;
#pragma unroll
  for (int k = 0; k < 4; k++) { s.voff[k] = (448 * c.v + r) * 1024 + TILE_C(c) * 128 + (((c.lane & 7) ^ (r & 6)) * 16) + k * 7168; pin(s.voff[k]); }
  return s;
}
DEV unsigned wd_raddr(int v, int n, int q) { return v * 4096 + (4 * q + (n >> 2)) * 128 + (((2 * q + (n >> 3)) & 3) * 32) + (n & 3) * 8; }
DEV void wd_ld(const WdSrc& s, int i, int r) {
  const unsigned so = s.par ^ ((i % 7) * 65536 + (i / 7) * 32768);
  if (r == 0) glds_at<0>(s.srd, s.voff[0], so);
  else if (r == 1) glds_at<1024>(s.srd, s.voff[1], so);
  else if (r == 2) glds_at<2048>(s.srd, s.voff[2], so);
  else glds_at<3072>(s.srd, s.voff[3], so);
}
DEV void wd_chunk(const Ctx& c, const WdSrc& s, int i) {
  set_m0(c.v * 4096, wd_base(i % 7)); asm volatile("s_nop 0");
#pragma unroll
  for (int r = 0; r < 4; r++) wd_ld(s, i, r);
}

struct Frags { bf4 a[4][2]; bf8 b[4]; };
DEV void rd_piece(Frags& Fr, int i, unsigned wa0, unsigned wa1, unsigned xa, int ks) {
  if (i == 0) Fr.b[0] = *LDSP(bf8, xa);
  else if (i < 9) { const int t = (i - 1) >> 1, h = (i - 1) & 1; Fr.a[t][h] = trd(((t & 1) ? wa1 : wa0) + (t >> 1) * 16384 + ks * 8192 + h * 1024); }
  else Fr.b[i - 8] = *LDSP(bf8, xa + (i - 8) * 2048);
}

struct G1Src { i4 sx, sg, su; unsigned vx0, vx1, vw0, vw1; int k0; };
DEV G1Src g1_src(const Ctx& c) {
  const int p = (c.xc & 3) * 4 + (c.jj & 3), ft = (c.xc >> 2) * 7 + (c.jj >> 2);
  G1Src g;
  g.k0 = 2 * (c.xc & 3);
  const int xr = c.lane >> 3;
  g.vx0 = (p * 128 + 16 * c.w + xr) * 1024 + (((c.lane & 7) ^ (xr >> 1)) * 16);
  g.vx1 = (g.vx0 ^ 64) + 8192 - 1024;
  g.vw0 = (8 * c.w + c.q) * 3584 + ft * 256 + ((c.n ^ ((c.q | ((c.w & 1) << 2)) << 1)) * 16);
  g.vw1 = g.vw0 + 4 * 3584 - 1024;
  asm volatile("" : "+v"(g.vx0), "+v"(g.vx1), "+v"(g.vw0), "+v"(g.vw1));
  return g;
}
DEV void g1_m0(const Ctx& c, int s, int i) { set_m0(c.w * 2048, slot_of(s) + (i >> 1) * 16384); }
DEV void g1_ld(const G1Src& g, int s, int i) {
  const int sk = (s + g.k0) & 7;
  if (i == 0) glds_at<0>(g.sx, g.vx0, sk * 128);
  else if (i == 1) glds_at<1024>(g.sx, g.vx1, sk * 128);
  else if (i & 1) glds_at<1024>(i > 3 ? g.su : g.sg, g.vw1, sk * 64 * 3584);
  else glds_at<0>(i > 3 ? g.su : g.sg, g.vw0, sk * 64 * 3584);
}
struct G1Rot { i4 srd[2]; unsigned vo[2], dst[4], so[2]; };
DEV G1Rot g1_rot(const Ctx& c, const G1Src& g) {
  const int mine = c.jj & 3;
  G1Rot r;
#pragma unroll
  for (int k = 0; k < 2; k++) {
    const bool up = ((mine >> 1) ^ k) & 1;
    r.srd[k] = g.sg; r.srd[k][0] = up ? g.su[0] : g.sg[0]; r.srd[k][1] = up ? g.su[1] : g.sg[1];
    r.vo[k] = g.vw0 + ((mine ^ k) & 1) * 4 * 3584;
    r.so[k] = (g.k0 + k) * 64 * 3584;
  }
#pragma unroll
  for (int k = 0; k < 4; k++) r.dst[k] = 16384 + ((mine ^ k) >> 1) * 16384 + c.w * 2048 + ((mine ^ k) & 1) * 1024;
  return r;
}
template <int S, int K> DEV void g1_ld_w(const G1Rot& r) {
  asm volatile("s_add_u32 m0, %3, %4\n\ts_nop 0\n\tbuffer_load_dwordx4 %1, %0, %2 offen lds"
               ::"s"(r.srd[K >> 1]), "v"(r.vo[K & 1]), "s"(r.so[S]), "s"(r.dst[K]), "i"(slot_of(S)) : "memory", "scc");
}

#define SCH 4, 4, 3, 4, 3, 3, 3, 3, 3, 3, 3
constexpr int FEED_N[11] = {SCH};
constexpr int feed_done(int h) { int e = 12; for (int k = 0; k <= h && k < 11; k++) e += FEED_N[k]; return e; }
constexpr int feed_at(int h, int i) {
  if (h > 10) return -1;
  const int nn = FEED_N[h], e0 = feed_done(h) - nn;
  for (int k = 0; k < nn; k++) if ((nn == 4 ? 1 + 4 * k : 4 + 5 * k) == i) return e0 + k;
  return -1;
}
constexpr int feed_younger(int s) { return feed_done(2 * s) - 6 * (s + 2); }
constexpr bool feed_ok() {
  for (int s = 0; s < 6; s++) if (feed_younger(s) < 0 || feed_done(2 * s) > 6 * (s + 3)) return false;
  return feed_done(10) == 48 && FEED_N[0] <= 4;
}
static_assert(feed_ok(), "load schedule");
constexpr int wd_at(int h, int i) { for (int e = 0; e < 28; e++) if (11 + e / 6 == h && 1 + (e % 6) * 5 / 2 == i) return e; return -1; }

template <int ROLE>
DEV void gemm1(Ctx& c, const Params& P, G1Src gs, KIn kin, WdSrc& wds, f4 (&acc)[4][4], bf4 (&W1)[7][4][2], unsigned (&wa)[4], KExp& kx, u4 (&ur)[2], f4 (&cv)[4], f4 (&tc)[4]) {
  const int p = (c.xc & 3) * 4 + (c.jj & 3), ft = (c.xc >> 2) * 7 + (c.jj >> 2);
  const int lane = c.lane, n = c.n, q = c.q, v = c.v;
  gs.sx = mksrd(P.x, L * D * 2); gs.sg = mksrd(P.wg, D * F * 2); gs.su = mksrd(P.wu, D * F * 2);
  G1Rot rot = g1_rot(c, gs);
#define R1C 0
  if (ROLE == 0 || !R1C) {
    set_m0(rot.dst[0], 0); asm volatile("s_nop 0"); glds_at<0>(rot.srd[0], rot.vo[0], rot.so[0]);
    if (R1C) { set_m0(rot.dst[0], 8192); asm volatile("s_nop 0"); glds_at<0>(rot.srd[0], rot.vo[0], rot.so[0] + 32 * 3584); }
    if (ROLE == 0) touch(gs.sx, (lane < 10 ? lane >> 1 : 4) * 1024 + (lane & 1) * 128, (p * 128 + (c.jj >> 2) * 18 + 5 * v) * 1024 + gs.k0 * 128);
    set_m0(rot.dst[0], slot_of(1)); asm volatile("s_nop 0"); glds_at<0>(rot.srd[0], rot.vo[0], rot.so[1]);
    if (R1C) { set_m0(rot.dst[0], slot_of(1) + 8192); asm volatile("s_nop 0"); glds_at<0>(rot.srd[0], rot.vo[0], rot.so[1] + 32 * 3584); }
  }
#define KPRI 1
  if (ROLE == 1 && (KPRI == 1 || (KPRI == 2 && v < 2))) __builtin_amdgcn_s_setprio(1);
  SB();
  KPre kp;
  kgen_pre<ROLE>(c, kp);
  unsigned xa[2][2], wr[3][2];
  xa[0][0] = (((ROLE ^ v) & 1) * 64 + n) * 128 + ((q ^ (n >> 1)) * 16); xa[0][1] = xa[0][0] ^ 64;
  wr[0][0] = 16384 + (8 * q + (n >> 2)) * 256 + (((2 * v) ^ ((n >> 2) | ((q & 1) << 2))) * 32) + (n & 3) * 8; wr[0][1] = wr[0][0] ^ 32;
  unsigned kbr = LKB + (c.w & 1) * 8672 + n * 544 + q * 16;
  asm volatile("" : "+v"(xa[0][0]), "+v"(xa[0][1]), "+v"(wr[0][0]), "+v"(wr[0][1]), "+v"(kbr));
  unsigned lfv, cpv, l4, stv, csa, sta, one0, uv; u4 ones;
  if (ROLE == 0) {
    if (threadIdx.x < 12) *LDSP(unsigned, LSY + threadIdx.x * 4) = 0u;
    ones = u4{0x3f803f80u, 0x3f803f80u, 0x3f803f80u, 0x3f803f80u}; pin(ones);
  }
  kgen_take<ROLE, R1C ? 5 - 5 * ROLE : 3 - ROLE>(c, P, kin);
  bf8 KA[8], KB[8]; KTab kt;
  kgen_rows<ROLE>(c, kp, kin, KA, LAM(H) {});
  if (ROLE == 1) ktab_pre<1>(c, kp);
  u2 tagv;
  if (ROLE == 1 && v < 2) {
    const unsigned bx0 = gs.vx0 - 65536, bx1 = gs.vx1 - 65536, bw0 = rot.vo[0] - 32 * 3584, bw1 = rot.vo[1] - 32 * 3584;
    const unsigned ox0 = gs.k0 * 128, ow0 = rot.so[0], dx = c.v * 2048, d1 = rot.dst[1] - 8192, d2 = rot.dst[2] - 8192, d3 = rot.dst[3] - 8192;
#pragma unroll
    for (unsigned it = 0; it < 8; it++) {
      const unsigned st = it >> 2, k = it & 3, dm = st * slot_of(1) + k * 4096;
      unsigned ox, ow;
      asm volatile("s_add_u32 m0, %11, %15\n\ts_add_u32 %0, %9, %16\n\tbuffer_load_dwordx4 %5, %2, %0 offen lds\n\t"
                   "buffer_load_dwordx4 %6, %2, %0 offen offset:1024 lds\n\t"
                   "s_add_u32 m0, %12, %15\n\ts_add_u32 %1, %10, %17\n\tbuffer_load_dwordx4 %8, %3, %1 offen lds\n\t"
                   "s_add_u32 m0, %13, %15\n\ts_nop 0\n\tbuffer_load_dwordx4 %7, %4, %1 offen lds\n\t"
                   "s_add_u32 m0, %14, %15\n\ts_nop 0\n\tbuffer_load_dwordx4 %8, %4, %1 offen lds"
                   : "=&s"(ox), "=&s"(ow)
                   : "s"(gs.sx), "s"(rot.srd[0]), "s"(rot.srd[1]), "v"(bx0), "v"(bx1), "v"(bw0), "v"(bw1), "s"(ox0), "s"(ow0),
                     "s"(dx), "s"(d1), "s"(d2), "s"(d3), "i"(dm), "i"(st * 128 + k * 32768), "i"((st * 64 + k * 16) * 3584)
                   : "memory", "scc");
#define THN 0
#define THS 2
      if (it < THN) __builtin_amdgcn_s_sleep(THS);
    }
    waitv<20>();
  } else waitv<R1C ? 2 : 1>();
  __builtin_amdgcn_s_setprio(1);
  bar();
  STAMP(2);
  Frags F0, Fb;
  auto rd = [&](Frags& Fr, int i, int s, int ks) __attribute__((always_inline)) {
    rd_piece(Fr, i, wr[s % 3][0], wr[s % 3][1], xa[s % 3 == 2][ks] + (s % 3 == 1 ? slot_of(1) : 0), ks);
  };
#pragma unroll
  for (int i = 0; i < 12; i++) rd(F0, i, 0, 0);
  SB();
  f4 cs[4];
  auto colsum = [&](const Frags& Fr, int rel) __attribute__((always_inline)) {
    if (ROLE == 0 && (c.jj & 3) == rel) {
#pragma unroll
      for (int t = 0; t < 4; t++) asm volatile("v_mfma_f32_16x16x32_bf16 %0, %1, %2, 0" : "+v"(cs[t]) : "v"(ones), "v"(cat(Fr.a[t][0], Fr.a[t][1])));
    }
    SB();
  };
  u2 lf; u4 cpr[4]; unsigned str; F1 cs4[4], tk[4], tr; U1 csm, t0, t1, ga, gb;
  auto feed = [&](auto H, auto I) __attribute__((always_inline)) {
    constexpr int h = CV(H), i = CV(I);
    constexpr int e = feed_at(h, i), en = feed_at(h, i + 1);
    if constexpr (e >= 0) g1_ld(gs, e / 6, e % 6);
    if constexpr (en >= 0 && !(en & 1)) g1_m0(c, en / 6, en % 6);
    constexpr int d = ROLE ? wd_at(h, i) : -1, dn = ROLE ? wd_at(h, i + 1) : -1;
    if constexpr (d >= 0) wd_ld(wds, d >> 2, d & 3);
    if constexpr (dn >= 0 && !(dn & 3)) set_m0(v * 4096, wd_base(dn >> 2));
  };
  sfor<0, 8>(LAM(S) {
    constexpr int s = CV(S);
    sfor<0, 16>(LAM(I) {
      constexpr int i = CV(I);
      if constexpr (s == 0) mma0(acc[i >> 2][i & 3], cat(F0.a[i & 3][0], F0.a[i & 3][1]), F0.b[i >> 2]);
      else mma(acc[i >> 2][i & 3], cat(F0.a[i & 3][0], F0.a[i & 3][1]), F0.b[i >> 2]);
      if constexpr (s == 0) {
        if constexpr (i == 14 || i == 15) { wr[1][i & 1] = wr[0][i & 1] + slot_of(1); pin(wr[1][i & 1]); }
        if constexpr (i < 12) rd(Fb, i, s, 1);
        if constexpr (i >= 6 && i < 14) KB[i - 6] = *LDSP(bf8, kbr + (i - 6) * 64);
      } else if constexpr (i != 4 && i != 9 && i < 14) rd(Fb, i - (i > 4) - (i > 9), s, 1);
      feed(IC<2 * s>{}, I);
      if constexpr (s == 1 && (i == 2 || i == 3)) { wr[2][i & 1] = wr[0][i & 1] + SLOT_C; pin(wr[2][i & 1]); }
      if constexpr (s == 1 && (i == 6 || i == 7)) { xa[1][i & 1] = xa[0][i & 1] + SLOT_C; pin(xa[1][i & 1]); }
      if constexpr (ROLE == 1 && s == 3) {
        if constexpr (i == 0) { t0.v = lane >> 3; pin(t0.v); }
        if constexpr (i == 1) { t1.v = (lane ^ (t0.v & 6)) & 7; pin(t1.v); }
        if constexpr (i == 3) { t0.v = t0.v * 1024 + (448 * v * 1024 + TILE_C(c) * 128); pin(t0.v); }
        if constexpr (i == 5) { wds.voff[0] = t1.v * 16 + t0.v; pin(wds.voff[0]); wds.srd = mksrd(P.wd, F * D * 2); wds.par = TILE_P(c) * 32768; }
        if constexpr (i >= 6 && i < 9) { wds.voff[i - 5] = wds.voff[0] + (i - 5) * 7168; pin(wds.voff[i - 5]); }
      }
      if constexpr (ROLE == 1 && s == 4) {
        if constexpr (i == 0) { t0.v = lane >> 2; pin(t0.v); }
        if constexpr (i == 1) { t0.v = t0.v * 128 + (v * 4096 + SLOT_C); pin(t0.v); }
        if constexpr (i == 2) { t1.v = lane & 3; pin(t1.v); }
        if constexpr (i == 3) { t0.v = t1.v * 8 + t0.v; pin(t0.v); }
        if constexpr (i == 5) { t1.v = lane & 24; pin(t1.v); }
        if constexpr (i == 6) { wa[0] = t1.v * 4 + t0.v; pin(wa[0]); }
        if constexpr (i >= 10 && i < 13) { wa[i - 9] = wa[0] ^ ((i - 9) * 32); pin(wa[i - 9]); }
      }
      if constexpr (ROLE == 0 && s == 3) kexp_op<16 + i>(c, kx);
      if constexpr (ROLE == 0 && s == 2) {
        if constexpr (i == 5) { pin(cs[0]); pin(cs[1]); pin(cs[2]); pin(cs[3]); csm.v = __float_as_uint(cs[0][0]); }
        if constexpr (i >= 5 && i < 8) { csm.v = (unsigned)__builtin_amdgcn_update_dpp((int)csm.v, (int)__float_as_uint(cs[i - 4][0]), 0xe4, 1 << (i - 4), 0xf, false); pin(csm.v); }
        if constexpr (i == 15) __builtin_amdgcn_raw_buffer_store_b32(csm.v, c.ws, l4, WS_CPART + (ft * 16 + p) * 1024 + v * 256, SC1);
      }
      if constexpr (ROLE == 0) {
        if constexpr (s == 5 && i == 15) lf = flag_sample(P, lfv, 0);
        if constexpr (s == 6 && (i & 3) == 0) cpr[i >> 2] = __builtin_amdgcn_raw_buffer_load_b128(c.ws, cpv, WS_CPART + ft * 16384 + (i >> 2) * 4096 + v * 256, SC1);
        if constexpr (s == 7) {
          if constexpr (i < 4) { cs4[i].v += __uint_as_float(cpr[3][i]); pin(cs4[i].v); }
          if constexpr (i == 4) { cs4[0].v = swap32_add(cs4[0].v, cs4[1].v); pin(cs4[0].v); }
          if constexpr (i == 5) { cs4[2].v = swap32_add(cs4[2].v, cs4[3].v); pin(cs4[2].v); }
#define CW 7
          if constexpr (i == 7) { cs4[0].v = swap16_add(cs4[0].v, cs4[2].v); pin(cs4[0].v); }
          if constexpr (i == CW) *LDSP(float, csa) = cs4[0].v;
          if constexpr (i == CW + 1) *LDSP(f4, sta) = f4{tk[0].v, tk[1].v, tk[2].v, tk[3].v};
        }
        if constexpr (s == 6 && i == 2) str = __builtin_amdgcn_raw_buffer_load_b32(c.ws, stv, WS_STATS + (p * 128 + 32 * v) * 4, SC1);
      }
      SB();
    });
    if constexpr (s == 0 && ROLE == 0) { asm volatile("" : "=v"(cs[0]), "=v"(cs[1]), "=v"(cs[2]), "=v"(cs[3])); }
    if constexpr (s < 2) colsum(F0, 2 * s);
    if constexpr (ROLE == 0 && s < 5) {
      int ln = lane; pin(ln);
      Ctx d = c; d.lane = ln; d.n = ln & 15; d.q = ln >> 4;
      __builtin_amdgcn_s_setprio(0);
      if constexpr (s == 0) { ktab_pre<0>(d, kp); ktab_skip(d, kp, kt, kin.dsk); l4 = ln * 4; pin(l4); }
      if constexpr (s == 1) kexp_pre<0>(d, kx);
      if constexpr (s == 2) kexp_pre<1>(d, kx);
      if constexpr (s == 3) {
        tagv = c.tag; one0 = flag_off(ln); pin(tagv); pin(one0);
        const unsigned f0 = WS_FC + (v * 14 + ft) * 2048, f1 = WS_FLN + 2048 * v - 2048, f2 = WS_FLN + (4 * p + v) * 128;
        lfv = ln < 32 ? ln * 128 + (ln < 16 ? f0 : f1) : f2; pin(lfv);
      }
      if constexpr (s == 4) {
        cpv = d.q * 1024 + d.n * 16; stv = l4 & 124; sta = LST + 512 * v + 4 * stv;
        csa = LCS + v * 256 + d.n * 16 + (((d.q & 1) << 1) | (d.q >> 1)) * 4; uv = u_off(d);
        pin(cpv); pin(stv); pin(sta); pin(csa); pin(uv);
      }
      __builtin_amdgcn_s_setprio(1);
      SB();
    }
    if constexpr (s == 3 && ROLE == 0) waitv<(feed_younger(s) < FEED_N[5] + FEED_N[6] ? feed_younger(s) : FEED_N[5] + FEED_N[6])>();
    else if constexpr (s < 6) waitv<feed_younger(s) + (ROLE == 0 && (s == 2 || s == 5))>();
    else if constexpr (ROLE == 1) waitv<12>();
    bar();
    if constexpr (s == 0) { STAMP(3); }
    if constexpr (s == 2) { STAMP(4); }
    if constexpr (s == 5) { STAMP(5); }
    if constexpr (s == 6) { STAMP(6); }
    if constexpr (s == 7) { STAMP(1); }
    SB();
#define PRI 0x60
    if constexpr (ROLE == 1) __builtin_amdgcn_s_setprio(1 + ((PRI >> s) & 1));
    sfor<0, 16>(LAM(I) {
      constexpr int i = CV(I);
      mma(acc[i >> 2][i & 3], cat(Fb.a[i & 3][0], Fb.a[i & 3][1]), Fb.b[i >> 2]);
      if constexpr (s < 7 && i != 4 && i != 9 && i < 14) rd(F0, i - (i > 4) - (i > 9), s + 1, 0);
      feed(IC<2 * s + 1>{}, I);
      if constexpr (s == 0 && i == 0) mma0(kt.t, KA[0], KB[0]);
      if constexpr (s == 0 && i > 0 && !(i & 1)) mma(kt.t, KA[i >> 1], KB[i >> 1]);
      if constexpr (s == 1 && i < KT_OPS) ktab_op<ROLE, i>(c, kp, kt);
      if constexpr (ROLE == 0 && s == 3 && i == 0) __builtin_amdgcn_raw_buffer_store_b64(tagv, c.ws, one0, WS_FC + ((v * 14 + ft) * 16 + p) * 128, SC1);
      if constexpr (ROLE == 0 && (s == 2 || s == 3) && 32 * (s - 2) + i < KX_OPS) kexp_op<32 * (s - 2) + i>(c, kx);
      if constexpr (s == 4 && i == 0) { ga.v = q * 16 + (LCS + v * 256); pin(ga.v); }
      if constexpr (s == 4 && i == 2) { gb.v = n * 16 + (LST + ((ROLE ^ v) & 1) * 1024); pin(gb.v); }
      if constexpr (s == 7 && (i & 1)) { if constexpr (i & 2) tc[i >> 2] = *LDSP(f4, gb.v + (i >> 2) * 256); else cv[i >> 2] = *LDSP(f4, ga.v + (i >> 2) * 64); }
      if constexpr (ROLE == 1 && s == 7) W1[i >> 3][(i >> 1) & 3][i & 1] = trd(wa[(i >> 1) & 3] + (i >> 3) * 16384 + (i & 1) * 2048);
      if constexpr (ROLE == 0) {
        if constexpr (s == 5 && i == 15) {
          asm volatile("s_waitcnt vmcnt(0)" : "+v"(lf)::"memory");
          if (!flags_up(c.tag, lf)) wait_flags<4>(c.ws, c.tag, ld_flag(c.ws, lfv, 0), lfv, 0);
        }
        if constexpr (s == 7 && (i == 0 || i == 2)) ur[i >> 1] = u_load(c, uv, i >> 1);
        if constexpr (s == 6 && i < 8) {
          if constexpr (i == 0) { tk[3].v = bhi(str); pin(tk[3].v); }
          if constexpr (i == 1) { tr.v = __builtin_amdgcn_rcpf(tk[3].v); pin(tr.v); }
          if constexpr (i == 2) { tk[0].v = blo(str); pin(tk[0].v); }
          if constexpr (i == 3) { tk[1].v = (-2.f * 0.7978845608028654f * 1.4426950408889634f) * tr.v; pin(tk[1].v); }
          if constexpr (i == 4) { tr.v *= tr.v; pin(tr.v); }
          if constexpr (i == 5) { tk[3].v *= tk[3].v; pin(tk[3].v); }
          if constexpr (i == 6) { tr.v *= 0.044715f; pin(tr.v); }
          if constexpr (i == 7) { tk[2].v = tr.v * tk[1].v; pin(tk[2].v); }
        }
        if constexpr (s == 6 && i >= 8) { constexpr int e = i & 3, k = (i - 8) >> 2; cs4[e].v = (k ? cs4[e].v : __uint_as_float(cpr[0][e])) + __uint_as_float(cpr[k + 1][e]); pin(cs4[e].v); }
      }
      SB();
    });
    if constexpr (s < 2) colsum(Fb, 2 * s + 1);
  });
  __builtin_amdgcn_s_setprio(0);
  STAMP(7);
}

template <int ROLE>
DEV void geglu(const Ctx& c, f4 (&acc)[4][4], const f4 (&cv)[4], const f4 (&tc)[4]) {
  const int p = (c.xc & 3) * 4 + (c.jj & 3), ft = (c.xc >> 2) * 7 + (c.jj >> 2);
  const unsigned hbase = WS_H + uni((2 * p + ((ROLE ^ c.v) & 1)) * 56 + 4 * ft + c.v) * 4096;
#pragma unroll
  for (int i = 0; i < 16; i++) pin(acc[i >> 2][i & 3]);
#pragma unroll
  for (int tt = 0; tt < 4; tt++) {
    const f2 nm = -f2{tc[tt][0], tc[tt][0]}, k0 = {tc[tt][1], tc[tt][1]}, k1 = {tc[tt][2], tc[tt][2]}, s2 = {tc[tt][3], tc[tt][3]};
    f2 a[4], b[4], z[4];
    auto half = [](const f4& x, int e) { return f2{x[2 * e], x[2 * e + 1]}; };
#define STEP(X) _Pragma("unroll") for (int j = 0; j < 4; j++) { X; } SB();
    STEP(a[j] = fma2(nm, half(cv[j >> 1], j & 1), half(acc[tt][j >> 1], j & 1)))
    STEP(z[j] = a[j] * a[j])
    STEP(z[j] = fma2(z[j], k1, k0))
    STEP(z[j] *= a[j])
    STEP(z[j] = (f2{__builtin_amdgcn_exp2f(z[j].x), __builtin_amdgcn_exp2f(z[j].y)}))
    STEP(b[j] = fma2(nm, half(cv[2 + (j >> 1)], j & 1), half(acc[tt][2 + (j >> 1)], j & 1)))
    STEP(z[j] = fma2(z[j], s2, s2))
    STEP(z[j] = (f2{__builtin_amdgcn_rcpf(z[j].x), __builtin_amdgcn_rcpf(z[j].y)}))
    STEP(a[j] *= b[j])
    STEP(a[j] *= z[j])
#undef STEP
    st128(u4{pk(a[0]), pk(a[1]), pk(a[2]), pk(a[3])}, c.ws, c.lane * 16 + tt * 1024, hbase);
    SB();
  }
}
template <int ROLE, int YOUNGER> DEV void h_publish(const Ctx& c) {
  const int p = (c.xc & 3) * 4 + (c.jj & 3), ft = (c.xc >> 2) * 7 + (c.jj >> 2);
  waitv<YOUNGER>();
  st_flag(c.ws, c.tag, WS_FH + uni((2 * p + ((ROLE ^ c.v) & 1)) * 56 + 4 * ft + c.v) * 128, c.lane);
}

DEV void ln_block(Ctx& c, const Params& P, KIn kin) {
  const int tok0 = (c.xc & 3) * 512 + (c.xc >> 2) * 256 + (c.jj - 28) * 64;
  const i4 sx = mksrd(P.x, L * D * 2);
  u4 rows[8];
#pragma unroll
  for (int i = 0; i < 8; i++) rows[i] = row_load(sx, c.lane * 16, (tok0 + 32 * (i >> 2) + 4 * c.w + (i & 3)) * 1024);
  if (threadIdx.x < 12) *LDSP(unsigned, LSY + threadIdx.x * 4) = 0u;
  kgen_take<0, 8>(c, P, kin);
  asm volatile("s_waitcnt vmcnt(4)" : "+v"(rows[0]), "+v"(rows[1]), "+v"(rows[2]), "+v"(rows[3])::"memory");
  STAMP(1);
  ln_compute(c, rows, 0, tok0);
  asm volatile("s_waitcnt vmcnt(0)" : "+v"(rows[4]), "+v"(rows[5]), "+v"(rows[6]), "+v"(rows[7])::"memory");
  bar();
  ln_store(c, 0, tok0);
  STAMP(2);
  ln_compute(c, rows + 4, 34816, tok0 + 32);
  waitv<0>();
  bar();
  if (c.w == 0) st_flag(c.ws, c.tag, WS_FLN + (tok0 >> 5) * 128, c.lane);
  ln_store(c, 34816, tok0 + 32);
  STAMP(3);
  waitv<0>();
  bar();
  if (c.w == 0) st_flag(c.ws, c.tag, WS_FLN + ((tok0 >> 5) + 1) * 128, c.lane);
  STAMP(4);
#define NW 4
  const i4 swd = mksrd(P.wd, F * D * 2);
  const unsigned wq = (c.jj - 28) * 448 * 1024;
  bf8 KA[8], KB[8]; KTab kt;
  KPre kp;
  auto warm = LAM(H) {
    constexpr int h = CV(H);
    if constexpr (h == 0 && NW < 7) { if (c.w >= 4 && c.v < 7 - NW) touch(swd, c.lane * 128, wq + NW * 65536 + (c.xc * (7 - NW) + c.v) * 8192); }
    if constexpr (h < NW) {
      if ((c.w >> 2) == (h & 1)) { unsigned k = h + ((c.xc >> 1) * NW >> 2); k = k >= NW ? k - NW : k;
        touch(swd, c.lane * 128, wq + (1 - TILE_P(c)) * 32768 + k * 65536 + c.v * 8192); }
    }
  };
  if (c.w < 4) { kgen_pre<0>(c, kp); kgen_rows<0>(c, kp, kin, KA, warm); } else { kgen_pre<1>(c, kp); kgen_rows<1>(c, kp, kin, KA, warm); }
  bar();
  kgen_cols(c, KB); kgen_mma(kt.t, KA, KB); mma_fence();
  if (c.w < 4) { ktab_pre<0>(c, kp); ktab_skip(c, kp, kt, kin.dsk); sfor<0, KT_OPS>(LAM(K) { ktab_op<0, CV(K)>(c, kp, kt); }); }
  else { ktab_pre<1>(c, kp); sfor<0, KT_OPS>(LAM(K) { ktab_op<1, CV(K)>(c, kp, kt); }); }
  bar();
  STAMP(5);
  asm volatile("fused_end_a:");
}

struct CFr { bf4 a[8][2]; bf8 b[8]; };
DEV void conv_rd(CFr& Fr, int i, unsigned ka, unsigned ua, int sp) {
  if (i < 2) Fr.a[0][i] = trd(ka + 64 * sp + (i ? 0 : 8));
  else if (i < 10) Fr.b[i - 2] = *LDSP(bf8, ua + 512 * (i - 2) + 192 - 64 * sp);
  else { const int g = (i - 8) >> 1, t = i & 1; Fr.a[g][t] = trd(ka + 512 * g + 64 * sp + (t ? 0 : 8)); }
}
DEV void conv(Ctx& c) {
  const int chl = c.v & 1, kh = c.v >> 1, n = c.n, q = c.q;
  unsigned ka = LKC + chl * KCH + (n >> 2) * KCS + 2 * (16 + 4 * (n & 3) + 128 * kh + 16 - 8 * q) - 8;
  unsigned ua = LU + chl * UCH + 2 * (256 + 16 * n - 16 - 128 * kh + 8 * q) - 192;
  asm volatile("" : "+v"(ka), "+v"(ua));
  f4 acc[8];
  CFr Fr[2];
#pragma unroll
  for (int i = 0; i < 24; i++) conv_rd(Fr[0], i, ka, ua, 0);
  unsigned xw = LKC + chl * KCH + 32 + (c.lane >> 3) * 512 + (c.lane & 7) * 16, yo = (q & 1) * 512 + n * 32 + (q >> 1) * 16;
  asm volatile("" : "+v"(xw), "+v"(yo));
  lds_wait<1>(LSY + 44, 4);
  if (c.jj < 4 && c.w == 0) st_flag(c.ws, c.tag, WS_FG + (c.xc * 4 + c.jj) * 128, c.lane);
  STAMP(11);
  SB();
#pragma unroll
  for (int sp = 0; sp < 4; sp++) {
    int i = 0;
#pragma unroll
    for (int g = 0; g < 8; g++)
#pragma unroll
      for (int m = 0; m + g < 8; m++) {
        if (sp == 0 && g == 0) mma0(acc[m], cat(Fr[0].a[0][0], Fr[0].a[0][1]), Fr[0].b[m]);
        else mma(acc[g + m], cat(Fr[sp & 1].a[g][0], Fr[sp & 1].a[g][1]), Fr[sp & 1].b[m]);
        if (sp < 3 && i < 24) conv_rd(Fr[(sp + 1) & 1], i, ka, ua, sp + 1);
        i++;
        SB();
      }
  }
  mma_fence();
#pragma unroll
  for (int i = 0; i < 8; i++) asm volatile("" : "+v"(acc[i]));
  unsigned pkd[4][2];
  auto finish = [&](auto KH) __attribute__((always_inline)) {
    constexpr int h = CV(KH);
#pragma unroll
    for (int j = 0; j < 4; j++) *LDSP(f4, xw + h * 256 + j * KCS) = acc[4 * (1 - h) + j];
    lds_post(LSY + 16 + c.v * 4, c.lane);
    STAMP(12);
    lds_wait<0>(LSY + 16 + (c.v ^ 2) * 4, 1);
#pragma unroll
    for (int j = 0; j < 4; j++) { const f4 t = acc[4 * h + j] + *LDSP(f4, xw + (1 - h) * 256 + j * KCS); pkd[j][0] = pk(f2{t[0], t[1]}); pkd[j][1] = pk(f2{t[2], t[3]}); }
  };
  if (kh) finish(IC<1>{}); else finish(IC<0>{});
  const int ch = 2 * (c.xc * 32 + c.jj) + chl;
#pragma unroll
  for (int j = 0; j < 4; j += 2) {
    swap16(pkd[j][0], pkd[j + 1][0]); swap16(pkd[j][1], pkd[j + 1][1]);
    st128(u4{pkd[j][0], pkd[j][1], pkd[j + 1][0], pkd[j + 1][1]}, c.ws,
                                           yo + j * 512, WS_YT + ch * 4096 + kh * 2048);
  }
  STAMP(13);
  waitv<0>();
  STAMP(14);
  st_flag(c.ws, c.tag, WS_FY + (kh * 512 + ch) * 128, c.lane);
}
DEV u4 x_load(const Ctx& c, const Params& P, int role, unsigned oa) {
  return __builtin_amdgcn_raw_buffer_load_b128(mkrs(P.x, L * D * 2), oa, (TILE_T(c) * 64 + 16 * c.v + 8 * role) * 1024 + TILE_C(c) * 128, 0);
}
DEV void y_gather(Ctx& c) {
  const int T64 = TILE_T(c), ct = TILE_C(c), l = c.lane;
  const int r0 = 32 * (c.v & 1) + 16 * (c.v >> 1);
  const unsigned fo = WS_FY + ((T64 >> 4) * 512 + 64 * ct + r0) * 128;
  unsigned fv = (l < 16 ? l : 15) * 128, yv = (l >> 3) * 4096 + (l & 7) * 16, ya = LU + (c.v & 1) * (UCH + 64) + (c.v >> 1) * 2080 + l * 16;
  asm volatile("" : "+v"(fv), "+v"(yv), "+v"(ya));
  lds_wait<2>(LSY + 40, 1);
  wait_flags<8>(c.ws, c.tag, ld_flag(c.ws, fv, fo), fv, fo);
  STAMP(15);
#pragma unroll
  for (int i = 0; i < 2; i++)
    *LDSP(u4, ya + i * 1040) = __builtin_amdgcn_raw_buffer_load_b128(c.ws, yv, WS_YT + (64 * ct + r0 + 8 * i) * 4096 + T64 * 128, SC1);
}
struct Fin { unsigned a0, a1, ya, oa; F1 s[8], y[8]; };
template <int ROLE> DEV void finish_addr(const Ctx& c, Fin& f) {
  const int t = c.lane >> 3, c8 = c.lane & 7;
  f.a0 = c.v * 16384 + (8 * ROLE + t) * 256 + ((2 * c8) ^ t) * 16;
  f.ya = LU + (c8 >> 2) * (UCH + 64) + (c8 & 3) * 1040 + (16 * c.v + 8 * ROLE + t) * 2;
  f.oa = t * 1024 + c8 * 16;
  pin(f.a0); pin(f.ya); pin(f.oa);
}
template <int k> DEV void finish_op(Fin& f, const u4& x) {
  if constexpr (k < 8) { f.s[k].v = (k & 1) ? bhi(x[k >> 1]) : blo(x[k >> 1]); pin(f.s[k].v); }
  else if constexpr (k < 16) asm volatile("v_mov_b32 %0, 0" : "=v"(f.y[k - 8].v));
  else { f.a1 = f.a0 ^ 16; pin(f.a1); }
}
template <int ROLE> DEV void finish_tile(Ctx& c, const Params& P, Fin f) {
  const int T64 = TILE_T(c), ct = TILE_C(c);
  const rsrc_t ro = mkrs(P.out, L * D * 2);
  bar();
  if (ROLE == 1) { STAMP(15); }
  float y[8] = {f.y[0].v, f.y[1].v, f.y[2].v, f.y[3].v, f.y[4].v, f.y[5].v, f.y[6].v, f.y[7].v};
  f4 sa = {f.s[0].v, f.s[1].v, f.s[2].v, f.s[3].v}, sb = {f.s[4].v, f.s[5].v, f.s[6].v, f.s[7].v};
  asm volatile("ds_read_u16_d16_hi %0, %8\n\tds_read_u16_d16_hi %1, %8 offset:128\n\tds_read_u16_d16_hi %2, %8 offset:256\n\tds_read_u16_d16_hi %3, %8 offset:384\n\t"
               "ds_read_u16_d16_hi %4, %8 offset:512\n\tds_read_u16_d16_hi %5, %8 offset:640\n\tds_read_u16_d16_hi %6, %8 offset:768\n\tds_read_u16_d16_hi %7, %8 offset:896"
               : "+v"(y[0]), "+v"(y[1]), "+v"(y[2]), "+v"(y[3]), "+v"(y[4]), "+v"(y[5]), "+v"(y[6]), "+v"(y[7]) : "v"(f.ya) : "memory");
  f4 pa[4], pb[4];
#pragma unroll
  for (int k = 0; k < 4; k++) { pa[k] = *LDSP(f4, f.a0 + k * 4096); pb[k] = *LDSP(f4, f.a1 + k * 4096); }
  SB();
#pragma unroll
  for (int k = 0; k < 4; k++) { sa += pa[k]; sb += pb[k]; }
  asm volatile("s_waitcnt lgkmcnt(0)" : "+v"(y[0]), "+v"(y[1]), "+v"(y[2]), "+v"(y[3]), "+v"(y[4]), "+v"(y[5]), "+v"(y[6]), "+v"(y[7])::"memory");
  sa += f4{y[0], y[1], y[2], y[3]}; sb += f4{y[4], y[5], y[6], y[7]};
  st128(u4{pk(f2{sa[0], sa[1]}), pk(f2{sa[2], sa[3]}), pk(f2{sb[0], sb[1]}), pk(f2{sb[2], sb[3]})}, ro, f.oa,
                                         (T64 * 64 + 16 * c.v + 8 * ROLE) * 1024 + ct * 128);
}

DEV u2 hflags_sample(const Ctx& c, const Params& P) {
  return flag_sample(P, (TILE_T(c) * 56 + 14 * c.v + TILE_P(c) + 2 * (c.lane < 7 ? c.lane : 6)) * 128, WS_FH);
}
DEV void gemm2(Ctx& c, const Params& P, const bf4 (&W1)[7][4][2], u2 hfl, unsigned (&wa)[4], Fin& fin, unsigned pw0) {
  const int T64 = TILE_T(c), v = c.v;
  unsigned l16 = c.lane * 16;
  asm volatile("" : "+v"(l16));
  const unsigned hk = uni(T64 * 56 + 14 * v + TILE_P(c));
  const unsigned fo = WS_FH + hk * 128, fo2 = WS_FH + (hk ^ 1) * 128, fv = (l16 < 112 ? l16 : 96) * 16;
  if (!flags_up(c.tag, hfl)) wait_flags<2>(c.ws, c.tag, ld_flag(c.ws, fv, fo), fv, fo);
  STAMP(12);
#define GWIN 2
  constexpr int WIN = GWIN;
  u4 H[14][4];
  auto hload = [&](int i, int t) __attribute__((always_inline)) {
    H[i][t] = __builtin_amdgcn_raw_buffer_load_b128(c.ws, l16 + t * 1024, WS_H + ((i < 7 ? hk : hk ^ 1) + 2 * (i % 7)) * 4096, SC1);
  };
#pragma unroll
  for (int i = 0; i < WIN; i++)
#pragma unroll
    for (int t = 0; t < 4; t++) hload(i, t);
  SB();
  f4 acc[4][4];
  bf4 W2[7][4][2];
  unsigned pw[4]; u4 xr;
  sfor<0, 14>(LAM(I) {
    constexpr int i = CV(I);
    sfor<0, 16>(LAM(J) {
      constexpr int j = CV(J), t = j >> 2, d = j & 3;
      if constexpr (i == 0) mma0(acc[t][d], cat(W1[0][d][0], W1[0][d][1]), H[0][t]);
      else if constexpr (i < 7) mma(acc[t][d], cat(W1[i][d][0], W1[i][d][1]), H[i][t]);
      else mma(acc[t][d], cat(W2[i - 7][d][0], W2[i - 7][d][1]), H[i][t]);
#define HS2 33
      if constexpr (i * 16 + j == HS2) hfl = ld_flag(c.ws, fv, fo2);
      if constexpr (i == 7 - WIN && j == 2) { if (!flags_up(c.tag, hfl)) wait_flags<2>(c.ws, c.tag, ld_flag(c.ws, fv, fo2), fv, fo2); STAMP(16); }
      if constexpr (i + WIN < 14 && (j & 3) == 3) hload(i + WIN, t);
      if constexpr (i >= 6 && i < 13 && (j & 1) == 0) W2[i - 6][j >> 2][(j >> 1) & 1] = trd(wa[j >> 2] + (i < 9 ? i - 6 : i - 9) * 16384 + ((j >> 1) & 1) * 2048);
#define YSIG 11
      if constexpr (i == YSIG && j == 14) *LDSP(volatile unsigned, LSY + 40) = 1u;
      if constexpr (i == 5 && (j & 3) == 1) { wa[t] += SLOT_C; pin(wa[t]); }
      if constexpr ((i == 8 && (j & 3) == 3)) { wa[t] -= SLOT_C; pin(wa[t]); }
      if constexpr (i == 6 && j == 1) xr = x_load(c, P, 1, fin.oa);
      if constexpr (i >= 10 && (j & 1) && 8 * (i - 10) + (j >> 1) < 17) finish_op<8 * (i - 10) + (j >> 1)>(fin, xr);
      if constexpr (i == 12 && (j & 3) == 3) { pw[t] = pw0 ^ (t * 64); pin(pw[t]); }
      if constexpr (i == 13 && j >= 8) { pin(acc[t - 2][d]); *LDSP(f4, pw[d] + (t - 2) * 16384) = acc[t - 2][d]; }
      SB();
    });
  });
  STAMP(13);
  asm volatile("s_nop 1");
#pragma unroll
  for (int j = 8; j < 16; j++) { pin(acc[j >> 2][j & 3]); *LDSP(f4, pw[j & 3] + (j >> 2) * 16384) = acc[j >> 2][j & 3]; if (j == 11) asm volatile("s_nop 3"); SB(); }
  STAMP(14);
  finish_tile<1>(c, P, fin);
}

__global__ void __launch_bounds__(512, 2) fused_s4_geglu(Params P) {
  Ctx c;
  STAMP(0);
  c.w = __builtin_amdgcn_readfirstlane(threadIdx.x >> 6);
  c.v = c.w & 3; c.lane = threadIdx.x & 63; c.n = c.lane & 15; c.q = c.lane >> 4;
  c.xc = blockIdx.x & 7; c.jj = blockIdx.x >> 3;
  {
    u64 pc; unsigned len;
    asm volatile("s_getpc_b64 %0\n.Lpc%=:\n\ts_mov_b32 %1, fused_end_a-.Lpc%=\n\ts_max_u32 %1, %1, fused_end_b-.Lpc%=" : "=s"(pc), "=s"(len)::"scc");
    if (c.w == 3) touch(mksrd((const void*)pc, len), (c.lane >> CPF) * 128, (c.jj & ((8 << CPF) - 1)) * (8192 >> CPF));
  }
  const bool g1 = c.jj < 28;
  const G1Src gs = g1_src(c);
  const KIn kin = kgen_load<0>(c, P);
  if (__builtin_expect(!g1, 0)) ln_block(c, P, kin);
  if (c.w >= 4) {
    WdSrc wds;
    f4 acc[4][4], cv[4], tc[4]; bf4 W1[7][4][2]; unsigned wa[4];
    if (__builtin_expect(g1, 1)) { KExp kx; u4 ur[2]; gemm1<1>(c, P, gs, kin, wds, acc, W1, wa, kx, ur, cv, tc); }
    else {
      wds = wd_src(c, P);
      lds_arrive(LSY + 44, c.lane);
#pragma unroll
      for (int i = 0; i < 7; i++) wd_chunk(c, wds, i);
#pragma unroll
      for (int d = 0; d < 4; d++) { wa[d] = (wd_raddr(c.v, c.n, c.q) ^ (d * 32)) + SLOT_C; pin(wa[d]); }
      waitv<16>();
#pragma unroll
      for (int i = 0; i < 16; i++) W1[i >> 3][(i >> 1) & 3][i & 1] = trd(wa[(i >> 1) & 3] + (i >> 3) * 16384 + (i & 1) * 2048);
      waitl();
    }
#define NPRE 2
#define NPUB 2
    auto w1_read = [&](int i) __attribute__((always_inline)) {
      if (i == 3) {
#pragma unroll
        for (int d = 0; d < 4; d++) { wa[d] -= SLOT_C; pin(wa[d]); }
      }
#pragma unroll
      for (int k = 0; k < 8; k++) W1[i][k >> 1][k & 1] = trd(wa[k >> 1] + (i < 3 ? i : i - 3) * 16384 + (k & 1) * 2048);
      SB();
    };
#pragma unroll
    for (int i = 2; i < NPRE; i++) { if (i == 3) waitv<4>(); if (i == 6) waitv<0>(); w1_read(i); }
    STAMP(8);
    if (g1) { geglu<1>(c, acc, cv, tc); waitv<4>(); } else waitv<0>();
    lds_arrive(LSY + 44, c.lane);
    STAMP(9);
    u2 hfl; Fin fin; unsigned pw0;
#pragma unroll
    for (int i = 0; i < 7; i++) {
      if (i + 2 >= NPRE && i + 2 < 7) w1_read(i + 2);
      if (i >= NPRE) { if (i < 5) asm volatile("s_waitcnt lgkmcnt(15)" ::: "memory"); else if (i == 5) asm volatile("s_waitcnt lgkmcnt(8)" ::: "memory"); else waitl(); }
      if (i == 5) {
#define HD 8
        if (!g1) {
          const unsigned go = WS_FG + (c.xc * 4 + c.jj - 28) * 128;
          wait_flags<4>(c.ws, c.tag, ld_flag(c.ws, 0, go), 0, go);
          __builtin_amdgcn_s_sleep(HD);
        }
        hfl = hflags_sample(c, P);
      }
      wd_chunk(c, wds, 7 + i);
      if (i == NPUB - 1) {
        finish_addr<1>(c, fin); pw0 = c.v * 4096 + c.n * 256 + (c.q ^ (c.n & 7)) * 16; pin(pw0);
        if (g1) h_publish<1, 4 * NPUB>(c);
        STAMP(10);
      }
      SB();
    }
    asm volatile("s_waitcnt vmcnt(8)" : "+v"(hfl)::"memory");
    STAMP(11);
    gemm2(c, P, W1, hfl, wa, fin, pw0);
    asm volatile("fused_end_b:");
  } else {
    if (__builtin_expect(g1, 1)) {
      f4 acc[4][4], cv[4], tc[4]; WdSrc none; bf4 W1[7][4][2]; unsigned wa[4]; KExp kx; u4 ur[2];
      gemm1<0>(c, P, gs, kin, none, acc, W1, wa, kx, ur, cv, tc);
      geglu<0>(c, acc, cv, tc);
      STAMP(8);
      sfor<0, 9>(LAM(K) { kexp_put<CV(K)>(c, kx); });
      u_write(c, ur[0], ur[1]);
    } else {
      const unsigned fv = (16 * c.v + (c.lane >> 2)) * 128;
      wait_flags<8>(c.ws, c.tag, ld_flag(c.ws, fv, WS_FLN), fv, WS_FLN);
      const u4 r0 = u_load(c, u_off(c), 0), r1 = u_load(c, u_off(c), 1);
      KExp kx;
      kexp_pre<0>(c, kx); kexp_pre<1>(c, kx);
      sfor<0, KX_OPS>(LAM(K) { kexp_op<CV(K)>(c, kx); });
      sfor<0, 9>(LAM(K) { kexp_put<CV(K)>(c, kx); });
      u_write(c, r0, r1);
    }
    lds_arrive(LSY, c.lane);
    STAMP(9);
    if (g1) h_publish<0, 0>(c);
    STAMP(10);
    lds_wait<1>(LSY, 4);
    Fin fin;
    finish_addr<0>(c, fin);
    const u4 xr = x_load(c, P, 0, fin.oa);
    conv(c);
    if (__builtin_expect(blockIdx.x == 224 && c.w == 0, 0)) {
#pragma unroll 1
      for (int i = 0; i < 4; i++) {
        const int b = 64 * i + c.lane;
        const unsigned fv = b < 224 ? WS_FC + b * 128 : WS_FLN + (b - 224) * 256;
        wait_flags<16>(c.ws, c.tag, ld_flag(c.ws, fv, 0), fv, 0);
      }
      st_flag(c.ws, c.tag, WS_EPOCH, c.lane);
    }
    sfor<0, 17>(LAM(K) { finish_op<CV(K)>(fin, xr); });
    y_gather(c);
    finish_tile<0>(c, P, fin);
  }
  FINISH();
}

extern "C" void kernel_launch(void* const* d_in, const int* in_sizes, int n_in, void* d_out, int out_size, void* d_ws, size_t ws_size,
                              hipStream_t stream) {
  Params P;
  P.x = (const u16*)d_in[0]; P.are = (const u16*)d_in[1]; P.aim = (const u16*)d_in[2]; P.bre = (const u16*)d_in[3];
  P.bim = (const u16*)d_in[4]; P.cre = (const u16*)d_in[5]; P.cim = (const u16*)d_in[6]; P.ldt = (const u16*)d_in[7];
  P.dsk = (const u16*)d_in[8]; P.wg = (const u16*)d_in[10]; P.wu = (const u16*)d_in[11]; P.wd = (const u16*)d_in[12];
  P.out = (u16*)d_out; P.ws = (char*)d_ws;
  (void)hipFuncSetAttribute((const void*)fused_s4_geglu, hipFuncAttributeMaxDynamicSharedMemorySize, LDS_BYTES);
  fused_s4_geglu<<<256, 512, LDS_BYTES, stream>>>(P);
}
```

```cpp
#define AMPK 3
#include <hip/hip_runtime.h>
#include <hip/hip_bf16.h>
#include <stdint.h>
#pragma clang diagnostic ignored "-Wint-to-pointer-cast"

typedef unsigned long long u64;
typedef unsigned short u16;
typedef __bf16 bf2 __attribute__((ext_vector_type(2)));
typedef __bf16 bf4 __attribute__((ext_vector_type(4)));
typedef __bf16 bf8 __attribute__((ext_vector_type(8)));
typedef float f2 __attribute__((ext_vector_type(2)));
typedef float f4 __attribute__((ext_vector_type(4)));
typedef int i4 __attribute__((ext_vector_type(4)));
typedef unsigned u2 __attribute__((ext_vector_type(2)));
typedef unsigned u4 __attribute__((ext_vector_type(4)));
typedef __amdgpu_buffer_rsrc_t rsrc_t;
#define LDSP(T, a) ((__attribute__((address_space(3))) T*)(a))
#define DEV __device__ __forceinline__
#define SB() __builtin_amdgcn_sched_barrier(0)
#define SC1 16

template <int V> struct IC { static constexpr int value = V; };
template <int B, int E, class Fn> DEV void sfor(Fn&& f) { if constexpr (B < E) { f(IC<B>{}); sfor<B + 1, E>(f); } }
#define LAM(I) [&](auto I) __attribute__((always_inline))
#define CV(I) decltype(I)::value

constexpr int L = 2048, D = 512, NS = 128, F = 1792;

constexpr unsigned WS_EPOCH = 0;
constexpr unsigned WS_FLN = 8192;
constexpr unsigned WS_FG = 4096;
constexpr unsigned WS_STATS = 16384;
constexpr unsigned WS_CPART = 65536;
constexpr unsigned WS_FC = 327680;
constexpr unsigned WS_FH = 524288;
constexpr unsigned WS_FY = 786432;
constexpr unsigned WS_XNT = 1u << 20;
constexpr unsigned WS_YT = 3u << 20;
constexpr unsigned WS_H = 5u << 20;
constexpr unsigned WS_BYTES = WS_H + 32u * 56u * 4096u;
constexpr unsigned WS_DBG = 16u << 20;

constexpr unsigned SLOT_C = 98816;
constexpr unsigned slot_of(int s) { return s % 3 == 0 ? 0u : s % 3 == 1 ? 49152u : SLOT_C; }
constexpr unsigned wd_base(int i) { return i < 3 ? SLOT_C + i * 16384 : (i - 3) * 16384; }
constexpr unsigned LKC = 65536;
constexpr unsigned KCS = 4160, KCH = 4 * KCS;
constexpr unsigned LKG = 98304;
constexpr unsigned LU = 147968;
constexpr unsigned UCH = 4608;
constexpr unsigned LCS = LU + 9216;
constexpr unsigned LST = LU + 10240;
constexpr unsigned LKT = 160064;
constexpr unsigned LSY = 163136;
constexpr unsigned LJK = 163328;
constexpr unsigned LDS_BYTES = 163840;

struct Params {
  const u16 *are, *aim, *ldt, *dsk, *bre, *bim, *cre, *cim;
  char* ws;
  const u16 *x, *wg, *wu, *wd;
  u16* out;
};

DEV i4 mksrd(const void* p, unsigned bytes) {
  u64 a = (u64)p; i4 r; r[0] = (int)(unsigned)a; r[1] = (int)(unsigned)(a >> 32); r[2] = (int)bytes; r[3] = 0x00020000; return r;
}
DEV rsrc_t mkrs(const void* p, unsigned bytes) { return __builtin_amdgcn_make_buffer_rsrc((void*)p, 0, (int)bytes, 0x00020000); }
DEV void glds(i4 srd, unsigned voff, unsigned soff, unsigned dst) {
  asm volatile("s_mov_b32 m0, %3\n\ts_nop 0\n\tbuffer_load_dwordx4 %1, %0, %2 offen lds" ::"s"(srd), "v"(voff), "s"(soff), "s"(dst) : "memory");
}
DEV void touch(i4 srd, unsigned voff, unsigned soff) {
  asm volatile("s_mov_b32 m0, %3\n\ts_nop 0\n\tbuffer_load_dword %1, %0, %2 offen lds" ::"s"(srd), "v"(voff), "s"(soff), "s"(LJK) : "memory");
}
DEV void st128(u4 v, rsrc_t rs, unsigned voff, unsigned soff) {
  __builtin_amdgcn_raw_buffer_store_b128(v, rs, voff, soff, SC1);
  asm volatile("s_nop 1" ::"v"(v) : "memory");
}
template <int N> DEV void waitv() { asm volatile("s_waitcnt vmcnt(%0)" ::"i"(N) : "memory"); }
DEV void waitl() { __builtin_amdgcn_s_waitcnt(0xC07F); asm volatile("" ::: "memory"); }
DEV void bar() { asm volatile("" ::: "memory"); __builtin_amdgcn_s_waitcnt(0xC07F); asm volatile("s_barrier" ::: "memory"); }
DEV void set_m0(unsigned base, unsigned imm) { asm volatile("s_add_u32 m0, %0, %1" ::"s"(base), "i"(imm) : "memory", "scc"); }
template <int OFF> DEV void glds_at(i4 srd, unsigned voff, unsigned soff) {
  asm volatile("buffer_load_dwordx4 %1, %0, %2 offen offset:%3 lds" ::"s"(srd), "v"(voff), "s"(soff), "i"(OFF) : "memory");
}
template <class T> DEV void pin(T& x) { asm volatile("" : "+v"(x)); }
template <class T> DEV void spin_(T& x) { asm volatile("" : "+s"(x)); }
DEV bf4 trd(unsigned a) { return __builtin_amdgcn_ds_read_tr16_b64_v4bf16(LDSP(bf4, a)); }
DEV bf8 cat(bf4 a, bf4 b) { return __builtin_shufflevector(a, b, 0, 1, 2, 3, 4, 5, 6, 7); }
template <class TA, class TB> DEV void mma(f4& c, TA a, TB b) { asm volatile("v_mfma_f32_16x16x32_bf16 %0, %1, %2, %0" : "+v"(c) : "v"(a), "v"(b)); }
template <class TA, class TB> DEV void mma0(f4& c, TA a, TB b) { asm volatile("v_mfma_f32_16x16x32_bf16 %0, %1, %2, 0" : "=&v"(c) : "v"(a), "v"(b)); }
DEV void mma_fence() { asm volatile("s_nop 7\n\ts_nop 1" ::: "memory"); }
DEV float blo(unsigned v) { return __uint_as_float(v << 16); }
DEV float bhi(unsigned v) { return __uint_as_float(v & 0xffff0000u); }
DEV f2 unpk(unsigned v) { return f2{blo(v), bhi(v)}; }
DEV unsigned pk(f2 v) { return __builtin_bit_cast(unsigned, __builtin_convertvector(v, bf2)); }
DEV float rbf(float v) { return blo(pk(f2{v, v})); }
DEV f2 fma2(f2 a, f2 b, f2 c) { return __builtin_elementwise_fma(a, b, c); }
template <int CTRL> DEV float dpp_add(float v) {
  return v + __builtin_bit_cast(float, __builtin_amdgcn_update_dpp(0, __builtin_bit_cast(int, v), CTRL, 0xf, 0xf, true));
}
DEV void swap16(unsigned& a, unsigned& b) { auto r = __builtin_amdgcn_permlane16_swap(a, b, false, false); a = r[0]; b = r[1]; }
DEV void swap32(unsigned& a, unsigned& b) { auto r = __builtin_amdgcn_permlane32_swap(a, b, false, false); a = r[0]; b = r[1]; }
DEV float swap16_add(float a, float b) { unsigned x = __float_as_uint(a), y = __float_as_uint(b); swap16(x, y); return __uint_as_float(x) + __uint_as_float(y); }
DEV float swap32_add(float a, float b) { unsigned x = __float_as_uint(a), y = __float_as_uint(b); swap32(x, y); return __uint_as_float(x) + __uint_as_float(y); }
DEV float red2(float a, float b) {
  float s = swap32_add(a, b);
  s = swap16_add(s, s);
  s = dpp_add<0x128>(s); s = dpp_add<0x124>(s); s = dpp_add<0x122>(s); s = dpp_add<0x121>(s);
  return s;
}
DEV float rdl(float v, int l) { return __builtin_bit_cast(float, __builtin_amdgcn_readlane(__builtin_bit_cast(int, v), l)); }
DEV unsigned uni(unsigned v) { return (unsigned)__builtin_amdgcn_readfirstlane((int)v); }
DEV u4 row_load(i4 srd, unsigned voff, unsigned soff) {
  u4 r; asm volatile("buffer_load_dwordx4 %0, %2, %1, %3 offen" : "=v"(r) : "s"(srd), "v"(voff), "s"(soff) : "memory"); return r;
}
constexpr unsigned SPIN_CAP = 1u << 15;
DEV u2 ld_flag(rsrc_t rs, unsigned voff, unsigned soff) { return __builtin_amdgcn_raw_buffer_load_b64(rs, voff, soff, SC1); }
DEV unsigned flag_off(int lane) { return lane < 16 ? lane * 8 : 0x7ffffff0u; }
DEV void st_flag(rsrc_t rs, u2 tag, unsigned soff, int lane) { __builtin_amdgcn_raw_buffer_store_b64(tag, rs, flag_off(lane), soff, SC1); }
DEV bool flags_up(u2 tag, u2 v) { return __builtin_amdgcn_ballot_w64(__builtin_bit_cast(u64, v) != __builtin_bit_cast(u64, tag)) == 0; }
template <int NAP> DEV void wait_flags(rsrc_t rs, u2 tag, u2 v, unsigned voff, unsigned soff) {
#pragma nounroll
  for (unsigned spin = 0; spin < SPIN_CAP; ++spin) {
    if (flags_up(tag, v)) break;
    __builtin_amdgcn_s_sleep(NAP);
    asm volatile("" ::: "memory");
    v = ld_flag(rs, voff, soff);
  }
  asm volatile("" : "+v"(v)::"memory");
}
DEV void lds_post(unsigned a, int lane) { asm volatile("" ::: "memory"); if (lane == 0) *LDSP(volatile unsigned, a) = 1u; }
template <int NAP> DEV void lds_wait(unsigned a, unsigned want) {
#pragma nounroll
  for (unsigned spin = 0; spin < (SPIN_CAP << 6); ++spin) {
    if (uni(*LDSP(volatile unsigned, a)) >= want) break;
    if (NAP) __builtin_amdgcn_s_sleep(NAP);
  }
  asm volatile("" ::: "memory");
}
DEV unsigned lds_arrive(unsigned a, int lane) {
  asm volatile("s_waitcnt lgkmcnt(0)" ::: "memory");
  unsigned o = 0;
  if (lane == 0) o = __hip_atomic_fetch_add(LDSP(unsigned, a), 1u, __ATOMIC_RELAXED, __HIP_MEMORY_SCOPE_WORKGROUP);
  return uni(o);
}

#define STAMP(i) do { if ((i) == AMPK && c.w >= 4) { u64 t_; asm volatile("s_memrealtime %0\n\ts_waitcnt lgkmcnt(0)" : "=s"(t_)::"memory"); c.tk = (unsigned)t_; } } while (0)
#define FINISH() do { asm volatile("s_waitcnt vmcnt(0)" ::: "memory"); STAMP(18); unsigned d_ = c.tk - c.t0; if (d_ > 5000 || c.jj >= 28) d_ = 0; \
  while ((unsigned)__builtin_amdgcn_s_memrealtime() - c.t0 < d_ * 50) __builtin_amdgcn_s_sleep(16); } while (0)

#define TILE_P(c) (c.xc & 1)
#define TILE_T(c) ((c.xc >> 1) * 8 + 2 * (c.jj & 3) + TILE_P(c))
#define TILE_C(c) (c.jj >> 2)
#define GPRIO 0
#define CPF 2
struct Ctx {
  int xc, jj, w, v, lane, n, q;
  u2 tag;
  rsrc_t ws; unsigned t0, tk;
};

struct alignas(16) F1 { float v; };
DEV float red4(float a, float b, float c, float d) {
  float s = swap16_add(swap32_add(a, b), swap32_add(c, d));
  s = dpp_add<0x128>(s); s = dpp_add<0x124>(s); s = dpp_add<0x122>(s); s = dpp_add<0x121>(s);
  return s;
}
DEV int ln_row(int r) { return r * 1088 + (r & 8) * 4; }
DEV void ln_compute(const Ctx& c, const u4* rows, unsigned img, int tok0) {
  f2 x[4][4]; float t[4];
#pragma unroll
  for (int k = 0; k < 4; k++) {
#pragma unroll
    for (int i = 0; i < 4; i++) x[k][i] = unpk(rows[k][i]);
    const f2 s = (x[k][0] + x[k][1]) + (x[k][2] + x[k][3]); t[k] = s.x + s.y;
  }
  float mean = red4(t[0], t[2], t[1], t[3]) * (1.f / D);
#pragma unroll
  for (int k = 0; k < 4; k++) {
    const float m = rdl(mean, 16 * k);
    f2 qq = {0.f, 0.f};
#pragma unroll
    for (int i = 0; i < 4; i++) { x[k][i] -= m; qq = fma2(x[k][i], x[k][i], qq); }
    t[k] = qq.x + qq.y;
  }
  float var = red4(t[0], t[2], t[1], t[3]) * (1.f / D);
  const unsigned st = pk(f2{mean, __builtin_amdgcn_sqrtf(rbf(rbf(var) + 1e-5f))});
  const float r = __builtin_amdgcn_rcpf(bhi(st)), o = (mean - blo(st)) * r;
#pragma unroll
  for (int k = 0; k < 4; k++) {
    const float rk = rdl(r, 16 * k), ok = rdl(o, 16 * k);
    u4 nv;
#pragma unroll
    for (int i = 0; i < 4; i++) nv[i] = pk(fma2(x[k][i], f2{rk, rk}, f2{ok, ok}));
    *LDSP(u4, img + ln_row(4 * c.w + k) + c.lane * 16) = nv;
  }
  if (c.n == 0) *LDSP(unsigned, img + 1024 + (c.w >> 1) * 1088 + (4 * (c.w & 1) + c.q) * 4) = st;
}
DEV void ln_store(const Ctx& c, unsigned img, int tok0) {
  const unsigned a = img + ln_row(8 * c.q + (c.n >> 2)) + (64 * c.w + 4 * (c.n & 3)) * 2;
  if (c.w == 7) __builtin_amdgcn_raw_buffer_store_b32(*LDSP(unsigned, img + 1024 + ((c.lane >> 3) & 3) * 1088 + (c.lane & 7) * 4), c.ws,
                                                      c.lane < 32 ? c.lane * 4 : 0x7ffffff0u, WS_STATS + tok0 * 4, SC1);
  bf8 t[4];
#pragma unroll
  for (int i = 0; i < 4; i++) t[i] = cat(trd(a + 32 * i), trd(a + 32 * i + 4 * 1088));
#pragma unroll
  for (int i = 0; i < 4; i++) st128(__builtin_bit_cast(u4, t[i]), c.ws, c.n * 64 + c.q * 16 + i * 1024, WS_XNT + tok0 * 1024 + c.w * 4096);
}
DEV unsigned u_off(const Ctx& c) { return (c.lane >> 3) * 32768 + (c.lane & 7) * 16; }
DEV u4 u_load(const Ctx& c, unsigned uv, int i) {
  return __builtin_amdgcn_raw_buffer_load_b128(c.ws, uv, WS_XNT + (16 * c.v + 8 * i) * 32768 + (c.xc * 32 + c.jj) * 128, SC1);
}
DEV void u_write(const Ctx& c, u4 r0, u4 r1) {
  const unsigned a = LU + ((c.lane >> 2) & 1) * UCH + 512 + (16 * c.v + (c.lane >> 3)) * 64 + (c.lane & 3) * 16;
  *LDSP(u4, a) = r0;
  *LDSP(u4, a + 512) = r1;
  if (c.v == 0) *LDSP(u4, LU + (c.lane & 1) * UCH + (c.lane >> 1) * 16) = u4{0, 0, 0, 0};
}
DEV u2 flag_sample(const Params& P, unsigned voff, unsigned soff) {
  u2 r; asm volatile("buffer_load_dwordx2 %0, %2, %1, %3 offen sc1" : "=v"(r) : "s"(mksrd(P.ws, WS_BYTES)), "v"(voff), "s"(soff) : "memory"); return r;
}

struct C2 { f2 re, im; };
DEV C2 cmul(C2 a, C2 b) { C2 r; r.re = fma2(a.re, b.re, -(a.im * b.im)); r.im = fma2(a.re, b.im, a.im * b.re); return r; }
DEV C2 zpow(f2 al, f2 th, float e) {
  const f2 a = al * e, t0 = th * e;
  const f2 t = {__builtin_amdgcn_fractf(t0.x), __builtin_amdgcn_fractf(t0.y)};
  const f2 m = {__builtin_amdgcn_exp2f(a.x), __builtin_amdgcn_exp2f(a.y)};
  C2 r; r.re = m * f2{__builtin_amdgcn_cosf(t.x), __builtin_amdgcn_cosf(t.y)}; r.im = m * f2{__builtin_amdgcn_sinf(t.x), __builtin_amdgcn_sinf(t.y)};
  return r;
}
struct KIn { unsigned are, aim, bre, bim, cre, cim, ldt, dsk; u64 epoch; };
DEV unsigned par_load(const u16* p, unsigned voff) {
  unsigned r; asm volatile("global_load_dword %0, %1, %2" : "=v"(r) : "v"(voff), "s"(p) : "memory"); return r;
}
DEV unsigned spar_load(const u16* p, unsigned off) { unsigned r; asm volatile("s_load_dword %0, %1, %2" : "=s"(r) : "s"(p), "s"(off) : "memory"); return r; }
template <int ROLE> DEV KIn kgen_load(const Ctx& c, const Params& P) {
  const unsigned pair = c.xc * 32 + c.jj, o = (2 * pair + (c.w & 1)) * 256 + c.lane * 4;
  KIn r;
  r.are = par_load(P.are, o); r.aim = par_load(P.aim, o);
  r.ldt = spar_load(P.ldt, pair * 4); r.dsk = spar_load(P.dsk, pair * 4);
  asm volatile("s_load_dwordx2 %0, %1, 0" : "=s"(r.epoch) : "s"(P.ws + WS_EPOCH) : "memory");
  if (ROLE == 0 && c.w < 2) { r.bre = par_load(P.bre, o); r.bim = par_load(P.bim, o); r.cre = par_load(P.cre, o); r.cim = par_load(P.cim, o); }
  return r;
}
template <int ROLE, int N> DEV void kgen_take(Ctx& c, const Params& P, KIn& k) {
  KIn o; char* ws = P.ws;
  asm volatile("s_waitcnt lgkmcnt(0)\n\ts_mov_b32 %0, %4\n\ts_mov_b32 %1, %5\n\ts_mov_b64 %2, %6"
               : "=&s"(o.ldt), "=&s"(o.dsk), "=&s"(o.epoch), "+s"(ws) : "s"(k.ldt), "s"(k.dsk), "s"(k.epoch) : "memory");
  asm volatile("s_waitcnt vmcnt(%4)\n\tv_mov_b32 %0, %2\n\tv_mov_b32 %1, %3" : "=&v"(o.are), "=&v"(o.aim) : "v"(k.are), "v"(k.aim), "i"(N) : "memory");
  if (ROLE == 0) asm volatile("v_mov_b32 %0, %4\n\tv_mov_b32 %1, %5\n\tv_mov_b32 %2, %6\n\tv_mov_b32 %3, %7"
               : "=&v"(o.bre), "=&v"(o.bim), "=&v"(o.cre), "=&v"(o.cim) : "v"(k.bre), "v"(k.bim), "v"(k.cre), "v"(k.cim) : "memory");
  k = o;
  c.tag = u2{(unsigned)(o.epoch + 1), (unsigned)((o.epoch + 1) >> 32)};
  c.ws = mkrs(ws, WS_BYTES);
}
constexpr unsigned LKB = LKG + 44416;
struct KPre { float del, g0, g1, rev; unsigned wa, wb, rd, sh, rs, ta, tb, s0, s1; bool sw; };
template <int ROLE> DEV void kgen_pre(const Ctx& c, KPre& k) {
  const bool hi = c.v >= 2;
  const int ch = c.w & 1;
  const unsigned T = ROLE == 0 ? (hi ? LKG : LKB) + ch * 8672 : (hi ? LKG + 17344 + ch * 8672 : LKG + 34688 + ch * 4864);
  k.del = ROLE == 0 ? (hi ? 1.f : 8.f) : (hi ? 128.f : 2.f);
  k.g0 = ROLE == 0 ? (hi ? 128.f : 64.f) : (hi ? 512.f : 256.f);
  k.g1 = hi ? 516.f : 2048.f;
  k.rev = (ROLE == 0 && !hi) ? -0.15915494309189535f : 0.15915494309189535f;
  k.wa = T + c.lane * 8; k.wb = k.wa + ((ROLE == 1 && !hi) ? 4 : 8) * 544; k.rd = T + c.n * 544 + c.q * 16;
  k.sh = ch ? 0 : 16;
  k.rs = hi ? 1088 : 544;
  pin(k.del); pin(k.g0); pin(k.wa); pin(k.rd);
  if (ROLE == 0) pin(k.rev); else { pin(k.g1); pin(k.wb); }
}
template <int ROLE> DEV void ktab_pre(const Ctx& c, KPre& k) {
  const bool hi = c.v >= 2;
  const int n = c.n, q = c.q;
  const unsigned tb = LKT + (c.w & 1) * 1536;
  if (ROLE == 0) { k.ta = hi ? tb + 256 * (q >> 1) + 8 * (q & 1) + 16 * n : LJK + c.lane * 8; pin(k.ta); }
  else {
    const bool far = hi && q >= 2;
    k.ta = tb + 128 * q + (!hi ? (q < 2 ? 512 : 1024) + 8 * n : 768 + (q < 2 ? 4 * n : 62 * (n & 1) + 2 * n));
    k.tb = k.ta + (!hi ? 4 : q < 2 ? 64 : 32);
    k.s0 = !far ? 0x03020100u : (n & 1) ? 0x01000504u : 0x05040100u;
    k.s1 = k.s0 + (far ? 0x02020202u : 0x04040404u);
    k.sw = far && (n & 1);
    pin(k.ta); pin(k.tb); pin(k.s0); pin(k.s1);
  }
}
template <int ROLE, class Hook>
DEV void kgen_rows(const Ctx& c, const KPre& p, const KIn& k, bf8 (&A)[8], Hook&& hook) {
  const float dt = rbf(__builtin_amdgcn_exp2f(bhi(k.ldt << p.sh) * 1.4426950408889634f));
  const f2 al = (unpk(k.are) * dt) * 1.4426950408889634f;
  const f2 th = (unpk(k.aim) * dt) * p.rev;
  auto put = [&](unsigned ad, C2 z) __attribute__((always_inline)) { *LDSP(u2, ad) = u2{pk(z.re), pk(z.im)}; };
  C2 st = zpow(al, th, p.del);
  pin(st.re); pin(st.im);
  hook(IC<0>{});
  C2 a, b, e;
  if (ROLE == 0) {
    a = C2{f2{1.f, 1.f}, f2{0.f, 0.f}}; b = zpow(al, th, p.g0);
    if (c.w < 2) { a = cmul(C2{unpk(k.cre), -unpk(k.cim)}, C2{unpk(k.bre) * dt, unpk(k.bim) * -dt}); b = cmul(b, a); }
  } else {
    a = zpow(al, th, p.g0);
    if (c.w >= 6) b = cmul(a, a); else b = zpow(al, th, 384.f);
  }
  pin(b.re); pin(b.im);
  hook(IC<1>{});
  sfor<0, 4>(LAM(I) {
    constexpr int i = CV(I);
    put(p.wa + i * (ROLE ? p.rs : 544), a); put(p.wb + i * 544, b);
    hook(IC<2 + i>{});
    if constexpr (ROLE == 0 || i < 3) a = cmul(a, st); else a = zpow(al, th, p.g1);
    b = cmul(b, st);
  });
  if (ROLE == 1 && c.w < 6) { put(p.wa + 8 * 544, a); hook(IC<6>{}); hook(IC<7>{}); hook(IC<8>{}); }
  else sfor<4, 8>(LAM(I) {
    constexpr int i = CV(I);
    put(p.wa + (ROLE ? 2 * i - 7 : i) * 544, a); put(p.wb + i * 544, b);
    if constexpr (i < 7) { hook(IC<2 + i>{}); a = cmul(a, st); b = cmul(b, st); }
  });
#pragma unroll
  for (int i = 0; i < 8; i++) A[i] = *LDSP(bf8, p.rd + i * 64);
}
DEV void kgen_cols(const Ctx& c, bf8 (&B)[8]) {
#pragma unroll
  for (int i = 0; i < 8; i++) B[i] = *LDSP(bf8, LKB + (c.w & 1) * 8672 + c.n * 544 + c.q * 16 + i * 64);
}
DEV void kgen_mma(f4& t, const bf8 (&A)[8], const bf8 (&B)[8]) {
  mma0(t, A[0], B[0]);
#pragma unroll
  for (int i = 1; i < 8; i++) mma(t, A[i], B[i]);
}
struct alignas(16) U1 { unsigned v; };
struct KTab { f4 t; U1 a, b, p[2]; };
constexpr int KT_OPS = 9;
template <int ROLE, int k> DEV void ktab_op(const Ctx& c, const KPre& p, KTab& z) {
  if constexpr (ROLE == 0) {
    if constexpr (k == 0) { pin(z.t); z.t[0] += __uint_as_float(z.a.v); pin(z.t); }
    if constexpr (k == 1) { z.p[1].v = pk(f2{z.t[2], z.t[3]}); pin(z.p[1].v); }
    if constexpr (k == 2) { z.p[0].v = pk(f2{z.t[0], z.t[1]}); pin(z.p[0].v); }
    if constexpr (k == 3) *LDSP(u2, p.ta) = u2{z.p[0].v, z.p[1].v};
  } else {
    if constexpr (k == 0) { pin(z.t); z.p[0].v = pk(f2{z.t[0], z.t[1]}); pin(z.p[0].v); }
    if constexpr (k == 1) { z.p[1].v = pk(f2{z.t[2], z.t[3]}); pin(z.p[1].v); }
    if constexpr (k == 2) { z.b.v = p.sw ? z.p[0].v : z.p[1].v; pin(z.b.v); }
    if constexpr (k == 3) { z.a.v = p.sw ? z.p[1].v : z.p[0].v; pin(z.a.v); }
    if constexpr (k == 4) { z.b.v = (unsigned)__builtin_amdgcn_update_dpp((int)z.b.v, (int)z.b.v, 0xb1, 0xc, 0xf, false); pin(z.b.v); }
    if constexpr (k == 5) { z.p[0].v = __builtin_amdgcn_perm(z.b.v, z.a.v, p.s0); pin(z.p[0].v); }
    if constexpr (k == 6) { z.p[1].v = __builtin_amdgcn_perm(z.b.v, z.a.v, p.s1); pin(z.p[1].v); }
    if constexpr (k == 7) *LDSP(unsigned, p.ta) = z.p[0].v;
    if constexpr (k == 8) *LDSP(unsigned, p.tb) = z.p[1].v;
  }
}
DEV void ktab_skip(const Ctx& c, const KPre& p, KTab& z, unsigned dsk) { z.a.v = (c.w >= 2 && c.lane == 0) ? (dsk << p.sh) & 0xffff0000u : 0u; pin(z.a.v); }
typedef unsigned u2a __attribute__((ext_vector_type(2), aligned(4)));
struct KExp { U1 a0, a1, a2, a3, s[7], wr, h0, pv[2][2], o[2][4][4], es[3]; u2 e[4]; };
template <int PART> DEV void kexp_pre(const Ctx& c, KExp& z) {
  const int kh = c.v >> 1, l = c.lane;
  const unsigned CC = LKT + (c.v & 1) * 1536;
  const bool up = l >= 32, odd = l & 1;
  if (PART == 0) {
    z.s[0].v = (kh || up) ? 0x01000100u : 0x03020100u;
    z.s[1].v = kh ? 0x03020100u : up ? 0x05040302u : 0x07060504u;
    z.s[2].v = kh ? 0x03020302u : z.s[0].v;
    z.s[3].v = kh ? 0x05040504u : z.s[1].v;
    z.s[4].v = odd ? 0x03020302u : 0x01000100u;
    z.s[5].v = odd ? 0x05040504u : 0x03020100u;
    z.s[6].v = odd ? 0x05040504u : 0x03020302u;
    z.es[0].v = kh ? 0x01000100u : 0x0c0c0c0cu; z.es[1].v = kh ? 0x05040302u : 0x0c0c0c0cu; z.es[2].v = kh ? 0x05040504u : 0x03020302u;
    asm volatile("" : "+s"(z.es[0].v), "+s"(z.es[1].v), "+s"(z.es[2].v));
    asm volatile("" : "+v"(z.s[0].v), "+v"(z.s[1].v), "+v"(z.s[2].v), "+v"(z.s[3].v), "+v"(z.s[4].v), "+v"(z.s[5].v), "+v"(z.s[6].v));
  } else {
    z.a0.v = CC + (kh ? 768 + l * 4 : up ? 256 + l * 8 : l * 16);
    z.a1.v = z.a0.v + (kh ? 0 : up ? 4 : 8);
    z.a2.v = CC + 1024 + 128 * kh + (l >> 1) * 4;
    z.a3.v = CC + 764;
    z.wr.v = LKC + (c.v & 1) * KCH + 32 + kh * 1024 + l * 16;
    asm volatile("" : "+v"(z.a0.v), "+v"(z.a1.v), "+v"(z.a2.v), "+v"(z.a3.v), "+v"(z.wr.v));
  }
}
constexpr int KX_OPS = 45;
template <int k> DEV void kexp_op(const Ctx& c, KExp& z) {
  auto set = [&](U1& d, unsigned x) __attribute__((always_inline)) { d.v = x; pin(d.v); };
  if constexpr (k == 0) z.e[0] = *LDSP(u2a, z.a0.v);
  else if constexpr (k == 1) z.e[1] = *LDSP(u2a, z.a1.v);
  else if constexpr (k == 2) z.e[2] = *LDSP(u2a, z.a2.v);
  else if constexpr (k == 3) z.e[3] = *LDSP(u2a, z.a3.v);
  else if constexpr (k == 4) z.h0.v = *LDSP(unsigned, z.a3.v + 256);
  else if constexpr (k < 9) { constexpr int i = k - 5; set(z.o[0][0][i], __builtin_amdgcn_perm(z.e[i >> 1][1], z.e[i >> 1][0], z.s[i].v)); }
  else if constexpr (k < 13) { constexpr int i = k - 9;
    if constexpr (i == 1) asm volatile("v_mov_b32 %0, %1" : "=v"(z.o[1][0][1].v) : "v"(z.o[1][0][0].v));
    else set(z.o[1][0][i], __builtin_amdgcn_perm(z.e[2][1], z.e[2][0], z.s[i ? 3 + i : 4].v));
  }
  else if constexpr (k == 13) { z.pv[0][0].v = __builtin_amdgcn_perm(z.e[3][1], z.e[3][0], z.es[0].v); pin(z.pv[0][0].v); }
  else if constexpr (k == 14) { z.pv[0][1].v = __builtin_amdgcn_perm(z.e[3][1], z.e[3][0], z.es[1].v); pin(z.pv[0][1].v); }
  else if constexpr (k == 15) { z.pv[1][0].v = __builtin_amdgcn_perm(z.e[2][0], z.h0.v, z.es[2].v); pin(z.pv[1][0].v); }
  else if constexpr (k == 16) { z.pv[1][1].v = z.o[1][0][0].v; pin(z.pv[1][1].v); }
  else if constexpr (k < 21) {
    constexpr int i = (k - 17) >> 1, j = (k - 17) & 1;
    z.pv[i][j].v = (unsigned)__builtin_amdgcn_update_dpp((int)z.pv[i][j].v, (int)z.o[i][0][2 + j].v, 0x138, 0xf, 0xf, false); pin(z.pv[i][j].v);
  } else {
    constexpr int i = (k - 21) / 12, cp = 1 + (k - 21) % 12 / 4, j = (k - 21) % 4;
    auto dw = [&](int m) __attribute__((always_inline)) { return m < 0 ? z.pv[i][2 + m].v : z.o[i][0][m].v; };
    if constexpr (cp == 2) asm volatile("v_mov_b32 %0, %1" : "=v"(z.o[i][2][j].v) : "v"(dw(j - 1)));
    else asm volatile("v_alignbit_b32 %0, %1, %2, 16" : "=v"(z.o[i][cp][j].v) : "v"(dw(j - (cp >> 1))), "v"(dw(j - 1 - (cp >> 1))));
  }
}
template <int k> DEV void kexp_put(const Ctx& c, const KExp& z) {
  if constexpr (k < 8) *LDSP(u4, z.wr.v + (k >> 2) * 2048 + (k & 3) * KCS) = u4{z.o[k >> 2][k & 3][0].v, z.o[k >> 2][k & 3][1].v, z.o[k >> 2][k & 3][2].v, z.o[k >> 2][k & 3][3].v};
  else *LDSP(unsigned, LKC + (c.lane >> 3) * KCS + (c.lane & 7) * 4) = 0u;
}

struct WdSrc { i4 srd; unsigned voff[4], par; };
DEV WdSrc wd_src(const Ctx& c, const Params& P) {
  WdSrc s; s.srd = mksrd(P.wd, F * D * 2); s.par = TILE_P(c) * 32768;
  const int r = c.lane >> 3;
#pragma unroll
  for (int k = 0; k < 4; k++) { s.voff[k] = (448 * c.v + r) * 1024 + TILE_C(c) * 128 + (((c.lane & 7) ^ (r & 6)) * 16) + k * 7168; pin(s.voff[k]); }
  return s;
}
DEV unsigned wd_raddr(int v, int n, int q) { return v * 4096 + (4 * q + (n >> 2)) * 128 + (((2 * q + (n >> 3)) & 3) * 32) + (n & 3) * 8; }
DEV void wd_ld(const WdSrc& s, int i, int r) {
  const unsigned so = s.par ^ ((i % 7) * 65536 + (i / 7) * 32768);
  if (r == 0) glds_at<0>(s.srd, s.voff[0], so);
  else if (r == 1) glds_at<1024>(s.srd, s.voff[1], so);
  else if (r == 2) glds_at<2048>(s.srd, s.voff[2], so);
  else glds_at<3072>(s.srd, s.voff[3], so);
}
DEV void wd_chunk(const Ctx& c, const WdSrc& s, int i) {
  set_m0(c.v * 4096, wd_base(i % 7)); asm volatile("s_nop 0");
#pragma unroll
  for (int r = 0; r < 4; r++) wd_ld(s, i, r);
}

struct Frags { bf4 a[4][2]; bf8 b[4]; };
DEV void rd_piece(Frags& Fr, int i, unsigned wa0, unsigned wa1, unsigned xa, int ks) {
  if (i == 0) Fr.b[0] = *LDSP(bf8, xa);
  else if (i < 9) { const int t = (i - 1) >> 1, h = (i - 1) & 1; Fr.a[t][h] = trd(((t & 1) ? wa1 : wa0) + (t >> 1) * 16384 + ks * 8192 + h * 1024); }
  else Fr.b[i - 8] = *LDSP(bf8, xa + (i - 8) * 2048);
}

struct G1Src { i4 sx, sg, su; unsigned vx0, vx1, vw0, vw1; int k0; };
DEV G1Src g1_src(const Ctx& c) {
  const int p = (c.xc & 3) * 4 + (c.jj & 3), ft = (c.xc >> 2) * 7 + (c.jj >> 2);
  G1Src g;
  g.k0 = 2 * (c.xc & 3);
  const int xr = c.lane >> 3;
  g.vx0 = (p * 128 + 16 * c.w + xr) * 1024 + (((c.lane & 7) ^ (xr >> 1)) * 16);
  g.vx1 = (g.vx0 ^ 64) + 8192 - 1024;
  g.vw0 = (8 * c.w + c.q) * 3584 + ft * 256 + ((c.n ^ ((c.q | ((c.w & 1) << 2)) << 1)) * 16);
  g.vw1 = g.vw0 + 4 * 3584 - 1024;
  asm volatile("" : "+v"(g.vx0), "+v"(g.vx1), "+v"(g.vw0), "+v"(g.vw1));
  return g;
}
DEV void g1_m0(const Ctx& c, int s, int i) { set_m0(c.w * 2048, slot_of(s) + (i >> 1) * 16384); }
DEV void g1_ld(const G1Src& g, int s, int i) {
  const int sk = (s + g.k0) & 7;
  if (i == 0) glds_at<0>(g.sx, g.vx0, sk * 128);
  else if (i == 1) glds_at<1024>(g.sx, g.vx1, sk * 128);
  else if (i & 1) glds_at<1024>(i > 3 ? g.su : g.sg, g.vw1, sk * 64 * 3584);
  else glds_at<0>(i > 3 ? g.su : g.sg, g.vw0, sk * 64 * 3584);
}
struct G1Rot { i4 srd[2]; unsigned vo[2], dst[4], so[2]; };
DEV G1Rot g1_rot(const Ctx& c, const G1Src& g) {
  const int mine = c.jj & 3;
  G1Rot r;
#pragma unroll
  for (int k = 0; k < 2; k++) {
    const bool up = ((mine >> 1) ^ k) & 1;
    r.srd[k] = g.sg; r.srd[k][0] = up ? g.su[0] : g.sg[0]; r.srd[k][1] = up ? g.su[1] : g.sg[1];
    r.vo[k] = g.vw0 + ((mine ^ k) & 1) * 4 * 3584;
    r.so[k] = (g.k0 + k) * 64 * 3584;
  }
#pragma unroll
  for (int k = 0; k < 4; k++) r.dst[k] = 16384 + ((mine ^ k) >> 1) * 16384 + c.w * 2048 + ((mine ^ k) & 1) * 1024;
  return r;
}
template <int S, int K> DEV void g1_ld_w(const G1Rot& r) {
  asm volatile("s_add_u32 m0, %3, %4\n\ts_nop 0\n\tbuffer_load_dwordx4 %1, %0, %2 offen lds"
               ::"s"(r.srd[K >> 1]), "v"(r.vo[K & 1]), "s"(r.so[S]), "s"(r.dst[K]), "i"(slot_of(S)) : "memory", "scc");
}

#define SCH 4, 4, 3, 4, 3, 3, 3, 3, 3, 3, 3
constexpr int FEED_N[11] = {SCH};
constexpr int feed_done(int h) { int e = 12; for (int k = 0; k <= h && k < 11; k++) e += FEED_N[k]; return e; }
constexpr int feed_at(int h, int i) {
  if (h > 10) return -1;
  const int nn = FEED_N[h], e0 = feed_done(h) - nn;
  for (int k = 0; k < nn; k++) if ((nn == 4 ? 1 + 4 * k : 4 + 5 * k) == i) return e0 + k;
  return -1;
}
constexpr int feed_younger(int s) { return feed_done(2 * s) - 6 * (s + 2); }
constexpr bool feed_ok() {
  for (int s = 0; s < 6; s++) if (feed_younger(s) < 0 || feed_done(2 * s) > 6 * (s + 3)) return false;
  return feed_done(10) == 48 && FEED_N[0] <= 4;
}
static_assert(feed_ok(), "load schedule");
constexpr int wd_at(int h, int i) { for (int e = 0; e < 28; e++) if (11 + e / 6 == h && 1 + (e % 6) * 5 / 2 == i) return e; return -1; }

template <int ROLE>
DEV void gemm1(Ctx& c, const Params& P, G1Src gs, KIn kin, WdSrc& wds, f4 (&acc)[4][4], bf4 (&W1)[7][4][2], unsigned (&wa)[4], KExp& kx, u4 (&ur)[2], f4 (&cv)[4], f4 (&tc)[4]) {
  const int p = (c.xc & 3) * 4 + (c.jj & 3), ft = (c.xc >> 2) * 7 + (c.jj >> 2);
  const int lane = c.lane, n = c.n, q = c.q, v = c.v;
  gs.sx = mksrd(P.x, L * D * 2); gs.sg = mksrd(P.wg, D * F * 2); gs.su = mksrd(P.wu, D * F * 2);
  G1Rot rot = g1_rot(c, gs);
#define R1C 0
  if (ROLE == 0 || !R1C) {
    set_m0(rot.dst[0], 0); asm volatile("s_nop 0"); glds_at<0>(rot.srd[0], rot.vo[0], rot.so[0]);
    if (R1C) { set_m0(rot.dst[0], 8192); asm volatile("s_nop 0"); glds_at<0>(rot.srd[0], rot.vo[0], rot.so[0] + 32 * 3584); }
    if (ROLE == 0) touch(gs.sx, (lane < 10 ? lane >> 1 : 4) * 1024 + (lane & 1) * 128, (p * 128 + (c.jj >> 2) * 18 + 5 * v) * 1024 + gs.k0 * 128);
    set_m0(rot.dst[0], slot_of(1)); asm volatile("s_nop 0"); glds_at<0>(rot.srd[0], rot.vo[0], rot.so[1]);
    if (R1C) { set_m0(rot.dst[0], slot_of(1) + 8192); asm volatile("s_nop 0"); glds_at<0>(rot.srd[0], rot.vo[0], rot.so[1] + 32 * 3584); }
  }
#define KPRI 1
  if (ROLE == 1 && (KPRI == 1 || (KPRI == 2 && v < 2))) __builtin_amdgcn_s_setprio(1);
  SB();
  KPre kp;
  kgen_pre<ROLE>(c, kp);
  unsigned xa[2][2], wr[3][2];
  xa[0][0] = (((ROLE ^ v) & 1) * 64 + n) * 128 + ((q ^ (n >> 1)) * 16); xa[0][1] = xa[0][0] ^ 64;
  wr[0][0] = 16384 + (8 * q + (n >> 2)) * 256 + (((2 * v) ^ ((n >> 2) | ((q & 1) << 2))) * 32) + (n & 3) * 8; wr[0][1] = wr[0][0] ^ 32;
  unsigned kbr = LKB + (c.w & 1) * 8672 + n * 544 + q * 16;
  asm volatile("" : "+v"(xa[0][0]), "+v"(xa[0][1]), "+v"(wr[0][0]), "+v"(wr[0][1]), "+v"(kbr));
  unsigned lfv, cpv, l4, stv, csa, sta, one0, uv; u4 ones;
  if (ROLE == 0) {
    if (threadIdx.x < 12) *LDSP(unsigned, LSY + threadIdx.x * 4) = 0u;
    ones = u4{0x3f803f80u, 0x3f803f80u, 0x3f803f80u, 0x3f803f80u}; pin(ones);
  }
  kgen_take<ROLE, R1C ? 5 - 5 * ROLE : 3 - ROLE>(c, P, kin);
  bf8 KA[8], KB[8]; KTab kt;
  kgen_rows<ROLE>(c, kp, kin, KA, LAM(H) {});
  if (ROLE == 1) ktab_pre<1>(c, kp);
  u2 tagv;
  if (ROLE == 1 && v < 2) {
    const unsigned bx0 = gs.vx0 - 65536, bx1 = gs.vx1 - 65536, bw0 = rot.vo[0] - 32 * 3584, bw1 = rot.vo[1] - 32 * 3584;
    const unsigned ox0 = gs.k0 * 128, ow0 = rot.so[0], dx = c.v * 2048, d1 = rot.dst[1] - 8192, d2 = rot.dst[2] - 8192, d3 = rot.dst[3] - 8192;
#pragma unroll
    for (unsigned it = 0; it < 8; it++) {
      const unsigned st = it >> 2, k = it & 3, dm = st * slot_of(1) + k * 4096;
      unsigned ox, ow;
      asm volatile("s_add_u32 m0, %11, %15\n\ts_add_u32 %0, %9, %16\n\tbuffer_load_dwordx4 %5, %2, %0 offen lds\n\t"
                   "buffer_load_dwordx4 %6, %2, %0 offen offset:1024 lds\n\t"
                   "s_add_u32 m0, %12, %15\n\ts_add_u32 %1, %10, %17\n\tbuffer_load_dwordx4 %8, %3, %1 offen lds\n\t"
                   "s_add_u32 m0, %13, %15\n\ts_nop 0\n\tbuffer_load_dwordx4 %7, %4, %1 offen lds\n\t"
                   "s_add_u32 m0, %14, %15\n\ts_nop 0\n\tbuffer_load_dwordx4 %8, %4, %1 offen lds"
                   : "=&s"(ox), "=&s"(ow)
                   : "s"(gs.sx), "s"(rot.srd[0]), "s"(rot.srd[1]), "v"(bx0), "v"(bx1), "v"(bw0), "v"(bw1), "s"(ox0), "s"(ow0),
                     "s"(dx), "s"(d1), "s"(d2), "s"(d3), "i"(dm), "i"(st * 128 + k * 32768), "i"((st * 64 + k * 16) * 3584)
                   : "memory", "scc");
#define THN 0
#define THS 2
      if (it < THN) __builtin_amdgcn_s_sleep(THS);
    }
    waitv<20>();
  } else waitv<R1C ? 2 : 1>();
  __builtin_amdgcn_s_setprio(1);
  bar();
  STAMP(2);
  Frags F0, Fb;
  auto rd = [&](Frags& Fr, int i, int s, int ks) __attribute__((always_inline)) {
    rd_piece(Fr, i, wr[s % 3][0], wr[s % 3][1], xa[s % 3 == 2][ks] + (s % 3 == 1 ? slot_of(1) : 0), ks);
  };
#pragma unroll
  for (int i = 0; i < 12; i++) rd(F0, i, 0, 0);
  SB();
  f4 cs[4];
  auto colsum = [&](const Frags& Fr, int rel) __attribute__((always_inline)) {
    if (ROLE == 0 && (c.jj & 3) == rel) {
#pragma unroll
      for (int t = 0; t < 4; t++) asm volatile("v_mfma_f32_16x16x32_bf16 %0, %1, %2, 0" : "+v"(cs[t]) : "v"(ones), "v"(cat(Fr.a[t][0], Fr.a[t][1])));
    }
    SB();
  };
  u2 lf; u4 cpr[4]; unsigned str; F1 cs4[4], tk[4], tr; U1 csm, t0, t1, ga, gb;
  auto feed = [&](auto H, auto I) __attribute__((always_inline)) {
    constexpr int h = CV(H), i = CV(I);
    constexpr int e = feed_at(h, i), en = feed_at(h, i + 1);
    if constexpr (e >= 0) g1_ld(gs, e / 6, e % 6);
    if constexpr (en >= 0 && !(en & 1)) g1_m0(c, en / 6, en % 6);
    constexpr int d = ROLE ? wd_at(h, i) : -1, dn = ROLE ? wd_at(h, i + 1) : -1;
    if constexpr (d >= 0) wd_ld(wds, d >> 2, d & 3);
    if constexpr (dn >= 0 && !(dn & 3)) set_m0(v * 4096, wd_base(dn >> 2));
  };
  sfor<0, 8>(LAM(S) {
    constexpr int s = CV(S);
    sfor<0, 16>(LAM(I) {
      constexpr int i = CV(I);
      if constexpr (s == 0) mma0(acc[i >> 2][i & 3], cat(F0.a[i & 3][0], F0.a[i & 3][1]), F0.b[i >> 2]);
      else mma(acc[i >> 2][i & 3], cat(F0.a[i & 3][0], F0.a[i & 3][1]), F0.b[i >> 2]);
      if constexpr (s == 0) {
        if constexpr (i == 14 || i == 15) { wr[1][i & 1] = wr[0][i & 1] + slot_of(1); pin(wr[1][i & 1]); }
        if constexpr (i < 12) rd(Fb, i, s, 1);
        if constexpr (i >= 6 && i < 14) KB[i - 6] = *LDSP(bf8, kbr + (i - 6) * 64);
      } else if constexpr (i != 4 && i != 9 && i < 14) rd(Fb, i - (i > 4) - (i > 9), s, 1);
      feed(IC<2 * s>{}, I);
      if constexpr (s == 1 && (i == 2 || i == 3)) { wr[2][i & 1] = wr[0][i & 1] + SLOT_C; pin(wr[2][i & 1]); }
      if constexpr (s == 1 && (i == 6 || i == 7)) { xa[1][i & 1] = xa[0][i & 1] + SLOT_C; pin(xa[1][i & 1]); }
      if constexpr (ROLE == 1 && s == 3) {
        if constexpr (i == 0) { t0.v = lane >> 3; pin(t0.v); }
        if constexpr (i == 1) { t1.v = (lane ^ (t0.v & 6)) & 7; pin(t1.v); }
        if constexpr (i == 3) { t0.v = t0.v * 1024 + (448 * v * 1024 + TILE_C(c) * 128); pin(t0.v); }
        if constexpr (i == 5) { wds.voff[0] = t1.v * 16 + t0.v; pin(wds.voff[0]); wds.srd = mksrd(P.wd, F * D * 2); wds.par = TILE_P(c) * 32768; }
        if constexpr (i >= 6 && i < 9) { wds.voff[i - 5] = wds.voff[0] + (i - 5) * 7168; pin(wds.voff[i - 5]); }
      }
      if constexpr (ROLE == 1 && s == 4) {
        if constexpr (i == 0) { t0.v = lane >> 2; pin(t0.v); }
        if constexpr (i == 1) { t0.v = t0.v * 128 + (v * 4096 + SLOT_C); pin(t0.v); }
        if constexpr (i == 2) { t1.v = lane & 3; pin(t1.v); }
        if constexpr (i == 3) { t0.v = t1.v * 8 + t0.v; pin(t0.v); }
        if constexpr (i == 5) { t1.v = lane & 24; pin(t1.v); }
        if constexpr (i == 6) { wa[0] = t1.v * 4 + t0.v; pin(wa[0]); }
        if constexpr (i >= 10 && i < 13) { wa[i - 9] = wa[0] ^ ((i - 9) * 32); pin(wa[i - 9]); }
      }
      if constexpr (ROLE == 0 && s == 3) kexp_op<16 + i>(c, kx);
      if constexpr (ROLE == 0 && s == 2) {
        if constexpr (i == 5) { pin(cs[0]); pin(cs[1]); pin(cs[2]); pin(cs[3]); csm.v = __float_as_uint(cs[0][0]); }
        if constexpr (i >= 5 && i < 8) { csm.v = (unsigned)__builtin_amdgcn_update_dpp((int)csm.v, (int)__float_as_uint(cs[i - 4][0]), 0xe4, 1 << (i - 4), 0xf, false); pin(csm.v); }
        if constexpr (i == 15) __builtin_amdgcn_raw_buffer_store_b32(csm.v, c.ws, l4, WS_CPART + (ft * 16 + p) * 1024 + v * 256, SC1);
      }
      if constexpr (ROLE == 0) {
        if constexpr (s == 5 && i == 15) lf = flag_sample(P, lfv, 0);
        if constexpr (s == 6 && (i & 3) == 0) cpr[i >> 2] = __builtin_amdgcn_raw_buffer_load_b128(c.ws, cpv, WS_CPART + ft * 16384 + (i >> 2) * 4096 + v * 256, SC1);
        if constexpr (s == 7) {
          if constexpr (i < 4) { cs4[i].v += __uint_as_float(cpr[3][i]); pin(cs4[i].v); }
          if constexpr (i == 4) { cs4[0].v = swap32_add(cs4[0].v, cs4[1].v); pin(cs4[0].v); }
          if constexpr (i == 5) { cs4[2].v = swap32_add(cs4[2].v, cs4[3].v); pin(cs4[2].v); }
#define CW 7
          if constexpr (i == 7) { cs4[0].v = swap16_add(cs4[0].v, cs4[2].v); pin(cs4[0].v); }
          if constexpr (i == CW) *LDSP(float, csa) = cs4[0].v;
          if constexpr (i == CW + 1) *LDSP(f4, sta) = f4{tk[0].v, tk[1].v, tk[2].v, tk[3].v};
        }
        if constexpr (s == 6 && i == 2) str = __builtin_amdgcn_raw_buffer_load_b32(c.ws, stv, WS_STATS + (p * 128 + 32 * v) * 4, SC1);
      }
      SB();
    });
    if constexpr (s == 0 && ROLE == 0) { asm volatile("" : "=v"(cs[0]), "=v"(cs[1]), "=v"(cs[2]), "=v"(cs[3])); }
    if constexpr (s < 2) colsum(F0, 2 * s);
    if constexpr (ROLE == 0 && s < 5) {
      int ln = lane; pin(ln);
      Ctx d = c; d.lane = ln; d.n = ln & 15; d.q = ln >> 4;
      __builtin_amdgcn_s_setprio(0);
      if constexpr (s == 0) { ktab_pre<0>(d, kp); ktab_skip(d, kp, kt, kin.dsk); l4 = ln * 4; pin(l4); }
      if constexpr (s == 1) kexp_pre<0>(d, kx);
      if constexpr (s == 2) kexp_pre<1>(d, kx);
      if constexpr (s == 3) {
        tagv = c.tag; one0 = flag_off(ln); pin(tagv); pin(one0);
        const unsigned f0 = WS_FC + (v * 14 + ft) * 2048, f1 = WS_FLN + 2048 * v - 2048, f2 = WS_FLN + (4 * p + v) * 128;
        lfv = ln < 32 ? ln * 128 + (ln < 16 ? f0 : f1) : f2; pin(lfv);
      }
      if constexpr (s == 4) {
        cpv = d.q * 1024 + d.n * 16; stv = l4 & 124; sta = LST + 512 * v + 4 * stv;
        csa = LCS + v * 256 + d.n * 16 + (((d.q & 1) << 1) | (d.q >> 1)) * 4; uv = u_off(d);
        pin(cpv); pin(stv); pin(sta); pin(csa); pin(uv);
      }
      __builtin_amdgcn_s_setprio(1);
      SB();
    }
    if constexpr (s == 3 && ROLE == 0) waitv<(feed_younger(s) < FEED_N[5] + FEED_N[6] ? feed_younger(s) : FEED_N[5] + FEED_N[6])>();
    else if constexpr (s < 6) waitv<feed_younger(s) + (ROLE == 0 && (s == 2 || s == 5))>();
    else if constexpr (ROLE == 1) waitv<12>();
    bar();
    if constexpr (s == 0) { STAMP(3); }
    if constexpr (s == 2) { STAMP(4); }
    if constexpr (s == 5) { STAMP(5); }
    if constexpr (s == 6) { STAMP(6); }
    if constexpr (s == 7) { STAMP(1); }
    SB();
#define PRI 0x60
    if constexpr (ROLE == 1) __builtin_amdgcn_s_setprio(1 + ((PRI >> s) & 1));
    sfor<0, 16>(LAM(I) {
      constexpr int i = CV(I);
      mma(acc[i >> 2][i & 3], cat(Fb.a[i & 3][0], Fb.a[i & 3][1]), Fb.b[i >> 2]);
      if constexpr (s < 7 && i != 4 && i != 9 && i < 14) rd(F0, i - (i > 4) - (i > 9), s + 1, 0);
      feed(IC<2 * s + 1>{}, I);
      if constexpr (s == 0 && i == 0) mma0(kt.t, KA[0], KB[0]);
      if constexpr (s == 0 && i > 0 && !(i & 1)) mma(kt.t, KA[i >> 1], KB[i >> 1]);
      if constexpr (s == 1 && i < KT_OPS) ktab_op<ROLE, i>(c, kp, kt);
      if constexpr (ROLE == 0 && s == 3 && i == 0) __builtin_amdgcn_raw_buffer_store_b64(tagv, c.ws, one0, WS_FC + ((v * 14 + ft) * 16 + p) * 128, SC1);
      if constexpr (ROLE == 0 && (s == 2 || s == 3) && 32 * (s - 2) + i < KX_OPS) kexp_op<32 * (s - 2) + i>(c, kx);
      if constexpr (s == 4 && i == 0) { ga.v = q * 16 + (LCS + v * 256); pin(ga.v); }
      if constexpr (s == 4 && i == 2) { gb.v = n * 16 + (LST + ((ROLE ^ v) & 1) * 1024); pin(gb.v); }
      if constexpr (s == 7 && (i & 1)) { if constexpr (i & 2) tc[i >> 2] = *LDSP(f4, gb.v + (i >> 2) * 256); else cv[i >> 2] = *LDSP(f4, ga.v + (i >> 2) * 64); }
      if constexpr (ROLE == 1 && s == 7) W1[i >> 3][(i >> 1) & 3][i & 1] = trd(wa[(i >> 1) & 3] + (i >> 3) * 16384 + (i & 1) * 2048);
      if constexpr (ROLE == 0) {
        if constexpr (s == 5 && i == 15) {
          asm volatile("s_waitcnt vmcnt(0)" : "+v"(lf)::"memory");
          if (!flags_up(c.tag, lf)) wait_flags<4>(c.ws, c.tag, ld_flag(c.ws, lfv, 0), lfv, 0);
        }
        if constexpr (s == 7 && (i == 0 || i == 2)) ur[i >> 1] = u_load(c, uv, i >> 1);
        if constexpr (s == 6 && i < 8) {
          if constexpr (i == 0) { tk[3].v = bhi(str); pin(tk[3].v); }
          if constexpr (i == 1) { tr.v = __builtin_amdgcn_rcpf(tk[3].v); pin(tr.v); }
          if constexpr (i == 2) { tk[0].v = blo(str); pin(tk[0].v); }
          if constexpr (i == 3) { tk[1].v = (-2.f * 0.7978845608028654f * 1.4426950408889634f) * tr.v; pin(tk[1].v); }
          if constexpr (i == 4) { tr.v *= tr.v; pin(tr.v); }
          if constexpr (i == 5) { tk[3].v *= tk[3].v; pin(tk[3].v); }
          if constexpr (i == 6) { tr.v *= 0.044715f; pin(tr.v); }
          if constexpr (i == 7) { tk[2].v = tr.v * tk[1].v; pin(tk[2].v); }
        }
        if constexpr (s == 6 && i >= 8) { constexpr int e = i & 3, k = (i - 8) >> 2; cs4[e].v = (k ? cs4[e].v : __uint_as_float(cpr[0][e])) + __uint_as_float(cpr[k + 1][e]); pin(cs4[e].v); }
      }
      SB();
    });
    if constexpr (s < 2) colsum(Fb, 2 * s + 1);
  });
  __builtin_amdgcn_s_setprio(0);
  STAMP(7);
}

template <int ROLE>
DEV void geglu(const Ctx& c, f4 (&acc)[4][4], const f4 (&cv)[4], const f4 (&tc)[4]) {
  const int p = (c.xc & 3) * 4 + (c.jj & 3), ft = (c.xc >> 2) * 7 + (c.jj >> 2);
  const unsigned hbase = WS_H + uni((2 * p + ((ROLE ^ c.v) & 1)) * 56 + 4 * ft + c.v) * 4096;
#pragma unroll
  for (int i = 0; i < 16; i++) pin(acc[i >> 2][i & 3]);
#pragma unroll
  for (int tt = 0; tt < 4; tt++) {
    const f2 nm = -f2{tc[tt][0], tc[tt][0]}, k0 = {tc[tt][1], tc[tt][1]}, k1 = {tc[tt][2], tc[tt][2]}, s2 = {tc[tt][3], tc[tt][3]};
    f2 a[4], b[4], z[4];
    auto half = [](const f4& x, int e) { return f2{x[2 * e], x[2 * e + 1]}; };
#define STEP(X) _Pragma("unroll") for (int j = 0; j < 4; j++) { X; } SB();
    STEP(a[j] = fma2(nm, half(cv[j >> 1], j & 1), half(acc[tt][j >> 1], j & 1)))
    STEP(z[j] = a[j] * a[j])
    STEP(z[j] = fma2(z[j], k1, k0))
    STEP(z[j] *= a[j])
    STEP(z[j] = (f2{__builtin_amdgcn_exp2f(z[j].x), __builtin_amdgcn_exp2f(z[j].y)}))
    STEP(b[j] = fma2(nm, half(cv[2 + (j >> 1)], j & 1), half(acc[tt][2 + (j >> 1)], j & 1)))
    STEP(z[j] = fma2(z[j], s2, s2))
    STEP(z[j] = (f2{__builtin_amdgcn_rcpf(z[j].x), __builtin_amdgcn_rcpf(z[j].y)}))
    STEP(a[j] *= b[j])
    STEP(a[j] *= z[j])
#undef STEP
    st128(u4{pk(a[0]), pk(a[1]), pk(a[2]), pk(a[3])}, c.ws, c.lane * 16 + tt * 1024, hbase);
    SB();
  }
}
template <int ROLE, int YOUNGER> DEV void h_publish(const Ctx& c) {
  const int p = (c.xc & 3) * 4 + (c.jj & 3), ft = (c.xc >> 2) * 7 + (c.jj >> 2);
  waitv<YOUNGER>();
  st_flag(c.ws, c.tag, WS_FH + uni((2 * p + ((ROLE ^ c.v) & 1)) * 56 + 4 * ft + c.v) * 128, c.lane);
}

DEV void ln_block(Ctx& c, const Params& P, KIn kin) {
  const int tok0 = (c.xc & 3) * 512 + (c.xc >> 2) * 256 + (c.jj - 28) * 64;
  const i4 sx = mksrd(P.x, L * D * 2);
  u4 rows[8];
#pragma unroll
  for (int i = 0; i < 8; i++) rows[i] = row_load(sx, c.lane * 16, (tok0 + 32 * (i >> 2) + 4 * c.w + (i & 3)) * 1024);
  if (threadIdx.x < 12) *LDSP(unsigned, LSY + threadIdx.x * 4) = 0u;
  kgen_take<0, 8>(c, P, kin);
  asm volatile("s_waitcnt vmcnt(4)" : "+v"(rows[0]), "+v"(rows[1]), "+v"(rows[2]), "+v"(rows[3])::"memory");
  STAMP(1);
  ln_compute(c, rows, 0, tok0);
  asm volatile("s_waitcnt vmcnt(0)" : "+v"(rows[4]), "+v"(rows[5]), "+v"(rows[6]), "+v"(rows[7])::"memory");
  bar();
  ln_store(c, 0, tok0);
  STAMP(2);
  ln_compute(c, rows + 4, 34816, tok0 + 32);
  waitv<0>();
  bar();
  if (c.w == 0) st_flag(c.ws, c.tag, WS_FLN + (tok0 >> 5) * 128, c.lane);
  ln_store(c, 34816, tok0 + 32);
  STAMP(3);
  waitv<0>();
  bar();
  if (c.w == 0) st_flag(c.ws, c.tag, WS_FLN + ((tok0 >> 5) + 1) * 128, c.lane);
  STAMP(4);
#define NW 4
  const i4 swd = mksrd(P.wd, F * D * 2);
  const unsigned wq = (c.jj - 28) * 448 * 1024;
  bf8 KA[8], KB[8]; KTab kt;
  KPre kp;
  auto warm = LAM(H) {
    constexpr int h = CV(H);
    if constexpr (h == 0 && NW < 7) { if (c.w >= 4 && c.v < 7 - NW) touch(swd, c.lane * 128, wq + NW * 65536 + (c.xc * (7 - NW) + c.v) * 8192); }
    if constexpr (h < NW) {
      if ((c.w >> 2) == (h & 1)) { unsigned k = h + ((c.xc >> 1) * NW >> 2); k = k >= NW ? k - NW : k;
        touch(swd, c.lane * 128, wq + (1 - TILE_P(c)) * 32768 + k * 65536 + c.v * 8192); }
    }
  };
  if (c.w < 4) { kgen_pre<0>(c, kp); kgen_rows<0>(c, kp, kin, KA, warm); } else { kgen_pre<1>(c, kp); kgen_rows<1>(c, kp, kin, KA, warm); }
  bar();
  kgen_cols(c, KB); kgen_mma(kt.t, KA, KB); mma_fence();
  if (c.w < 4) { ktab_pre<0>(c, kp); ktab_skip(c, kp, kt, kin.dsk); sfor<0, KT_OPS>(LAM(K) { ktab_op<0, CV(K)>(c, kp, kt); }); }
  else { ktab_pre<1>(c, kp); sfor<0, KT_OPS>(LAM(K) { ktab_op<1, CV(K)>(c, kp, kt); }); }
  bar();
  STAMP(5);
  asm volatile("fused_end_a:");
}

struct CFr { bf4 a[8][2]; bf8 b[8]; };
DEV void conv_rd(CFr& Fr, int i, unsigned ka, unsigned ua, int sp) {
  if (i < 2) Fr.a[0][i] = trd(ka + 64 * sp + (i ? 0 : 8));
  else if (i < 10) Fr.b[i - 2] = *LDSP(bf8, ua + 512 * (i - 2) + 192 - 64 * sp);
  else { const int g = (i - 8) >> 1, t = i & 1; Fr.a[g][t] = trd(ka + 512 * g + 64 * sp + (t ? 0 : 8)); }
}
DEV void conv(Ctx& c) {
  const int chl = c.v & 1, kh = c.v >> 1, n = c.n, q = c.q;
  unsigned ka = LKC + chl * KCH + (n >> 2) * KCS + 2 * (16 + 4 * (n & 3) + 128 * kh + 16 - 8 * q) - 8;
  unsigned ua = LU + chl * UCH + 2 * (256 + 16 * n - 16 - 128 * kh + 8 * q) - 192;
  asm volatile("" : "+v"(ka), "+v"(ua));
  f4 acc[8];
  CFr Fr[2];
#pragma unroll
  for (int i = 0; i < 24; i++) conv_rd(Fr[0], i, ka, ua, 0);
  unsigned xw = LKC + chl * KCH + 32 + (c.lane >> 3) * 512 + (c.lane & 7) * 16, yo = (q & 1) * 512 + n * 32 + (q >> 1) * 16;
  asm volatile("" : "+v"(xw), "+v"(yo));
  lds_wait<1>(LSY + 44, 4);
  if (c.jj < 4 && c.w == 0) st_flag(c.ws, c.tag, WS_FG + (c.xc * 4 + c.jj) * 128, c.lane);
  STAMP(11);
  SB();
#pragma unroll
  for (int sp = 0; sp < 4; sp++) {
    int i = 0;
#pragma unroll
    for (int g = 0; g < 8; g++)
#pragma unroll
      for (int m = 0; m + g < 8; m++) {
        if (sp == 0 && g == 0) mma0(acc[m], cat(Fr[0].a[0][0], Fr[0].a[0][1]), Fr[0].b[m]);
        else mma(acc[g + m], cat(Fr[sp & 1].a[g][0], Fr[sp & 1].a[g][1]), Fr[sp & 1].b[m]);
        if (sp < 3 && i < 24) conv_rd(Fr[(sp + 1) & 1], i, ka, ua, sp + 1);
        i++;
        SB();
      }
  }
  mma_fence();
#pragma unroll
  for (int i = 0; i < 8; i++) asm volatile("" : "+v"(acc[i]));
  unsigned pkd[4][2];
  auto finish = [&](auto KH) __attribute__((always_inline)) {
    constexpr int h = CV(KH);
#pragma unroll
    for (int j = 0; j < 4; j++) *LDSP(f4, xw + h * 256 + j * KCS) = acc[4 * (1 - h) + j];
    lds_post(LSY + 16 + c.v * 4, c.lane);
    STAMP(12);
    lds_wait<0>(LSY + 16 + (c.v ^ 2) * 4, 1);
#pragma unroll
    for (int j = 0; j < 4; j++) { const f4 t = acc[4 * h + j] + *LDSP(f4, xw + (1 - h) * 256 + j * KCS); pkd[j][0] = pk(f2{t[0], t[1]}); pkd[j][1] = pk(f2{t[2], t[3]}); }
  };
  if (kh) finish(IC<1>{}); else finish(IC<0>{});
  const int ch = 2 * (c.xc * 32 + c.jj) + chl;
#pragma unroll
  for (int j = 0; j < 4; j += 2) {
    swap16(pkd[j][0], pkd[j + 1][0]); swap16(pkd[j][1], pkd[j + 1][1]);
    st128(u4{pkd[j][0], pkd[j][1], pkd[j + 1][0], pkd[j + 1][1]}, c.ws,
                                           yo + j * 512, WS_YT + ch * 4096 + kh * 2048);
  }
  STAMP(13);
  waitv<0>();
  STAMP(14);
  st_flag(c.ws, c.tag, WS_FY + (kh * 512 + ch) * 128, c.lane);
}
DEV u4 x_load(const Ctx& c, const Params& P, int role, unsigned oa) {
  return __builtin_amdgcn_raw_buffer_load_b128(mkrs(P.x, L * D * 2), oa, (TILE_T(c) * 64 + 16 * c.v + 8 * role) * 1024 + TILE_C(c) * 128, 0);
}
DEV void y_gather(Ctx& c) {
  const int T64 = TILE_T(c), ct = TILE_C(c), l = c.lane;
  const int r0 = 32 * (c.v & 1) + 16 * (c.v >> 1);
  const unsigned fo = WS_FY + ((T64 >> 4) * 512 + 64 * ct + r0) * 128;
  unsigned fv = (l < 16 ? l : 15) * 128, yv = (l >> 3) * 4096 + (l & 7) * 16, ya = LU + (c.v & 1) * (UCH + 64) + (c.v >> 1) * 2080 + l * 16;
  asm volatile("" : "+v"(fv), "+v"(yv), "+v"(ya));
  lds_wait<2>(LSY + 40, 1);
  wait_flags<8>(c.ws, c.tag, ld_flag(c.ws, fv, fo), fv, fo);
  STAMP(15);
#pragma unroll
  for (int i = 0; i < 2; i++)
    *LDSP(u4, ya + i * 1040) = __builtin_amdgcn_raw_buffer_load_b128(c.ws, yv, WS_YT + (64 * ct + r0 + 8 * i) * 4096 + T64 * 128, SC1);
}
struct Fin { unsigned a0, a1, ya, oa; F1 s[8], y[8]; };
template <int ROLE> DEV void finish_addr(const Ctx& c, Fin& f) {
  const int t = c.lane >> 3, c8 = c.lane & 7;
  f.a0 = c.v * 16384 + (8 * ROLE + t) * 256 + ((2 * c8) ^ t) * 16;
  f.ya = LU + (c8 >> 2) * (UCH + 64) + (c8 & 3) * 1040 + (16 * c.v + 8 * ROLE + t) * 2;
  f.oa = t * 1024 + c8 * 16;
  pin(f.a0); pin(f.ya); pin(f.oa);
}
template <int k> DEV void finish_op(Fin& f, const u4& x) {
  if constexpr (k < 8) { f.s[k].v = (k & 1) ? bhi(x[k >> 1]) : blo(x[k >> 1]); pin(f.s[k].v); }
  else if constexpr (k < 16) asm volatile("v_mov_b32 %0, 0" : "=v"(f.y[k - 8].v));
  else { f.a1 = f.a0 ^ 16; pin(f.a1); }
}
template <int ROLE> DEV void finish_tile(Ctx& c, const Params& P, Fin f) {
  const int T64 = TILE_T(c), ct = TILE_C(c);
  const rsrc_t ro = mkrs(P.out, L * D * 2);
  bar();
  if (ROLE == 1) { STAMP(15); }
  float y[8] = {f.y[0].v, f.y[1].v, f.y[2].v, f.y[3].v, f.y[4].v, f.y[5].v, f.y[6].v, f.y[7].v};
  f4 sa = {f.s[0].v, f.s[1].v, f.s[2].v, f.s[3].v}, sb = {f.s[4].v, f.s[5].v, f.s[6].v, f.s[7].v};
  asm volatile("ds_read_u16_d16_hi %0, %8\n\tds_read_u16_d16_hi %1, %8 offset:128\n\tds_read_u16_d16_hi %2, %8 offset:256\n\tds_read_u16_d16_hi %3, %8 offset:384\n\t"
               "ds_read_u16_d16_hi %4, %8 offset:512\n\tds_read_u16_d16_hi %5, %8 offset:640\n\tds_read_u16_d16_hi %6, %8 offset:768\n\tds_read_u16_d16_hi %7, %8 offset:896"
               : "+v"(y[0]), "+v"(y[1]), "+v"(y[2]), "+v"(y[3]), "+v"(y[4]), "+v"(y[5]), "+v"(y[6]), "+v"(y[7]) : "v"(f.ya) : "memory");
  f4 pa[4], pb[4];
#pragma unroll
  for (int k = 0; k < 4; k++) { pa[k] = *LDSP(f4, f.a0 + k * 4096); pb[k] = *LDSP(f4, f.a1 + k * 4096); }
  SB();
#pragma unroll
  for (int k = 0; k < 4; k++) { sa += pa[k]; sb += pb[k]; }
  asm volatile("s_waitcnt lgkmcnt(0)" : "+v"(y[0]), "+v"(y[1]), "+v"(y[2]), "+v"(y[3]), "+v"(y[4]), "+v"(y[5]), "+v"(y[6]), "+v"(y[7])::"memory");
  sa += f4{y[0], y[1], y[2], y[3]}; sb += f4{y[4], y[5], y[6], y[7]};
  st128(u4{pk(f2{sa[0], sa[1]}), pk(f2{sa[2], sa[3]}), pk(f2{sb[0], sb[1]}), pk(f2{sb[2], sb[3]})}, ro, f.oa,
                                         (T64 * 64 + 16 * c.v + 8 * ROLE) * 1024 + ct * 128);
}

DEV u2 hflags_sample(const Ctx& c, const Params& P) {
  return flag_sample(P, (TILE_T(c) * 56 + 14 * c.v + TILE_P(c) + 2 * (c.lane < 7 ? c.lane : 6)) * 128, WS_FH);
}
DEV void gemm2(Ctx& c, const Params& P, const bf4 (&W1)[7][4][2], u2 hfl, unsigned (&wa)[4], Fin& fin, unsigned pw0) {
  const int T64 = TILE_T(c), v = c.v;
  unsigned l16 = c.lane * 16;
  asm volatile("" : "+v"(l16));
  const unsigned hk = uni(T64 * 56 + 14 * v + TILE_P(c));
  const unsigned fo = WS_FH + hk * 128, fo2 = WS_FH + (hk ^ 1) * 128, fv = (l16 < 112 ? l16 : 96) * 16;
  if (!flags_up(c.tag, hfl)) wait_flags<2>(c.ws, c.tag, ld_flag(c.ws, fv, fo), fv, fo);
  STAMP(12);
#define GWIN 2
  constexpr int WIN = GWIN;
  u4 H[14][4];
  auto hload = [&](int i, int t) __attribute__((always_inline)) {
    H[i][t] = __builtin_amdgcn_raw_buffer_load_b128(c.ws, l16 + t * 1024, WS_H + ((i < 7 ? hk : hk ^ 1) + 2 * (i % 7)) * 4096, SC1);
  };
#pragma unroll
  for (int i = 0; i < WIN; i++)
#pragma unroll
    for (int t = 0; t < 4; t++) hload(i, t);
  SB();
  f4 acc[4][4];
  bf4 W2[7][4][2];
  unsigned pw[4]; u4 xr;
  sfor<0, 14>(LAM(I) {
    constexpr int i = CV(I);
    sfor<0, 16>(LAM(J) {
      constexpr int j = CV(J), t = j >> 2, d = j & 3;
      if constexpr (i == 0) mma0(acc[t][d], cat(W1[0][d][0], W1[0][d][1]), H[0][t]);
      else if constexpr (i < 7) mma(acc[t][d], cat(W1[i][d][0], W1[i][d][1]), H[i][t]);
      else mma(acc[t][d], cat(W2[i - 7][d][0], W2[i - 7][d][1]), H[i][t]);
#define HS2 33
      if constexpr (i * 16 + j == HS2) hfl = ld_flag(c.ws, fv, fo2);
      if constexpr (i == 7 - WIN && j == 2) { if (!flags_up(c.tag, hfl)) wait_flags<2>(c.ws, c.tag, ld_flag(c.ws, fv, fo2), fv, fo2); STAMP(16); }
      if constexpr (i + WIN < 14 && (j & 3) == 3) hload(i + WIN, t);
      if constexpr (i >= 6 && i < 13 && (j & 1) == 0) W2[i - 6][j >> 2][(j >> 1) & 1] = trd(wa[j >> 2] + (i < 9 ? i - 6 : i - 9) * 16384 + ((j >> 1) & 1) * 2048);
#define YSIG 11
      if constexpr (i == YSIG && j == 14) *LDSP(volatile unsigned, LSY + 40) = 1u;
      if constexpr (i == 5 && (j & 3) == 1) { wa[t] += SLOT_C; pin(wa[t]); }
      if constexpr ((i == 8 && (j & 3) == 3)) { wa[t] -= SLOT_C; pin(wa[t]); }
      if constexpr (i == 6 && j == 1) xr = x_load(c, P, 1, fin.oa);
      if constexpr (i >= 10 && (j & 1) && 8 * (i - 10) + (j >> 1) < 17) finish_op<8 * (i - 10) + (j >> 1)>(fin, xr);
      if constexpr (i == 12 && (j & 3) == 3) { pw[t] = pw0 ^ (t * 64); pin(pw[t]); }
      if constexpr (i == 13 && j >= 8) { pin(acc[t - 2][d]); *LDSP(f4, pw[d] + (t - 2) * 16384) = acc[t - 2][d]; }
      SB();
    });
  });
  STAMP(13);
  asm volatile("s_nop 1");
#pragma unroll
  for (int j = 8; j < 16; j++) { pin(acc[j >> 2][j & 3]); *LDSP(f4, pw[j & 3] + (j >> 2) * 16384) = acc[j >> 2][j & 3]; if (j == 11) asm volatile("s_nop 3"); SB(); }
  STAMP(14);
  finish_tile<1>(c, P, fin);
}

__global__ void __launch_bounds__(512, 2) fused_s4_geglu(Params P) {
  Ctx c;
  u64 t00; asm volatile("s_memrealtime %0" : "=s"(t00));
  c.w = __builtin_amdgcn_readfirstlane(threadIdx.x >> 6);
  c.v = c.w & 3; c.lane = threadIdx.x & 63; c.n = c.lane & 15; c.q = c.lane >> 4;
  c.xc = blockIdx.x & 7; c.jj = blockIdx.x >> 3;
  {
    u64 pc; unsigned len;
    asm volatile("s_getpc_b64 %0\n.Lpc%=:\n\ts_mov_b32 %1, fused_end_a-.Lpc%=\n\ts_max_u32 %1, %1, fused_end_b-.Lpc%=" : "=s"(pc), "=s"(len)::"scc");
    if (c.w == 3) touch(mksrd((const void*)pc, len), (c.lane >> CPF) * 128, (c.jj & ((8 << CPF) - 1)) * (8192 >> CPF));
  }
  const bool g1 = c.jj < 28;
  const G1Src gs = g1_src(c);
  asm volatile("s_waitcnt lgkmcnt(0)" : "+s"(t00)); c.t0 = c.tk = (unsigned)t00;
  const KIn kin = kgen_load<0>(c, P);
  if (__builtin_expect(!g1, 0)) ln_block(c, P, kin);
  if (c.w >= 4) {
    WdSrc wds;
    f4 acc[4][4], cv[4], tc[4]; bf4 W1[7][4][2]; unsigned wa[4];
    if (__builtin_expect(g1, 1)) { KExp kx; u4 ur[2]; gemm1<1>(c, P, gs, kin, wds, acc, W1, wa, kx, ur, cv, tc); }
    else {
      wds = wd_src(c, P);
      lds_arrive(LSY + 44, c.lane);
#pragma unroll
      for (int i = 0; i < 7; i++) wd_chunk(c, wds, i);
#pragma unroll
      for (int d = 0; d < 4; d++) { wa[d] = (wd_raddr(c.v, c.n, c.q) ^ (d * 32)) + SLOT_C; pin(wa[d]); }
      waitv<16>();
#pragma unroll
      for (int i = 0; i < 16; i++) W1[i >> 3][(i >> 1) & 3][i & 1] = trd(wa[(i >> 1) & 3] + (i >> 3) * 16384 + (i & 1) * 2048);
      waitl();
    }
#define NPRE 2
#define NPUB 2
    auto w1_read = [&](int i) __attribute__((always_inline)) {
      if (i == 3) {
#pragma unroll
        for (int d = 0; d < 4; d++) { wa[d] -= SLOT_C; pin(wa[d]); }
      }
#pragma unroll
      for (int k = 0; k < 8; k++) W1[i][k >> 1][k & 1] = trd(wa[k >> 1] + (i < 3 ? i : i - 3) * 16384 + (k & 1) * 2048);
      SB();
    };
#pragma unroll
    for (int i = 2; i < NPRE; i++) { if (i == 3) waitv<4>(); if (i == 6) waitv<0>(); w1_read(i); }
    STAMP(8);
    if (g1) { geglu<1>(c, acc, cv, tc); waitv<4>(); } else waitv<0>();
    lds_arrive(LSY + 44, c.lane);
    STAMP(9);
    u2 hfl; Fin fin; unsigned pw0;
#pragma unroll
    for (int i = 0; i < 7; i++) {
      if (i + 2 >= NPRE && i + 2 < 7) w1_read(i + 2);
      if (i >= NPRE) { if (i < 5) asm volatile("s_waitcnt lgkmcnt(15)" ::: "memory"); else if (i == 5) asm volatile("s_waitcnt lgkmcnt(8)" ::: "memory"); else waitl(); }
      if (i == 5) {
#define HD 8
        if (!g1) {
          const unsigned go = WS_FG + (c.xc * 4 + c.jj - 28) * 128;
          wait_flags<4>(c.ws, c.tag, ld_flag(c.ws, 0, go), 0, go);
          __builtin_amdgcn_s_sleep(HD);
        }
        hfl = hflags_sample(c, P);
      }
      wd_chunk(c, wds, 7 + i);
      if (i == NPUB - 1) {
        finish_addr<1>(c, fin); pw0 = c.v * 4096 + c.n * 256 + (c.q ^ (c.n & 7)) * 16; pin(pw0);
        if (g1) h_publish<1, 4 * NPUB>(c);
        STAMP(10);
      }
      SB();
    }
    asm volatile("s_waitcnt vmcnt(8)" : "+v"(hfl)::"memory");
    STAMP(11);
    gemm2(c, P, W1, hfl, wa, fin, pw0);
    asm volatile("fused_end_b:");
  } else {
    if (__builtin_expect(g1, 1)) {
      f4 acc[4][4], cv[4], tc[4]; WdSrc none; bf4 W1[7][4][2]; unsigned wa[4]; KExp kx; u4 ur[2];
      gemm1<0>(c, P, gs, kin, none, acc, W1, wa, kx, ur, cv, tc);
      geglu<0>(c, acc, cv, tc);
      STAMP(8);
      sfor<0, 9>(LAM(K) { kexp_put<CV(K)>(c, kx); });
      u_write(c, ur[0], ur[1]);
    } else {
      const unsigned fv = (16 * c.v + (c.lane >> 2)) * 128;
      wait_flags<8>(c.ws, c.tag, ld_flag(c.ws, fv, WS_FLN), fv, WS_FLN);
      const u4 r0 = u_load(c, u_off(c), 0), r1 = u_load(c, u_off(c), 1);
      KExp kx;
      kexp_pre<0>(c, kx); kexp_pre<1>(c, kx);
      sfor<0, KX_OPS>(LAM(K) { kexp_op<CV(K)>(c, kx); });
      sfor<0, 9>(LAM(K) { kexp_put<CV(K)>(c, kx); });
      u_write(c, r0, r1);
    }
    lds_arrive(LSY, c.lane);
    STAMP(9);
    if (g1) h_publish<0, 0>(c);
    STAMP(10);
    lds_wait<1>(LSY, 4);
    Fin fin;
    finish_addr<0>(c, fin);
    const u4 xr = x_load(c, P, 0, fin.oa);
    conv(c);
    if (__builtin_expect(blockIdx.x == 224 && c.w == 0, 0)) {
#pragma unroll 1
      for (int i = 0; i < 4; i++) {
        const int b = 64 * i + c.lane;
        const unsigned fv = b < 224 ? WS_FC + b * 128 : WS_FLN + (b - 224) * 256;
        wait_flags<16>(c.ws, c.tag, ld_flag(c.ws, fv, 0), fv, 0);
      }
      st_flag(c.ws, c.tag, WS_EPOCH, c.lane);
    }
    sfor<0, 17>(LAM(K) { finish_op<CV(K)>(fin, xr); });
    y_gather(c);
    finish_tile<0>(c, P, fin);
  }
  FINISH();
}

extern "C" void kernel_launch(void* const* d_in, const int* in_sizes, int n_in, void* d_out, int out_size, void* d_ws, size_t ws_size,
                              hipStream_t stream) {
  Params P;
  P.x = (const u16*)d_in[0]; P.are = (const u16*)d_in[1]; P.aim = (const u16*)d_in[2]; P.bre = (const u16*)d_in[3];
  P.bim = (const u16*)d_in[4]; P.cre = (const u16*)d_in[5]; P.cim = (const u16*)d_in[6]; P.ldt = (const u16*)d_in[7];
  P.dsk = (const u16*)d_in[8]; P.wg = (const u16*)d_in[10]; P.wu = (const u16*)d_in[11]; P.wd = (const u16*)d_in[12];
  P.out = (u16*)d_out; P.ws = (char*)d_ws;
  (void)hipFuncSetAttribute((const void*)fused_s4_geglu, hipFuncAttributeMaxDynamicSharedMemorySize, LDS_BYTES);
  fused_s4_geglu<<<256, 512, LDS_BYTES, stream>>>(P);
}
```

```cpp
#include <hip/hip_runtime.h>
#include <hip/hip_bf16.h>
#include <stdint.h>
#pragma clang diagnostic ignored "-Wint-to-pointer-cast"

typedef unsigned long long u64;
typedef unsigned short u16;
typedef __bf16 bf2 __attribute__((ext_vector_type(2)));
typedef __bf16 bf4 __attribute__((ext_vector_type(4)));
typedef __bf16 bf8 __attribute__((ext_vector_type(8)));
typedef float f2 __attribute__((ext_vector_type(2)));
typedef float f4 __attribute__((ext_vector_type(4)));
typedef int i4 __attribute__((ext_vector_type(4)));
typedef unsigned u2 __attribute__((ext_vector_type(2)));
typedef unsigned u4 __attribute__((ext_vector_type(4)));
typedef __amdgpu_buffer_rsrc_t rsrc_t;
#define LDSP(T, a) ((__attribute__((address_space(3))) T*)(a))
#define DEV __device__ __forceinline__
#define SB() __builtin_amdgcn_sched_barrier(0)
#define SC1 16

template <int V> struct IC { static constexpr int value = V; };
template <int B, int E, class Fn> DEV void sfor(Fn&& f) { if constexpr (B < E) { f(IC<B>{}); sfor<B + 1, E>(f); } }
#define LAM(I) [&](auto I) __attribute__((always_inline))
#define CV(I) decltype(I)::value

constexpr int L = 2048, D = 512, F = 1792;

constexpr int YS = 64, HSZ = 64, CSZ = 32, LSZ = 32;
constexpr unsigned WS_EPOCH = 0;
constexpr unsigned WS_FLN = 8192;
constexpr unsigned WS_FG = 4096;
constexpr unsigned WS_STATS = 16384;
constexpr unsigned WS_CPART = 65536;
constexpr unsigned WS_FC = 327680;
constexpr unsigned WS_FH = 524288;
constexpr unsigned WS_FY = 786432;
constexpr unsigned WS_XNT = 1u << 20;
constexpr unsigned WS_YT = 3u << 20;
constexpr unsigned WS_H = 5u << 20;
constexpr unsigned WS_BYTES = WS_H + 32u * 56u * 4096u;

constexpr unsigned SLOT_C = 98816;
constexpr unsigned slot_of(int s) { return s % 3 == 0 ? 0u : s % 3 == 1 ? 49152u : SLOT_C; }
constexpr unsigned wd_base(int i) { return i < 3 ? SLOT_C + i * 16384 : (i - 3) * 16384; }
constexpr unsigned LKC = 65536;
constexpr unsigned KCS = 4160, KCH = 4 * KCS;
constexpr unsigned LKG = 98304;
constexpr unsigned LU = 147968;
constexpr unsigned UCH = 4608;
constexpr unsigned LCS = LU + 9216;
constexpr unsigned LST = LU + 10240;
constexpr unsigned LKT = 160064;
constexpr unsigned LSY = 163136;
constexpr unsigned LJK = 163328;
constexpr unsigned LDS_BYTES = 163840;

struct Params {
  const u16 *are, *aim, *ldt, *dsk, *bre, *bim, *cre, *cim;
  char* ws;
  const u16 *x, *wg, *wu, *wd;
  u16* out;
};

DEV i4 mksrd(const void* p, unsigned bytes) {
  u64 a = (u64)p; i4 r; r[0] = (int)(unsigned)a; r[1] = (int)(unsigned)(a >> 32); r[2] = (int)bytes; r[3] = 0x00020000; return r;
}
DEV rsrc_t mkrs(const void* p, unsigned bytes) { return __builtin_amdgcn_make_buffer_rsrc((void*)p, 0, (int)bytes, 0x00020000); }
DEV void touch(i4 srd, unsigned voff, unsigned soff) {
  asm volatile("s_mov_b32 m0, %3\n\ts_nop 0\n\tbuffer_load_dword %1, %0, %2 offen lds" ::"s"(srd), "v"(voff), "s"(soff), "s"(LJK) : "memory");
}
DEV void st128(u4 v, rsrc_t rs, unsigned voff, unsigned soff) {
  __builtin_amdgcn_raw_buffer_store_b128(v, rs, voff, soff, SC1);
  asm volatile("s_nop 1" ::"v"(v) : "memory");
}
template <int N> DEV void waitv() { asm volatile("s_waitcnt vmcnt(%0)" ::"i"(N) : "memory"); }
DEV void waitl() { __builtin_amdgcn_s_waitcnt(0xC07F); asm volatile("" ::: "memory"); }
DEV void bar() { asm volatile("" ::: "memory"); __builtin_amdgcn_s_waitcnt(0xC07F); asm volatile("s_barrier" ::: "memory"); }
DEV void set_m0(unsigned base, unsigned imm) { asm volatile("s_add_u32 m0, %0, %1" ::"s"(base), "i"(imm) : "memory", "scc"); }
template <int OFF> DEV void glds_at(i4 srd, unsigned voff, unsigned soff) {
  asm volatile("buffer_load_dwordx4 %1, %0, %2 offen offset:%3 lds" ::"s"(srd), "v"(voff), "s"(soff), "i"(OFF) : "memory");
}
template <class T> DEV void pin(T& x) { asm volatile("" : "+v"(x)); }
template <class T> DEV void pins(T& x) { asm volatile("" : "+s"(x)); }
DEV bf4 trd(unsigned a) { return __builtin_amdgcn_ds_read_tr16_b64_v4bf16(LDSP(bf4, a)); }
DEV bf8 cat(bf4 a, bf4 b) { return __builtin_shufflevector(a, b, 0, 1, 2, 3, 4, 5, 6, 7); }
template <class TA, class TB> DEV void mma(f4& c, TA a, TB b) { asm volatile("v_mfma_f32_16x16x32_bf16 %0, %1, %2, %0" : "+v"(c) : "v"(a), "v"(b)); }
template <class TA, class TB> DEV void mma0(f4& c, TA a, TB b) { asm volatile("v_mfma_f32_16x16x32_bf16 %0, %1, %2, 0" : "=&v"(c) : "v"(a), "v"(b)); }
DEV void mma_pick(f4& c, u4 a, bf8 b0, bf8 b1, bf8 b2, bf8 b3, int t) {
  asm volatile("s_bitcmp1_b32 %6, 1\n\ts_cbranch_scc1 .Lp2_%=\n\ts_bitcmp1_b32 %6, 0\n\ts_cbranch_scc1 .Lp1_%=\n\t"
               "v_mfma_f32_16x16x32_bf16 %0, %1, %2, %0\n\ts_branch .Lp4_%=\n"
               ".Lp1_%=:\n\tv_mfma_f32_16x16x32_bf16 %0, %1, %3, %0\n\ts_branch .Lp4_%=\n"
               ".Lp2_%=:\n\ts_bitcmp1_b32 %6, 0\n\ts_cbranch_scc1 .Lp3_%=\n\t"
               "v_mfma_f32_16x16x32_bf16 %0, %1, %4, %0\n\ts_branch .Lp4_%=\n"
               ".Lp3_%=:\n\tv_mfma_f32_16x16x32_bf16 %0, %1, %5, %0\n"
               ".Lp4_%=:" : "+v"(c) : "v"(a), "v"(b0), "v"(b1), "v"(b2), "v"(b3), "s"(t) : "scc");
}
DEV void mma_fence() { asm volatile("s_nop 7\n\ts_nop 1" ::: "memory"); }
DEV float blo(unsigned v) { return __uint_as_float(v << 16); }
DEV float bhi(unsigned v) { return __uint_as_float(v & 0xffff0000u); }
DEV f2 unpk(unsigned v) { return f2{blo(v), bhi(v)}; }
DEV unsigned pk(f2 v) { return __builtin_bit_cast(unsigned, __builtin_convertvector(v, bf2)); }
DEV float rbf(float v) { return blo(pk(f2{v, v})); }
DEV f2 fma2(f2 a, f2 b, f2 c) { return __builtin_elementwise_fma(a, b, c); }
template <int CTRL> DEV float dpp_add(float v) {
  return v + __builtin_bit_cast(float, __builtin_amdgcn_update_dpp(0, __builtin_bit_cast(int, v), CTRL, 0xf, 0xf, true));
}
DEV void swap16(unsigned& a, unsigned& b) { auto r = __builtin_amdgcn_permlane16_swap(a, b, false, false); a = r[0]; b = r[1]; }
DEV void swap32(unsigned& a, unsigned& b) { auto r = __builtin_amdgcn_permlane32_swap(a, b, false, false); a = r[0]; b = r[1]; }
DEV float swap16_add(float a, float b) { unsigned x = __float_as_uint(a), y = __float_as_uint(b); swap16(x, y); return __uint_as_float(x) + __uint_as_float(y); }
DEV float swap32_add(float a, float b) { unsigned x = __float_as_uint(a), y = __float_as_uint(b); swap32(x, y); return __uint_as_float(x) + __uint_as_float(y); }
DEV float rdl(float v, int l) { return __builtin_bit_cast(float, __builtin_amdgcn_readlane(__builtin_bit_cast(int, v), l)); }
DEV unsigned uni(unsigned v) { return (unsigned)__builtin_amdgcn_readfirstlane((int)v); }
DEV u4 row_load(i4 srd, unsigned voff, unsigned soff) {
  u4 r; asm volatile("buffer_load_dwordx4 %0, %2, %1, %3 offen" : "=v"(r) : "s"(srd), "v"(voff), "s"(soff) : "memory"); return r;
}
constexpr unsigned SPIN_CAP = 1u << 15;
DEV u2 ld_flag(rsrc_t rs, unsigned voff, unsigned soff) { return __builtin_amdgcn_raw_buffer_load_b64(rs, voff, soff, SC1); }
DEV unsigned flag_off(int lane, int size) { return lane < size / 8 ? lane * 8 : 0x7ffffff0u; }
DEV void st_flag(rsrc_t rs, u2 tag, unsigned soff, int lane, int size = 128) { __builtin_amdgcn_raw_buffer_store_b64(tag, rs, flag_off(lane, size), soff, SC1); }
DEV bool flags_up(u2 tag, u2 v) { return __builtin_amdgcn_ballot_w64(__builtin_bit_cast(u64, v) != __builtin_bit_cast(u64, tag)) == 0; }
template <int NAP> DEV void wait_flags(rsrc_t rs, u2 tag, u2 v, unsigned voff, unsigned soff) {
#pragma nounroll
  for (unsigned spin = 0; spin < SPIN_CAP; ++spin) {
    if (flags_up(tag, v)) break;
    __builtin_amdgcn_s_sleep(NAP);
    asm volatile("" ::: "memory");
    v = ld_flag(rs, voff, soff);
  }
  asm volatile("" : "+v"(v)::"memory");
}
DEV void lds_post(unsigned a, int lane) { asm volatile("" ::: "memory"); if (lane == 0) *LDSP(volatile unsigned, a) = 1u; }
template <int NAP> DEV void lds_wait(unsigned a, unsigned want) {
#pragma nounroll
  for (unsigned spin = 0; spin < (SPIN_CAP << 6); ++spin) {
    if (uni(*LDSP(volatile unsigned, a)) >= want) break;
    if (NAP) __builtin_amdgcn_s_sleep(NAP);
  }
  asm volatile("" ::: "memory");
}
DEV unsigned lds_arrive(unsigned a, int lane) {
  asm volatile("s_waitcnt lgkmcnt(0)" ::: "memory");
  unsigned o = 0;
  if (lane == 0) o = __hip_atomic_fetch_add(LDSP(unsigned, a), 1u, __ATOMIC_RELAXED, __HIP_MEMORY_SCOPE_WORKGROUP);
  return uni(o);
}

#define TILE_P(c) (c.xc >> 2)
#define TILE_G(c) (c.xc & 3)
#define TILE_T(c) (TILE_G(c) * 8 + 2 * (c.jj & 3) + TILE_P(c))
#define TILE_C(c) (c.jj >> 2)
struct Ctx {
  int xc, jj, w, v, lane, n, q;
  u2 tag;
  rsrc_t ws;
};

struct alignas(16) F1 { float v; };
DEV float red4(float a, float b, float c, float d) {
  float s = swap16_add(swap32_add(a, b), swap32_add(c, d));
  s = dpp_add<0x128>(s); s = dpp_add<0x124>(s); s = dpp_add<0x122>(s); s = dpp_add<0x121>(s);
  return s;
}
DEV int ln_row(int r) { return r * 1088 + (r & 8) * 4; }
DEV void ln_compute(const Ctx& c, const u4* rows, unsigned img, int tok0) {
  f2 x[4][4]; float t[4];
#pragma unroll
  for (int k = 0; k < 4; k++) {
#pragma unroll
    for (int i = 0; i < 4; i++) x[k][i] = unpk(rows[k][i]);
    const f2 s = (x[k][0] + x[k][1]) + (x[k][2] + x[k][3]); t[k] = s.x + s.y;
  }
  float mean = red4(t[0], t[2], t[1], t[3]) * (1.f / D);
#pragma unroll
  for (int k = 0; k < 4; k++) {
    const float m = rdl(mean, 16 * k);
    f2 qq = {0.f, 0.f};
#pragma unroll
    for (int i = 0; i < 4; i++) { x[k][i] -= m; qq = fma2(x[k][i], x[k][i], qq); }
    t[k] = qq.x + qq.y;
  }
  float var = red4(t[0], t[2], t[1], t[3]) * (1.f / D);
  const unsigned st = pk(f2{mean, __builtin_amdgcn_sqrtf(rbf(rbf(var) + 1e-5f))});
  const float r = __builtin_amdgcn_rcpf(bhi(st)), o = (mean - blo(st)) * r;
#pragma unroll
  for (int k = 0; k < 4; k++) {
    const float rk = rdl(r, 16 * k), ok = rdl(o, 16 * k);
    u4 nv;
#pragma unroll
    for (int i = 0; i < 4; i++) nv[i] = pk(fma2(x[k][i], f2{rk, rk}, f2{ok, ok}));
    *LDSP(u4, img + ln_row(4 * c.w + k) + c.lane * 16) = nv;
  }
  if (c.n == 0) *LDSP(unsigned, img + 1024 + (c.w >> 1) * 1088 + (4 * (c.w & 1) + c.q) * 4) = st;
}
DEV void ln_store(const Ctx& c, unsigned img, int tok0) {
  const unsigned a = img + ln_row(8 * c.q + (c.n >> 2)) + (64 * c.w + 4 * (c.n & 3)) * 2;
  if (c.w == 7) __builtin_amdgcn_raw_buffer_store_b32(*LDSP(unsigned, img + 1024 + ((c.lane >> 3) & 3) * 1088 + (c.lane & 7) * 4), c.ws,
                                                      c.lane < 32 ? c.lane * 4 : 0x7ffffff0u, WS_STATS + tok0 * 4, SC1);
  bf8 t[4];
#pragma unroll
  for (int i = 0; i < 4; i++) t[i] = cat(trd(a + 32 * i), trd(a + 32 * i + 4 * 1088));
#pragma unroll
  for (int i = 0; i < 4; i++) st128(__builtin_bit_cast(u4, t[i]), c.ws, c.n * 64 + c.q * 16 + i * 1024, WS_XNT + tok0 * 1024 + c.w * 4096);
}
DEV unsigned u_off(const Ctx& c) { return (c.lane >> 3) * 32768 + (c.lane & 7) * 16; }
DEV u4 u_load(const Ctx& c, unsigned uv, int i) {
  return __builtin_amdgcn_raw_buffer_load_b128(c.ws, uv, WS_XNT + (16 * c.v + 8 * i) * 32768 + (c.xc * 32 + c.jj) * 128, SC1);
}
DEV void u_write(const Ctx& c, u4 r0, u4 r1) {
  const unsigned a = LU + ((c.lane >> 2) & 1) * UCH + 512 + (16 * c.v + (c.lane >> 3)) * 64 + (c.lane & 3) * 16;
  *LDSP(u4, a) = r0;
  *LDSP(u4, a + 512) = r1;
  if (c.v == 0) *LDSP(u4, LU + (c.lane & 1) * UCH + (c.lane >> 1) * 16) = u4{0, 0, 0, 0};
}
DEV u2 flag_sample(const Params& P, unsigned voff, unsigned soff) {
  u2 r; asm volatile("buffer_load_dwordx2 %0, %2, %1, %3 offen sc1" : "=v"(r) : "s"(mksrd(P.ws, WS_BYTES)), "v"(voff), "s"(soff) : "memory"); return r;
}

struct C2 { f2 re, im; };
DEV C2 cmul(C2 a, C2 b) { C2 r; r.re = fma2(a.re, b.re, -(a.im * b.im)); r.im = fma2(a.re, b.im, a.im * b.re); return r; }
DEV C2 zpow(f2 al, f2 th, float e) {
  const f2 a = al * e, t0 = th * e;
  const f2 t = {__builtin_amdgcn_fractf(t0.x), __builtin_amdgcn_fractf(t0.y)};
  const f2 m = {__builtin_amdgcn_exp2f(a.x), __builtin_amdgcn_exp2f(a.y)};
  C2 r; r.re = m * f2{__builtin_amdgcn_cosf(t.x), __builtin_amdgcn_cosf(t.y)}; r.im = m * f2{__builtin_amdgcn_sinf(t.x), __builtin_amdgcn_sinf(t.y)};
  return r;
}
struct KIn { unsigned are, aim, bre, bim, cre, cim, ldt, dsk; u64 epoch; };
DEV unsigned par_load(const u16* p, unsigned voff) {
  unsigned r; asm volatile("global_load_dword %0, %1, %2" : "=v"(r) : "v"(voff), "s"(p) : "memory"); return r;
}
DEV unsigned spar_load(const u16* p, unsigned off) { unsigned r; asm volatile("s_load_dword %0, %1, %2" : "=s"(r) : "s"(p), "s"(off) : "memory"); return r; }
template <int ROLE> DEV KIn kgen_load(const Ctx& c, const Params& P) {
  const unsigned pair = c.xc * 32 + c.jj, o = (2 * pair + (c.w & 1)) * 256 + c.lane * 4;
  KIn r;
  r.are = par_load(P.are, o); r.aim = par_load(P.aim, o);
  r.ldt = spar_load(P.ldt, pair * 4); r.dsk = spar_load(P.dsk, pair * 4);
  asm volatile("s_load_dwordx2 %0, %1, 0" : "=s"(r.epoch) : "s"(P.ws + WS_EPOCH) : "memory");
  if (ROLE == 0 && c.w < 2) { r.bre = par_load(P.bre, o); r.bim = par_load(P.bim, o); r.cre = par_load(P.cre, o); r.cim = par_load(P.cim, o); }
  return r;
}
template <int ROLE, int N> DEV void kgen_take(Ctx& c, const Params& P, KIn& k) {
  KIn o; char* ws = P.ws;
  asm volatile("s_waitcnt lgkmcnt(0)\n\ts_mov_b32 %0, %4\n\ts_mov_b32 %1, %5\n\ts_mov_b64 %2, %6"
               : "=&s"(o.ldt), "=&s"(o.dsk), "=&s"(o.epoch), "+s"(ws) : "s"(k.ldt), "s"(k.dsk), "s"(k.epoch) : "memory");
  asm volatile("s_waitcnt vmcnt(%4)\n\tv_mov_b32 %0, %2\n\tv_mov_b32 %1, %3" : "=&v"(o.are), "=&v"(o.aim) : "v"(k.are), "v"(k.aim), "i"(N) : "memory");
  if (ROLE == 0) asm volatile("v_mov_b32 %0, %4\n\tv_mov_b32 %1, %5\n\tv_mov_b32 %2, %6\n\tv_mov_b32 %3, %7"
               : "=&v"(o.bre), "=&v"(o.bim), "=&v"(o.cre), "=&v"(o.cim) : "v"(k.bre), "v"(k.bim), "v"(k.cre), "v"(k.cim) : "memory");
  k = o;
  c.tag = u2{(unsigned)(o.epoch + 1), (unsigned)((o.epoch + 1) >> 32)};
  c.ws = mkrs(ws, WS_BYTES);
}
constexpr unsigned LKB = LKG + 44416;
struct KPre { float del, g0, g1, rev; unsigned wa, wb, rd, sh, rs, ta, tb, s0, s1; bool sw; };
template <int ROLE> DEV void kgen_pre(const Ctx& c, KPre& k) {
  const bool hi = c.v >= 2;
  const int ch = c.w & 1;
  const unsigned T = ROLE == 0 ? (hi ? LKG : LKB) + ch * 8672 : (hi ? LKG + 17344 + ch * 8672 : LKG + 34688 + ch * 4864);
  k.del = ROLE == 0 ? (hi ? 1.f : 8.f) : (hi ? 128.f : 2.f);
  k.g0 = ROLE == 0 ? (hi ? 128.f : 64.f) : (hi ? 512.f : 256.f);
  k.g1 = hi ? 516.f : 2048.f;
  k.rev = (ROLE == 0 && !hi) ? -0.15915494309189535f : 0.15915494309189535f;
  k.wa = T + c.lane * 8; k.wb = k.wa + ((ROLE == 1 && !hi) ? 4 : 8) * 544; k.rd = T + c.n * 544 + c.q * 16;
  k.sh = ch ? 0 : 16;
  k.rs = hi ? 1088 : 544;
  pin(k.del); pin(k.g0); pin(k.wa); pin(k.rd);
  if (ROLE == 0) pin(k.rev); else { pin(k.g1); pin(k.wb); }
}
template <int ROLE> DEV void ktab_pre(const Ctx& c, KPre& k) {
  const bool hi = c.v >= 2;
  const int n = c.n, q = c.q;
  const unsigned tb = LKT + (c.w & 1) * 1536;
  if (ROLE == 0) { k.ta = hi ? tb + 256 * (q >> 1) + 8 * (q & 1) + 16 * n : LJK + c.lane * 8; pin(k.ta); }
  else {
    const bool far = hi && q >= 2;
    k.ta = tb + 128 * q + (!hi ? (q < 2 ? 512 : 1024) + 8 * n : 768 + (q < 2 ? 4 * n : 62 * (n & 1) + 2 * n));
    k.tb = k.ta + (!hi ? 4 : q < 2 ? 64 : 32);
    k.s0 = !far ? 0x03020100u : (n & 1) ? 0x01000504u : 0x05040100u;
    k.s1 = k.s0 + (far ? 0x02020202u : 0x04040404u);
    k.sw = far && (n & 1);
    pin(k.ta); pin(k.tb); pin(k.s0); pin(k.s1);
  }
}
template <int ROLE, class Hook>
DEV void kgen_rows(const Ctx& c, const KPre& p, const KIn& k, bf8 (&A)[8], Hook&& hook) {
  const float dt = rbf(__builtin_amdgcn_exp2f(bhi(k.ldt << p.sh) * 1.4426950408889634f));
  const f2 al = (unpk(k.are) * dt) * 1.4426950408889634f;
  const f2 th = (unpk(k.aim) * dt) * p.rev;
  auto put = [&](unsigned ad, C2 z) __attribute__((always_inline)) { *LDSP(u2, ad) = u2{pk(z.re), pk(z.im)}; };
  C2 st = zpow(al, th, p.del);
  pin(st.re); pin(st.im);
  hook(IC<0>{});
  C2 a, b, e;
  if (ROLE == 0) {
    a = C2{f2{1.f, 1.f}, f2{0.f, 0.f}}; b = zpow(al, th, p.g0);
    if (c.w < 2) { a = cmul(C2{unpk(k.cre), -unpk(k.cim)}, C2{unpk(k.bre) * dt, unpk(k.bim) * -dt}); b = cmul(b, a); }
  } else {
    a = zpow(al, th, p.g0);
    if (c.w >= 6) b = cmul(a, a); else b = zpow(al, th, 384.f);
  }
  pin(b.re); pin(b.im);
  hook(IC<1>{});
  sfor<0, 4>(LAM(I) {
    constexpr int i = CV(I);
    put(p.wa + i * (ROLE ? p.rs : 544), a); put(p.wb + i * 544, b);
    hook(IC<2 + i>{});
    if constexpr (ROLE == 0 || i < 3) a = cmul(a, st); else a = zpow(al, th, p.g1);
    b = cmul(b, st);
  });
  if (ROLE == 1 && c.w < 6) { put(p.wa + 8 * 544, a); hook(IC<6>{}); hook(IC<7>{}); hook(IC<8>{}); }
  else sfor<4, 8>(LAM(I) {
    constexpr int i = CV(I);
    put(p.wa + (ROLE ? 2 * i - 7 : i) * 544, a); put(p.wb + i * 544, b);
    if constexpr (i < 7) { hook(IC<2 + i>{}); a = cmul(a, st); b = cmul(b, st); }
  });
#pragma unroll
  for (int i = 0; i < 8; i++) A[i] = *LDSP(bf8, p.rd + i * 64);
}
DEV void kgen_cols(const Ctx& c, bf8 (&B)[8]) {
#pragma unroll
  for (int i = 0; i < 8; i++) B[i] = *LDSP(bf8, LKB + (c.w & 1) * 8672 + c.n * 544 + c.q * 16 + i * 64);
}
DEV void kgen_mma(f4& t, const bf8 (&A)[8], const bf8 (&B)[8]) {
  mma0(t, A[0], B[0]);
#pragma unroll
  for (int i = 1; i < 8; i++) mma(t, A[i], B[i]);
}
struct alignas(16) U1 { unsigned v; };
struct KTab { f4 t; U1 a, b, p[2]; };
constexpr int KT_OPS = 9;
template <int ROLE, int k> DEV void ktab_op(const Ctx& c, const KPre& p, KTab& z) {
  if constexpr (ROLE == 0) {
    if constexpr (k == 0) { pin(z.t); z.t[0] += __uint_as_float(z.a.v); pin(z.t); }
    if constexpr (k == 1) { z.p[1].v = pk(f2{z.t[2], z.t[3]}); pin(z.p[1].v); }
    if constexpr (k == 2) { z.p[0].v = pk(f2{z.t[0], z.t[1]}); pin(z.p[0].v); }
    if constexpr (k == 3) *LDSP(u2, p.ta) = u2{z.p[0].v, z.p[1].v};
  } else {
    if constexpr (k == 0) { pin(z.t); z.p[0].v = pk(f2{z.t[0], z.t[1]}); pin(z.p[0].v); }
    if constexpr (k == 1) { z.p[1].v = pk(f2{z.t[2], z.t[3]}); pin(z.p[1].v); }
    if constexpr (k == 2) { z.b.v = p.sw ? z.p[0].v : z.p[1].v; pin(z.b.v); }
    if constexpr (k == 3) { z.a.v = p.sw ? z.p[1].v : z.p[0].v; pin(z.a.v); }
    if constexpr (k == 4) { z.b.v = (unsigned)__builtin_amdgcn_update_dpp((int)z.b.v, (int)z.b.v, 0xb1, 0xc, 0xf, false); pin(z.b.v); }
    if constexpr (k == 5) { z.p[0].v = __builtin_amdgcn_perm(z.b.v, z.a.v, p.s0); pin(z.p[0].v); }
    if constexpr (k == 6) { z.p[1].v = __builtin_amdgcn_perm(z.b.v, z.a.v, p.s1); pin(z.p[1].v); }
    if constexpr (k == 7) *LDSP(unsigned, p.ta) = z.p[0].v;
    if constexpr (k == 8) *LDSP(unsigned, p.tb) = z.p[1].v;
  }
}
DEV void ktab_skip(const Ctx& c, const KPre& p, KTab& z, unsigned dsk) { z.a.v = (c.w >= 2 && c.lane == 0) ? (dsk << p.sh) & 0xffff0000u : 0u; pin(z.a.v); }
typedef unsigned u2a __attribute__((ext_vector_type(2), aligned(4)));
struct KExp { U1 a0, a1, a2, a3, s[7], wr, h0, pv[2][2], o[2][4][4], es[3]; u2 e[4]; };
template <int PART> DEV void kexp_pre(const Ctx& c, KExp& z) {
  const int kh = c.v >> 1, l = c.lane;
  const unsigned CC = LKT + (c.v & 1) * 1536;
  const bool up = l >= 32, odd = l & 1;
  if (PART == 0) {
    z.s[0].v = (kh || up) ? 0x01000100u : 0x03020100u;
    z.s[1].v = kh ? 0x03020100u : up ? 0x05040302u : 0x07060504u;
    z.s[2].v = kh ? 0x03020302u : z.s[0].v;
    z.s[3].v = kh ? 0x05040504u : z.s[1].v;
    z.s[4].v = odd ? 0x03020302u : 0x01000100u;
    z.s[5].v = odd ? 0x05040504u : 0x03020100u;
    z.s[6].v = odd ? 0x05040504u : 0x03020302u;
    z.es[0].v = kh ? 0x01000100u : 0x0c0c0c0cu; z.es[1].v = kh ? 0x05040302u : 0x0c0c0c0cu; z.es[2].v = kh ? 0x05040504u : 0x03020302u;
    asm volatile("" : "+s"(z.es[0].v), "+s"(z.es[1].v), "+s"(z.es[2].v));
    asm volatile("" : "+v"(z.s[0].v), "+v"(z.s[1].v), "+v"(z.s[2].v), "+v"(z.s[3].v), "+v"(z.s[4].v), "+v"(z.s[5].v), "+v"(z.s[6].v));
  } else {
    z.a0.v = CC + (kh ? 768 + l * 4 : up ? 256 + l * 8 : l * 16);
    z.a1.v = z.a0.v + (kh ? 0 : up ? 4 : 8);
    z.a2.v = CC + 1024 + 128 * kh + (l >> 1) * 4;
    z.a3.v = CC + 764;
    z.wr.v = LKC + (c.v & 1) * KCH + 32 + kh * 1024 + l * 16;
    asm volatile("" : "+v"(z.a0.v), "+v"(z.a1.v), "+v"(z.a2.v), "+v"(z.a3.v), "+v"(z.wr.v));
  }
}
constexpr int KX_OPS = 45;
template <int k> DEV void kexp_op(const Ctx& c, KExp& z) {
  auto set = [&](U1& d, unsigned x) __attribute__((always_inline)) { d.v = x; pin(d.v); };
  if constexpr (k == 0) z.e[0] = *LDSP(u2a, z.a0.v);
  else if constexpr (k == 1) z.e[1] = *LDSP(u2a, z.a1.v);
  else if constexpr (k == 2) z.e[2] = *LDSP(u2a, z.a2.v);
  else if constexpr (k == 3) z.e[3] = *LDSP(u2a, z.a3.v);
  else if constexpr (k == 4) z.h0.v = *LDSP(unsigned, z.a3.v + 256);
  else if constexpr (k < 9) { constexpr int i = k - 5; set(z.o[0][0][i], __builtin_amdgcn_perm(z.e[i >> 1][1], z.e[i >> 1][0], z.s[i].v)); }
  else if constexpr (k < 13) { constexpr int i = k - 9;
    if constexpr (i == 1) asm volatile("v_mov_b32 %0, %1" : "=v"(z.o[1][0][1].v) : "v"(z.o[1][0][0].v));
    else set(z.o[1][0][i], __builtin_amdgcn_perm(z.e[2][1], z.e[2][0], z.s[i ? 3 + i : 4].v));
  }
  else if constexpr (k == 13) { z.pv[0][0].v = __builtin_amdgcn_perm(z.e[3][1], z.e[3][0], z.es[0].v); pin(z.pv[0][0].v); }
  else if constexpr (k == 14) { z.pv[0][1].v = __builtin_amdgcn_perm(z.e[3][1], z.e[3][0], z.es[1].v); pin(z.pv[0][1].v); }
  else if constexpr (k == 15) { z.pv[1][0].v = __builtin_amdgcn_perm(z.e[2][0], z.h0.v, z.es[2].v); pin(z.pv[1][0].v); }
  else if constexpr (k == 16) { z.pv[1][1].v = z.o[1][0][0].v; pin(z.pv[1][1].v); }
  else if constexpr (k < 21) {
    constexpr int i = (k - 17) >> 1, j = (k - 17) & 1;
    z.pv[i][j].v = (unsigned)__builtin_amdgcn_update_dpp((int)z.pv[i][j].v, (int)z.o[i][0][2 + j].v, 0x138, 0xf, 0xf, false); pin(z.pv[i][j].v);
  } else {
    constexpr int i = (k - 21) / 12, cp = 1 + (k - 21) % 12 / 4, j = (k - 21) % 4;
    auto dw = [&](int m) __attribute__((always_inline)) { return m < 0 ? z.pv[i][2 + m].v : z.o[i][0][m].v; };
    if constexpr (cp == 2) asm volatile("v_mov_b32 %0, %1" : "=v"(z.o[i][2][j].v) : "v"(dw(j - 1)));
    else asm volatile("v_alignbit_b32 %0, %1, %2, 16" : "=v"(z.o[i][cp][j].v) : "v"(dw(j - (cp >> 1))), "v"(dw(j - 1 - (cp >> 1))));
  }
}
template <int k> DEV void kexp_put(const Ctx& c, const KExp& z) {
  if constexpr (k < 8) *LDSP(u4, z.wr.v + (k >> 2) * 2048 + (k & 3) * KCS) = u4{z.o[k >> 2][k & 3][0].v, z.o[k >> 2][k & 3][1].v, z.o[k >> 2][k & 3][2].v, z.o[k >> 2][k & 3][3].v};
  else *LDSP(unsigned, LKC + (c.lane >> 3) * KCS + (c.lane & 7) * 4) = 0u;
}

struct WdSrc { i4 srd; unsigned voff[4], par; };
DEV WdSrc wd_src(const Ctx& c, const Params& P) {
  WdSrc s; s.srd = mksrd(P.wd, F * D * 2); s.par = TILE_P(c) * 32768;
  const int r = c.lane >> 3;
#pragma unroll
  for (int k = 0; k < 4; k++) { s.voff[k] = (448 * c.v + r) * 1024 + TILE_C(c) * 128 + (((c.lane & 7) ^ (r & 6)) * 16) + k * 7168; pin(s.voff[k]); }
  return s;
}
DEV unsigned wd_raddr(int v, int n, int q) { return v * 4096 + (4 * q + (n >> 2)) * 128 + (((2 * q + (n >> 3)) & 3) * 32) + (n & 3) * 8; }
DEV void wd_ld(const WdSrc& s, int i, int r) {
  const unsigned so = s.par ^ ((i % 7) * 65536 + (i / 7) * 32768);
  if (r == 0) glds_at<0>(s.srd, s.voff[0], so);
  else if (r == 1) glds_at<1024>(s.srd, s.voff[1], so);
  else if (r == 2) glds_at<2048>(s.srd, s.voff[2], so);
  else glds_at<3072>(s.srd, s.voff[3], so);
}
DEV void wd_chunk(const Ctx& c, const WdSrc& s, int i) {
  set_m0(c.v * 4096, wd_base(i % 7)); asm volatile("s_nop 0");
#pragma unroll
  for (int r = 0; r < 4; r++) wd_ld(s, i, r);
}

struct Frags { bf4 a[4][2]; bf8 b[4]; };
DEV void rd_piece(Frags& Fr, int i, unsigned wa0, unsigned wa1, unsigned xa, int ks) {
  if (i == 0) Fr.b[0] = *LDSP(bf8, xa);
  else if (i < 9) { const int t = (i - 1) >> 1, h = (i - 1) & 1; Fr.a[t][h] = trd(((t & 1) ? wa1 : wa0) + (t >> 1) * 16384 + ks * 8192 + h * 1024); }
  else Fr.b[i - 8] = *LDSP(bf8, xa + (i - 8) * 2048);
}

struct G1Src { i4 sx, sg, su; unsigned vx0, vx1, vw0, vw1; int k0; };
DEV G1Src g1_src(const Ctx& c) {
  const int p = (c.xc & 3) * 4 + (c.jj & 3), ft = (c.xc >> 2) * 7 + (c.jj >> 2);
  G1Src g;
  g.k0 = 2 * (c.xc & 3);
  const int xr = c.lane >> 3;
  g.vx0 = (p * 128 + 16 * c.w + xr) * 1024 + (((c.lane & 7) ^ (xr >> 1)) * 16);
  g.vx1 = (g.vx0 ^ 64) + 8192 - 1024;
  g.vw0 = (8 * c.w + c.q) * 3584 + ft * 256 + ((c.n ^ ((c.q | ((c.w & 1) << 2)) << 1)) * 16);
  g.vw1 = g.vw0 + 4 * 3584 - 1024;
  asm volatile("" : "+v"(g.vx0), "+v"(g.vx1), "+v"(g.vw0), "+v"(g.vw1));
  return g;
}
DEV void g1_m0(const Ctx& c, int s, int i) { set_m0(c.w * 2048, slot_of(s) + (i >> 1) * 16384); }
DEV void g1_ld(const G1Src& g, int s, int i) {
  const int sk = (s + g.k0) & 7;
  if (i == 0) glds_at<0>(g.sx, g.vx0, sk * 128);
  else if (i == 1) glds_at<1024>(g.sx, g.vx1, sk * 128);
  else if (i & 1) glds_at<1024>(i > 3 ? g.su : g.sg, g.vw1, sk * 64 * 3584);
  else glds_at<0>(i > 3 ? g.su : g.sg, g.vw0, sk * 64 * 3584);
}
struct G1Rot { i4 srd[2]; unsigned vo[2], dst[4], so[2]; };
DEV G1Rot g1_rot(const Ctx& c, const G1Src& g) {
  const int mine = c.jj & 3;
  G1Rot r;
#pragma unroll
  for (int k = 0; k < 2; k++) {
    const bool up = ((mine >> 1) ^ k) & 1;
    r.srd[k] = g.sg; r.srd[k][0] = up ? g.su[0] : g.sg[0]; r.srd[k][1] = up ? g.su[1] : g.sg[1];
    r.vo[k] = g.vw0 + ((mine ^ k) & 1) * 4 * 3584;
    r.so[k] = (g.k0 + k) * 64 * 3584;
  }
#pragma unroll
  for (int k = 0; k < 4; k++) r.dst[k] = 16384 + ((mine ^ k) >> 1) * 16384 + c.w * 2048 + ((mine ^ k) & 1) * 1024;
  return r;
}
template <unsigned IT0, unsigned IT1> DEV void g1_fetch(const Ctx& c, const G1Src& gs, const G1Rot& rot) {
    const unsigned bx0 = gs.vx0 - 65536, bx1 = gs.vx1 - 65536, bw0 = rot.vo[0] - 32 * 3584, bw1 = rot.vo[1] - 32 * 3584;
    const unsigned ox0 = gs.k0 * 128, ow0 = rot.so[0], dx = c.v * 2048, d1 = rot.dst[1] - 8192, d2 = rot.dst[2] - 8192, d3 = rot.dst[3] - 8192;
#pragma unroll
    for (unsigned it = IT0; it < IT1; it++) {
      const unsigned st = it >> 2, k = it & 3, dm = st * slot_of(1) + k * 4096;
      unsigned ox, ow;
      asm volatile("s_add_u32 m0, %11, %15\n\ts_add_u32 %0, %9, %16\n\tbuffer_load_dwordx4 %5, %2, %0 offen lds\n\t"
                   "buffer_load_dwordx4 %6, %2, %0 offen offset:1024 lds\n\t"
                   "s_add_u32 m0, %12, %15\n\ts_add_u32 %1, %10, %17\n\tbuffer_load_dwordx4 %8, %3, %1 offen lds\n\t"
                   "s_add_u32 m0, %13, %15\n\ts_nop 0\n\tbuffer_load_dwordx4 %7, %4, %1 offen lds\n\t"
                   "s_add_u32 m0, %14, %15\n\ts_nop 0\n\tbuffer_load_dwordx4 %8, %4, %1 offen lds"
                   : "=&s"(ox), "=&s"(ow)
                   : "s"(gs.sx), "s"(rot.srd[0]), "s"(rot.srd[1]), "v"(bx0), "v"(bx1), "v"(bw0), "v"(bw1), "s"(ox0), "s"(ow0),
                     "s"(dx), "s"(d1), "s"(d2), "s"(d3), "i"(dm), "i"(st * 128 + k * 32768), "i"((st * 64 + k * 16) * 3584)
                   : "memory", "scc");
      if (it < 2) __builtin_amdgcn_s_sleep(2);
    }
}

constexpr int FEED_N[11] = {4, 4, 3, 4, 3, 3, 3, 3, 3, 3, 3};
constexpr int feed_done(int h) { int e = 12; for (int k = 0; k <= h && k < 11; k++) e += FEED_N[k]; return e; }
constexpr int feed_at(int h, int i) {
  if (h > 10) return -1;
  const int nn = FEED_N[h], e0 = feed_done(h) - nn;
  for (int k = 0; k < nn; k++) if ((nn == 4 ? 1 + 4 * k : 4 + 5 * k) == i) return e0 + k;
  return -1;
}
constexpr int feed_younger(int s) { return feed_done(2 * s) - 6 * (s + 2); }
constexpr bool feed_ok() {
  for (int s = 0; s < 6; s++) if (feed_younger(s) < 0 || feed_done(2 * s) > 6 * (s + 3)) return false;
  return feed_done(10) == 48 && FEED_N[0] <= 4;
}
static_assert(feed_ok(), "load schedule");
constexpr int wd_at(int h, int i) { for (int e = 0; e < 28; e++) if (11 + e / 6 == h && 1 + (e % 6) * 5 / 2 == i) return e; return -1; }

template <int ROLE>
DEV void gemm1(Ctx& c, const Params& P, G1Src gs, KIn kin, WdSrc& wds, f4 (&acc)[4][4], bf4 (&W1)[7][4][2], unsigned (&wa)[4], KExp& kx, u4 (&ur)[2], f4 (&cv)[4], f4 (&tc)[4]) {
  const int p = (c.xc & 3) * 4 + (c.jj & 3), ft = (c.xc >> 2) * 7 + (c.jj >> 2);
  const int lane = c.lane, n = c.n, q = c.q, v = c.v;
  gs.sx = mksrd(P.x, L * D * 2); gs.sg = mksrd(P.wg, D * F * 2); gs.su = mksrd(P.wu, D * F * 2);
  G1Rot rot = g1_rot(c, gs);
  set_m0(rot.dst[0], 0); asm volatile("s_nop 0"); glds_at<0>(rot.srd[0], rot.vo[0], rot.so[0]);
  if (ROLE == 0) touch(gs.sx, (lane < 10 ? lane >> 1 : 4) * 1024 + (lane & 1) * 128, (p * 128 + (c.jj >> 2) * 18 + 5 * v) * 1024 + gs.k0 * 128);
  set_m0(rot.dst[0], slot_of(1)); asm volatile("s_nop 0"); glds_at<0>(rot.srd[0], rot.vo[0], rot.so[1]);
  if (ROLE == 1) __builtin_amdgcn_s_setprio(1);
  SB();
  KPre kp;
  kgen_pre<ROLE>(c, kp);
  unsigned xa[2][2], wr[3][2];
  xa[0][0] = (((ROLE ^ v) & 1) * 64 + n) * 128 + ((q ^ (n >> 1)) * 16); xa[0][1] = xa[0][0] ^ 64;
  wr[0][0] = 16384 + (8 * q + (n >> 2)) * 256 + (((2 * v) ^ ((n >> 2) | ((q & 1) << 2))) * 32) + (n & 3) * 8; wr[0][1] = wr[0][0] ^ 32;
  unsigned kbr = LKB + (c.w & 1) * 8672 + n * 544 + q * 16;
  asm volatile("" : "+v"(xa[0][0]), "+v"(xa[0][1]), "+v"(wr[0][0]), "+v"(wr[0][1]), "+v"(kbr));
  unsigned lfv, cpv, l4, stv, csa, sta, one0, uv, csv, fco; u4 ones; f4 cs0;
  if (ROLE == 0) {
    if (threadIdx.x < 12) *LDSP(unsigned, LSY + threadIdx.x * 4) = 0u;
    ones = u4{0x3f803f80u, 0x3f803f80u, 0x3f803f80u, 0x3f803f80u}; pin(ones);
    cs0 = f4{0.f, 0.f, 0.f, 0.f}; pin(cs0);
  }
  kgen_take<ROLE, 3 - ROLE>(c, P, kin);
  bf8 KA[8], KB[8]; KTab kt;
  kgen_rows<ROLE>(c, kp, kin, KA, LAM(H) {});
  u2 tagv;
  U1 ga, gb;
  if (ROLE == 1 && v < 2) g1_fetch<0, 4>(c, gs, rot);
  {
#pragma unroll
    for (int k = 0; k < 2; k++) { wr[1][k] = wr[0][k] + slot_of(1); wr[2][k] = wr[0][k] + SLOT_C; xa[1][k] = xa[0][k] + SLOT_C; pin(wr[1][k]); pin(wr[2][k]); pin(xa[1][k]); }
    ga.v = q * 16 + (LCS + v * 256); gb.v = n * 16 + (LST + ((ROLE ^ v) & 1) * 1024); pin(ga.v); pin(gb.v);
    if (ROLE == 1) {
      ktab_pre<1>(c, kp);
      wds = wd_src(c, P);
#pragma unroll
      for (int d = 0; d < 4; d++) { wa[d] = (wd_raddr(v, n, q) ^ (d * 32)) + SLOT_C; pin(wa[d]); }
    }
  }
  if (ROLE == 1 && v < 2) { g1_fetch<4, 8>(c, gs, rot); waitv<20>(); } else waitv<1>();
  __builtin_amdgcn_s_setprio(1);
  bar();
  Frags F0, Fb;
  auto rd = [&](Frags& Fr, int i, int s, int ks) __attribute__((always_inline)) {
    rd_piece(Fr, i, wr[s % 3][0], wr[s % 3][1], xa[s % 3 == 2][ks] + (s % 3 == 1 ? slot_of(1) : 0), ks);
  };
#pragma unroll
  for (int i = 0; i < 12; i++) rd(F0, i, 0, 0);
  SB();
  f4 cs = cs0;
  auto colsum = [&](const Frags& Fr) __attribute__((always_inline)) {
    if (ROLE == 0) {
      mma_pick(cs, ones, cat(Fr.a[0][0], Fr.a[0][1]), cat(Fr.a[1][0], Fr.a[1][1]), cat(Fr.a[2][0], Fr.a[2][1]), cat(Fr.a[3][0], Fr.a[3][1]), c.jj);
    }
    SB();
  };
  u2 lf; u4 cpr; unsigned str; F1 cs4[4], tk[4], tr;
  auto feed = [&](auto H, auto I) __attribute__((always_inline)) {
    constexpr int h = CV(H), i = CV(I);
    constexpr int e = feed_at(h, i), en = feed_at(h, i + 1);
    if constexpr (e >= 0) g1_ld(gs, e / 6, e % 6);
    if constexpr (en >= 0 && !(en & 1)) g1_m0(c, en / 6, en % 6);
    constexpr int d = ROLE ? wd_at(h, i) : -1, dn = ROLE ? wd_at(h, i + 1) : -1;
    if constexpr (d >= 0) wd_ld(wds, d >> 2, d & 3);
    if constexpr (dn >= 0 && !(dn & 3)) set_m0(v * 4096, wd_base(dn >> 2));
  };
  sfor<0, 8>(LAM(S) {
    constexpr int s = CV(S);
    sfor<0, 16>(LAM(I) {
      constexpr int i = CV(I);
      if constexpr (s == 0) mma0(acc[i >> 2][i & 3], cat(F0.a[i & 3][0], F0.a[i & 3][1]), F0.b[i >> 2]);
      else mma(acc[i >> 2][i & 3], cat(F0.a[i & 3][0], F0.a[i & 3][1]), F0.b[i >> 2]);
      if constexpr (s == 0) {
        if constexpr (i < 12) rd(Fb, i, s, 1);
        if constexpr (i >= 6 && i < 14) KB[i - 6] = *LDSP(bf8, kbr + (i - 6) * 64);
      } else if constexpr (i != 4 && i != 9 && i < 14) rd(Fb, i - (i > 4) - (i > 9), s, 1);
      feed(IC<2 * s>{}, I);
      if constexpr (ROLE == 0 && s == 3) kexp_op<16 + i>(c, kx);
      if constexpr (ROLE == 0 && s == 2 && i == 15) {
        pin(cs);
        __builtin_amdgcn_raw_buffer_store_b32(__float_as_uint(cs[0]), c.ws, csv, WS_CPART + (ft * 4 + v) * 1024 + p * 64, SC1);
      }
      if constexpr (ROLE == 0) {
        if constexpr (s == 5 && i == 15) lf = flag_sample(P, lfv, 0);
        if constexpr (s == 6 && i == 4) cpr = __builtin_amdgcn_raw_buffer_load_b128(c.ws, cpv, WS_CPART + (ft * 4 + v) * 1024, SC1);
        if constexpr (s == 7) {
          if constexpr (i == 1) { cs4[0].v = swap32_add(__uint_as_float(cpr[0]), __uint_as_float(cpr[1])); pin(cs4[0].v); }
          if constexpr (i == 4) { cs4[2].v = swap32_add(__uint_as_float(cpr[2]), __uint_as_float(cpr[3])); pin(cs4[2].v); }
          if constexpr (i == 7) { cs4[0].v = swap16_add(cs4[0].v, cs4[2].v); pin(cs4[0].v); }
          if constexpr (i == 7) *LDSP(float, csa) = cs4[0].v;
          if constexpr (i == 8) *LDSP(f4, sta) = f4{tk[0].v, tk[1].v, tk[2].v, tk[3].v};
        }
        if constexpr (s == 6 && i == 2) str = __builtin_amdgcn_raw_buffer_load_b32(c.ws, stv, WS_STATS + (p * 128 + 32 * v) * 4, SC1);
      }
      SB();
    });
    if constexpr (s < 2) colsum(F0);
    if constexpr (ROLE == 0 && s < 5) {
      int ln = lane; pin(ln);
      Ctx d = c; d.lane = ln; d.n = ln & 15; d.q = ln >> 4;
      __builtin_amdgcn_s_setprio(0);
      if constexpr (s == 0) { ktab_pre<0>(d, kp); ktab_skip(d, kp, kt, kin.dsk); l4 = ln * 4; csv = ln < 16 ? l4 : 0x7ffffff0u; pin(l4); pin(csv); }
      if constexpr (s == 1) kexp_pre<0>(d, kx);
      if constexpr (s == 2) kexp_pre<1>(d, kx);
      if constexpr (s == 3) {
        tagv = c.tag; one0 = flag_off(ln, CSZ); pin(tagv); pin(one0);
        unsigned f0 = WS_FC + (v * 14 + ft) * 16 * CSZ, f1 = WS_FLN + (8 * v - 16) * LSZ, f2 = WS_FLN + (2 * p + (v >> 1)) * LSZ;
        fco = f0 + p * CSZ; pins(f0); pins(f1); pins(f2); pins(fco);
        unsigned a0 = ln * CSZ + f0, a1 = ln * LSZ + f1; pin(a0); pin(a1);
        lfv = ln < 16 ? a0 : ln < 24 ? a1 : f2; pin(lfv);
      }
      if constexpr (s == 4) {
        cpv = ln * 16; stv = l4 & 124; sta = LST + 512 * v + 4 * stv;
        csa = LCS + v * 256 + d.n * 16 + (((d.q & 1) << 1) | (d.q >> 1)) * 4; uv = u_off(d);
        pin(cpv); pin(stv); pin(sta); pin(csa); pin(uv);
      }
      __builtin_amdgcn_s_setprio(1);
      SB();
    }
    if constexpr (s == 3 && ROLE == 0) waitv<(feed_younger(s) < FEED_N[5] + FEED_N[6] ? feed_younger(s) : FEED_N[5] + FEED_N[6])>();
    else if constexpr (s < 6) waitv<feed_younger(s) + (ROLE == 0 && (s == 2 || s == 5))>();
    else if constexpr (ROLE == 1) waitv<12>();
    bar();
    SB();
    sfor<0, 16>(LAM(I) {
      constexpr int i = CV(I);
      mma(acc[i >> 2][i & 3], cat(Fb.a[i & 3][0], Fb.a[i & 3][1]), Fb.b[i >> 2]);
      if constexpr (s < 7 && i != 4 && i != 9 && i < 14) rd(F0, i - (i > 4) - (i > 9), s + 1, 0);
      feed(IC<2 * s + 1>{}, I);
      if constexpr (s == 0 && i == 0) mma0(kt.t, KA[0], KB[0]);
      if constexpr (s == 0 && i > 0 && !(i & 1)) mma(kt.t, KA[i >> 1], KB[i >> 1]);
      if constexpr (s == 1 && i < KT_OPS) ktab_op<ROLE, i>(c, kp, kt);
      if constexpr (ROLE == 0 && s == 3 && i == 0) __builtin_amdgcn_raw_buffer_store_b64(tagv, c.ws, one0, fco, SC1);
      if constexpr (ROLE == 0 && (s == 2 || s == 3) && 32 * (s - 2) + i < KX_OPS) kexp_op<32 * (s - 2) + i>(c, kx);
      if constexpr (s == 7 && (i & 1)) { if constexpr (i & 2) tc[i >> 2] = *LDSP(f4, gb.v + (i >> 2) * 256); else cv[i >> 2] = *LDSP(f4, ga.v + (i >> 2) * 64); }
      if constexpr (ROLE == 1 && s == 7) W1[i >> 3][(i >> 1) & 3][i & 1] = trd(wa[(i >> 1) & 3] + (i >> 3) * 16384 + (i & 1) * 2048);
      if constexpr (ROLE == 0) {
        if constexpr (s == 5 && i == 15) {
          asm volatile("s_waitcnt vmcnt(0)" : "+v"(lf)::"memory");
          if (!flags_up(c.tag, lf)) wait_flags<4>(c.ws, c.tag, ld_flag(c.ws, lfv, 0), lfv, 0);
          asm volatile("" ::: "memory");
        }
        if constexpr (s == 7 && (i == 0 || i == 2)) ur[i >> 1] = u_load(c, uv, i >> 1);
        if constexpr (s == 6 && i < 8) {
          if constexpr (i == 0) { tk[3].v = bhi(str); pin(tk[3].v); }
          if constexpr (i == 1) { tr.v = __builtin_amdgcn_rcpf(tk[3].v); pin(tr.v); }
          if constexpr (i == 2) { tk[0].v = blo(str); pin(tk[0].v); }
          if constexpr (i == 3) { tk[1].v = (-2.f * 0.7978845608028654f * 1.4426950408889634f) * tr.v; pin(tk[1].v); }
          if constexpr (i == 4) { tr.v *= tr.v; pin(tr.v); }
          if constexpr (i == 5) { tk[3].v *= tk[3].v; pin(tk[3].v); }
          if constexpr (i == 6) { tr.v *= 0.044715f; pin(tr.v); }
          if constexpr (i == 7) { tk[2].v = tr.v * tk[1].v; pin(tk[2].v); }
        }
      }
      SB();
    });
    if constexpr (s < 2) colsum(Fb);
  });
  __builtin_amdgcn_s_setprio(0);
}

template <int ROLE>
DEV void geglu(const Ctx& c, f4 (&acc)[4][4], const f4 (&cv)[4], const f4 (&tc)[4]) {
  const int p = (c.xc & 3) * 4 + (c.jj & 3), ft = (c.xc >> 2) * 7 + (c.jj >> 2);
  const unsigned hbase = WS_H + uni((2 * p + ((ROLE ^ c.v) & 1)) * 56 + 4 * ft + c.v) * 4096;
#pragma unroll
  for (int i = 0; i < 16; i++) pin(acc[i >> 2][i & 3]);
#pragma unroll
  for (int tt = 0; tt < 4; tt++) {
    const f2 nm = -f2{tc[tt][0], tc[tt][0]}, k0 = {tc[tt][1], tc[tt][1]}, k1 = {tc[tt][2], tc[tt][2]}, s2 = {tc[tt][3], tc[tt][3]};
    f2 a[4], b[4], z[4];
    auto half = [](const f4& x, int e) { return f2{x[2 * e], x[2 * e + 1]}; };
#define STEP(X) _Pragma("unroll") for (int j = 0; j < 4; j++) { X; } SB();
    STEP(a[j] = fma2(nm, half(cv[j >> 1], j & 1), half(acc[tt][j >> 1], j & 1)))
    STEP(z[j] = a[j] * a[j])
    STEP(z[j] = fma2(z[j], k1, k0))
    STEP(z[j] *= a[j])
    STEP(z[j] = (f2{__builtin_amdgcn_exp2f(z[j].x), __builtin_amdgcn_exp2f(z[j].y)}))
    STEP(b[j] = fma2(nm, half(cv[2 + (j >> 1)], j & 1), half(acc[tt][2 + (j >> 1)], j & 1)))
    STEP(z[j] = fma2(z[j], s2, s2))
    STEP(z[j] = (f2{__builtin_amdgcn_rcpf(z[j].x), __builtin_amdgcn_rcpf(z[j].y)}))
    STEP(a[j] *= b[j])
    STEP(a[j] *= z[j])
#undef STEP
    st128(u4{pk(a[0]), pk(a[1]), pk(a[2]), pk(a[3])}, c.ws, c.lane * 16 + tt * 1024, hbase);
    SB();
  }
}
template <int ROLE, int YOUNGER> DEV void h_publish(const Ctx& c) {
  const int p = (c.xc & 3) * 4 + (c.jj & 3), ft = (c.xc >> 2) * 7 + (c.jj >> 2);
  waitv<YOUNGER>();
  st_flag(c.ws, c.tag, WS_FH + uni((2 * p + ((ROLE ^ c.v) & 1)) * 56 + 28 * ROLE + 2 * ft + (c.v >> 1)) * HSZ, c.lane, HSZ);
}

DEV void ln_block(Ctx& c, const Params& P, KIn kin) {
  const int tok0 = (c.xc & 3) * 512 + (c.xc >> 2) * 256 + (c.jj - 28) * 64;
  const i4 sx = mksrd(P.x, L * D * 2);
  u4 rows[8];
#pragma unroll
  for (int i = 0; i < 8; i++) rows[i] = row_load(sx, c.lane * 16, (tok0 + 32 * (i >> 2) + 4 * c.w + (i & 3)) * 1024);
  if (threadIdx.x < 12) *LDSP(unsigned, LSY + threadIdx.x * 4) = 0u;
  kgen_take<0, 8>(c, P, kin);
  asm volatile("s_waitcnt vmcnt(4)" : "+v"(rows[0]), "+v"(rows[1]), "+v"(rows[2]), "+v"(rows[3])::"memory");
  ln_compute(c, rows, 0, tok0);
  asm volatile("s_waitcnt vmcnt(0)" : "+v"(rows[4]), "+v"(rows[5]), "+v"(rows[6]), "+v"(rows[7])::"memory");
  bar();
  ln_store(c, 0, tok0);
  ln_compute(c, rows + 4, 34816, tok0 + 32);
  bar();
  ln_store(c, 34816, tok0 + 32);
  waitv<0>();
  bar();
  if (c.w == 0) st_flag(c.ws, c.tag, WS_FLN + (tok0 >> 6) * LSZ, c.lane, LSZ);
  constexpr int NW = 4;
  const i4 swd = mksrd(P.wd, F * D * 2);
  const unsigned wq = (c.jj - 28) * 448 * 1024;
  bf8 KA[8], KB[8]; KTab kt;
  KPre kp;
  auto warm = LAM(H) {
    constexpr int h = CV(H);
    if constexpr (h == 0) { if (c.w >= 4 && c.v < 7 - NW) touch(swd, c.lane * 128, wq + NW * 65536 + (c.xc * (7 - NW) + c.v) * 8192); }
    if constexpr (h < NW) {
      if ((c.w >> 2) == (h & 1)) { unsigned k = h + (TILE_G(c) * NW >> 2); k = k >= NW ? k - NW : k;
        touch(swd, c.lane * 128, wq + (1 - TILE_P(c)) * 32768 + k * 65536 + c.v * 8192); }
    }
  };
  if (c.w < 4) { kgen_pre<0>(c, kp); kgen_rows<0>(c, kp, kin, KA, warm); } else { kgen_pre<1>(c, kp); kgen_rows<1>(c, kp, kin, KA, warm); }
  bar();
  kgen_cols(c, KB); kgen_mma(kt.t, KA, KB); mma_fence();
  if (c.w < 4) { ktab_pre<0>(c, kp); ktab_skip(c, kp, kt, kin.dsk); sfor<0, KT_OPS>(LAM(K) { ktab_op<0, CV(K)>(c, kp, kt); }); }
  else { ktab_pre<1>(c, kp); sfor<0, KT_OPS>(LAM(K) { ktab_op<1, CV(K)>(c, kp, kt); }); }
  bar();
  asm volatile("fused_end_a:");
}

struct CFr { bf4 a[8][2]; bf8 b[8]; };
DEV void conv_rd(CFr& Fr, int i, unsigned ka, unsigned ua, int sp) {
  if (i < 2) Fr.a[0][i] = trd(ka + 64 * sp + (i ? 0 : 8));
  else if (i < 10) Fr.b[i - 2] = *LDSP(bf8, ua + 512 * (i - 2) + 192 - 64 * sp);
  else { const int g = (i - 8) >> 1, t = i & 1; Fr.a[g][t] = trd(ka + 512 * g + 64 * sp + (t ? 0 : 8)); }
}
DEV void conv(Ctx& c) {
  const int chl = c.v & 1, kh = c.v >> 1, n = c.n, q = c.q;
  unsigned ka = LKC + chl * KCH + (n >> 2) * KCS + 2 * (16 + 4 * (n & 3) + 128 * kh + 16 - 8 * q) - 8;
  unsigned ua = LU + chl * UCH + 2 * (256 + 16 * n - 16 - 128 * kh + 8 * q) - 192;
  asm volatile("" : "+v"(ka), "+v"(ua));
  f4 acc[8];
  CFr Fr[2];
#pragma unroll
  for (int i = 0; i < 24; i++) conv_rd(Fr[0], i, ka, ua, 0);
  unsigned xw = LKC + chl * KCH + 32 + (c.lane >> 3) * 512 + (c.lane & 7) * 16, yo = (q & 1) * 512 + n * 32 + (q >> 1) * 16;
  asm volatile("" : "+v"(xw), "+v"(yo));
  lds_wait<1>(LSY + 44, 4);
  if (c.jj < 4 && c.w == 0) st_flag(c.ws, c.tag, WS_FG + (c.xc * 4 + c.jj) * 128, c.lane);
  SB();
#pragma unroll
  for (int sp = 0; sp < 4; sp++) {
    int i = 0;
#pragma unroll
    for (int g = 0; g < 8; g++)
#pragma unroll
      for (int m = 0; m + g < 8; m++) {
        if (sp == 0 && g == 0) mma0(acc[m], cat(Fr[0].a[0][0], Fr[0].a[0][1]), Fr[0].b[m]);
        else mma(acc[g + m], cat(Fr[sp & 1].a[g][0], Fr[sp & 1].a[g][1]), Fr[sp & 1].b[m]);
        if (sp < 3 && i < 24) conv_rd(Fr[(sp + 1) & 1], i, ka, ua, sp + 1);
        i++;
        SB();
      }
  }
  mma_fence();
#pragma unroll
  for (int i = 0; i < 8; i++) asm volatile("" : "+v"(acc[i]));
  unsigned pkd[4][2];
  auto finish = [&](auto KH) __attribute__((always_inline)) {
    constexpr int h = CV(KH);
#pragma unroll
    for (int j = 0; j < 4; j++) *LDSP(f4, xw + h * 256 + j * KCS) = acc[4 * (1 - h) + j];
    lds_post(LSY + 16 + c.v * 4, c.lane);
    lds_wait<0>(LSY + 16 + (c.v ^ 2) * 4, 1);
#pragma unroll
    for (int j = 0; j < 4; j++) { const f4 t = acc[4 * h + j] + *LDSP(f4, xw + (1 - h) * 256 + j * KCS); pkd[j][0] = pk(f2{t[0], t[1]}); pkd[j][1] = pk(f2{t[2], t[3]}); }
  };
  if (kh) finish(IC<1>{}); else finish(IC<0>{});
  const int ch = 2 * (c.xc * 32 + c.jj) + chl;
#pragma unroll
  for (int j = 0; j < 4; j += 2) {
    swap16(pkd[j][0], pkd[j + 1][0]); swap16(pkd[j][1], pkd[j + 1][1]);
    st128(u4{pkd[j][0], pkd[j][1], pkd[j + 1][0], pkd[j + 1][1]}, c.ws,
                                           yo + j * 512, WS_YT + ch * 4096 + kh * 2048);
  }
  waitv<0>();
  st_flag(c.ws, c.tag, WS_FY + (kh * 512 + ch) * YS, c.lane, YS);
}
DEV u4 x_load(const Ctx& c, const Params& P, int role, unsigned oa) {
  return __builtin_amdgcn_raw_buffer_load_b128(mkrs(P.x, L * D * 2), oa, (TILE_T(c) * 64 + 16 * c.v + 8 * role) * 1024 + TILE_C(c) * 128, 0);
}
DEV void y_gather(Ctx& c) {
  const int T64 = TILE_T(c), ct = TILE_C(c), l = c.lane;
  const int r0 = 32 * (c.v & 1) + 16 * (c.v >> 1);
  const unsigned fo = WS_FY + ((T64 >> 4) * 512 + 64 * ct + r0) * YS;
  unsigned fv = (l < 16 ? l : 15) * YS, yv = (l >> 3) * 4096 + (l & 7) * 16, ya = LU + (c.v & 1) * (UCH + 64) + (c.v >> 1) * 2080 + l * 16;
  asm volatile("" : "+v"(fv), "+v"(yv), "+v"(ya));
  lds_wait<2>(LSY + 40, 1);
  wait_flags<8>(c.ws, c.tag, ld_flag(c.ws, fv, fo), fv, fo);
#pragma unroll
  for (int i = 0; i < 2; i++)
    *LDSP(u4, ya + i * 1040) = __builtin_amdgcn_raw_buffer_load_b128(c.ws, yv, WS_YT + (64 * ct + r0 + 8 * i) * 4096 + T64 * 128, SC1);
}
struct Fin { unsigned a0, a1, ya, oa; F1 s[8], y[8]; };
template <int ROLE> DEV void finish_addr(const Ctx& c, Fin& f) {
  const int t = c.lane >> 3, c8 = c.lane & 7;
  f.a0 = c.v * 16384 + (8 * ROLE + t) * 256 + ((2 * c8) ^ t) * 16;
  f.ya = LU + (c8 >> 2) * (UCH + 64) + (c8 & 3) * 1040 + (16 * c.v + 8 * ROLE + t) * 2;
  f.oa = t * 1024 + c8 * 16;
  pin(f.a0); pin(f.ya); pin(f.oa);
}
template <int k> DEV void finish_op(Fin& f, const u4& x) {
  if constexpr (k < 8) { f.s[k].v = (k & 1) ? bhi(x[k >> 1]) : blo(x[k >> 1]); pin(f.s[k].v); }
  else if constexpr (k < 16) asm volatile("v_mov_b32 %0, 0" : "=v"(f.y[k - 8].v));
  else { f.a1 = f.a0 ^ 16; pin(f.a1); }
}
template <int ROLE> DEV void finish_tile(Ctx& c, const Params& P, Fin f) {
  const int T64 = TILE_T(c), ct = TILE_C(c);
  const rsrc_t ro = mkrs(P.out, L * D * 2);
  bar();
  float y[8] = {f.y[0].v, f.y[1].v, f.y[2].v, f.y[3].v, f.y[4].v, f.y[5].v, f.y[6].v, f.y[7].v};
  f4 sa = {f.s[0].v, f.s[1].v, f.s[2].v, f.s[3].v}, sb = {f.s[4].v, f.s[5].v, f.s[6].v, f.s[7].v};
  asm volatile("ds_read_u16_d16_hi %0, %8\n\tds_read_u16_d16_hi %1, %8 offset:128\n\tds_read_u16_d16_hi %2, %8 offset:256\n\tds_read_u16_d16_hi %3, %8 offset:384\n\t"
               "ds_read_u16_d16_hi %4, %8 offset:512\n\tds_read_u16_d16_hi %5, %8 offset:640\n\tds_read_u16_d16_hi %6, %8 offset:768\n\tds_read_u16_d16_hi %7, %8 offset:896"
               : "+v"(y[0]), "+v"(y[1]), "+v"(y[2]), "+v"(y[3]), "+v"(y[4]), "+v"(y[5]), "+v"(y[6]), "+v"(y[7]) : "v"(f.ya) : "memory");
  f4 pa[4], pb[4];
#pragma unroll
  for (int k = 0; k < 4; k++) { pa[k] = *LDSP(f4, f.a0 + k * 4096); pb[k] = *LDSP(f4, f.a1 + k * 4096); }
  SB();
#pragma unroll
  for (int k = 0; k < 4; k++) { sa += pa[k]; sb += pb[k]; }
  asm volatile("s_waitcnt lgkmcnt(0)" : "+v"(y[0]), "+v"(y[1]), "+v"(y[2]), "+v"(y[3]), "+v"(y[4]), "+v"(y[5]), "+v"(y[6]), "+v"(y[7])::"memory");
  sa += f4{y[0], y[1], y[2], y[3]}; sb += f4{y[4], y[5], y[6], y[7]};
  st128(u4{pk(f2{sa[0], sa[1]}), pk(f2{sa[2], sa[3]}), pk(f2{sb[0], sb[1]}), pk(f2{sb[2], sb[3]})}, ro, f.oa,
                                         (T64 * 64 + 16 * c.v + 8 * ROLE) * 1024 + ct * 128);
}

DEV void gemm2(Ctx& c, const Params& P, const bf4 (&W1)[7][4][2], u2 hfl, unsigned (&wa)[4], Fin& fin, unsigned pw0) {
  const int T64 = TILE_T(c), v = c.v;
  unsigned l16 = c.lane * 16;
  asm volatile("" : "+v"(l16));
  const unsigned hk = uni(T64 * 56 + 14 * v + TILE_P(c));
  const unsigned fo = WS_FH + uni(T64 * 56 + 7 * v) * HSZ, fo2 = fo + 28 * HSZ, fv = (l16 < 112 ? l16 : 96) * (HSZ / 16);
  if (!flags_up(c.tag, hfl)) wait_flags<2>(c.ws, c.tag, ld_flag(c.ws, fv, fo), fv, fo);
  asm volatile("" ::: "memory");
  constexpr int WIN = 2;
  u4 H[14][4];
  auto hload = [&](int i, int t) __attribute__((always_inline)) {
    H[i][t] = __builtin_amdgcn_raw_buffer_load_b128(c.ws, l16 + t * 1024, WS_H + ((i < 7 ? hk : hk ^ 1) + 2 * (i % 7)) * 4096, SC1);
  };
#pragma unroll
  for (int i = 0; i < WIN; i++)
#pragma unroll
    for (int t = 0; t < 4; t++) hload(i, t);
  SB();
  f4 acc[4][4];
  bf4 W2[7][4][2];
  unsigned pw[4]; u4 xr;
  sfor<0, 14>(LAM(I) {
    constexpr int i = CV(I);
    sfor<0, 16>(LAM(J) {
      constexpr int j = CV(J), t = j >> 2, d = j & 3;
      if constexpr (i == 0) mma0(acc[t][d], cat(W1[0][d][0], W1[0][d][1]), H[0][t]);
      else if constexpr (i < 7) mma(acc[t][d], cat(W1[i][d][0], W1[i][d][1]), H[i][t]);
      else mma(acc[t][d], cat(W2[i - 7][d][0], W2[i - 7][d][1]), H[i][t]);
      if constexpr (i == 2 && j == 1) hfl = ld_flag(c.ws, fv, fo2);
      if constexpr (i == 7 - WIN && j == 2) { if (!flags_up(c.tag, hfl)) wait_flags<2>(c.ws, c.tag, ld_flag(c.ws, fv, fo2), fv, fo2); asm volatile("" ::: "memory"); }
      if constexpr (i + WIN < 14 && (j & 3) == 3) hload(i + WIN, t);
      if constexpr (i >= 6 && i < 13 && (j & 1) == 0) W2[i - 6][j >> 2][(j >> 1) & 1] = trd(wa[j >> 2] + (i < 9 ? i - 6 : i - 9) * 16384 + ((j >> 1) & 1) * 2048);
      if constexpr (i == 11 && j == 14) *LDSP(volatile unsigned, LSY + 40) = 1u;
      if constexpr (i == 5 && (j & 3) == 1) { wa[t] += SLOT_C; pin(wa[t]); }
      if constexpr ((i == 8 && (j & 3) == 3)) { wa[t] -= SLOT_C; pin(wa[t]); }
      if constexpr (i == 6 && j == 1) xr = x_load(c, P, 1, fin.oa);
      if constexpr (i >= 10 && (j & 1) && 8 * (i - 10) + (j >> 1) < 17) finish_op<8 * (i - 10) + (j >> 1)>(fin, xr);
      if constexpr (i == 12 && (j & 3) == 3) { pw[t] = pw0 ^ (t * 64); pin(pw[t]); }
      if constexpr (i == 13 && j >= 8) { pin(acc[t - 2][d]); *LDSP(f4, pw[d] + (t - 2) * 16384) = acc[t - 2][d]; }
      SB();
    });
  });
  asm volatile("s_nop 1");
#pragma unroll
  for (int j = 8; j < 16; j++) { pin(acc[j >> 2][j & 3]); *LDSP(f4, pw[j & 3] + (j >> 2) * 16384) = acc[j >> 2][j & 3]; if (j == 11) asm volatile("s_nop 3"); SB(); }
  finish_tile<1>(c, P, fin);
}

__global__ void __launch_bounds__(512, 2) fused_s4_geglu(Params P) {
  Ctx c;
  c.w = __builtin_amdgcn_readfirstlane(threadIdx.x >> 6);
  c.v = c.w & 3; c.lane = threadIdx.x & 63; c.n = c.lane & 15; c.q = c.lane >> 4;
  c.xc = blockIdx.x & 7; c.jj = blockIdx.x >> 3;
  {
    u64 pc; unsigned len;
    asm volatile("s_getpc_b64 %0\n.Lpc%=:\n\ts_mov_b32 %1, fused_end_a-.Lpc%=\n\ts_max_u32 %1, %1, fused_end_b-.Lpc%=" : "=s"(pc), "=s"(len)::"scc");
    if (c.w == 3) touch(mksrd((const void*)pc, len), (c.lane >> 2) * 128, (c.jj & 31) * 2048);
  }
  const bool g1 = c.jj < 28;
  const G1Src gs = g1_src(c);
  const KIn kin = kgen_load<0>(c, P);
  if (__builtin_expect(!g1, 0)) ln_block(c, P, kin);
  if (c.w >= 4) {
    WdSrc wds;
    f4 acc[4][4], cv[4], tc[4]; bf4 W1[7][4][2]; unsigned wa[4];
    if (__builtin_expect(g1, 1)) { KExp kx; u4 ur[2]; gemm1<1>(c, P, gs, kin, wds, acc, W1, wa, kx, ur, cv, tc); }
    else {
      wds = wd_src(c, P);
      lds_arrive(LSY + 44, c.lane);
      lds_wait<2>(LSY + 8, 4);
#pragma unroll
      for (int i = 0; i < 7; i++) wd_chunk(c, wds, i);
#pragma unroll
      for (int d = 0; d < 4; d++) { wa[d] = (wd_raddr(c.v, c.n, c.q) ^ (d * 32)) + SLOT_C; pin(wa[d]); }
      waitv<16>();
#pragma unroll
      for (int i = 0; i < 16; i++) W1[i >> 3][(i >> 1) & 3][i & 1] = trd(wa[(i >> 1) & 3] + (i >> 3) * 16384 + (i & 1) * 2048);
      waitl();
    }
    auto w1_read = [&](int i) __attribute__((always_inline)) {
      if (i == 3) {
#pragma unroll
        for (int d = 0; d < 4; d++) { wa[d] -= SLOT_C; pin(wa[d]); }
      }
#pragma unroll
      for (int k = 0; k < 8; k++) W1[i][k >> 1][k & 1] = trd(wa[k >> 1] + (i < 3 ? i : i - 3) * 16384 + (k & 1) * 2048);
      SB();
    };
    if (g1) { geglu<1>(c, acc, cv, tc); waitv<4>(); } else waitv<0>();
    lds_arrive(LSY + 44, c.lane);
    u2 hfl; Fin fin; unsigned pw0;
    const unsigned go = WS_FG + (c.xc * 4 + (c.jj & 3)) * 128;
#pragma unroll
    for (int i = 0; i < 7; i++) {
      if (i + 2 < 7) w1_read(i + 2);
      if (i >= 2) { if (i < 5) asm volatile("s_waitcnt lgkmcnt(15)" ::: "memory"); else if (i == 5) asm volatile("s_waitcnt lgkmcnt(8)" ::: "memory"); else waitl(); }
      if (i == 5) hfl = flag_sample(P, g1 ? (TILE_T(c) * 56 + 7 * c.v + (c.lane < 7 ? c.lane : 6)) * HSZ + WS_FH : go, 0);
      wd_chunk(c, wds, 7 + i);
      if (i == 1) {
        finish_addr<1>(c, fin); pw0 = c.v * 4096 + c.n * 256 + (c.q ^ (c.n & 7)) * 16; pin(pw0);
        if (g1) h_publish<1, 8>(c);
      }
      SB();
    }
    asm volatile("s_waitcnt vmcnt(8)" : "+v"(hfl)::"memory");
    if (!g1) {
      if (!flags_up(c.tag, hfl)) { wait_flags<4>(c.ws, c.tag, hfl, 0, go); __builtin_amdgcn_s_sleep(8); }
      hfl = ~c.tag;
    }
    gemm2(c, P, W1, hfl, wa, fin, pw0);
    asm volatile("fused_end_b:");
  } else {
    if (__builtin_expect(g1, 1)) {
      f4 acc[4][4], cv[4], tc[4]; WdSrc none; bf4 W1[7][4][2]; unsigned wa[4]; KExp kx; u4 ur[2];
      gemm1<0>(c, P, gs, kin, none, acc, W1, wa, kx, ur, cv, tc);
      geglu<0>(c, acc, cv, tc);
      sfor<0, 9>(LAM(K) { kexp_put<CV(K)>(c, kx); });
      u_write(c, ur[0], ur[1]);
    } else {
      const unsigned fv = (8 * c.v + (c.lane >> 3)) * LSZ;
      const u2 fl = ld_flag(c.ws, fv, WS_FLN);
      KExp kx;
      kexp_pre<0>(c, kx); kexp_pre<1>(c, kx);
      sfor<0, KX_OPS>(LAM(K) { kexp_op<CV(K)>(c, kx); });
      wait_flags<8>(c.ws, c.tag, fl, fv, WS_FLN);
      const u4 r0 = u_load(c, u_off(c), 0), r1 = u_load(c, u_off(c), 1);
      sfor<0, 9>(LAM(K) { kexp_put<CV(K)>(c, kx); });
      lds_arrive(LSY + 8, c.lane);
      u_write(c, r0, r1);
    }
    lds_arrive(LSY, c.lane);
    if (g1) h_publish<0, 0>(c);
    lds_wait<1>(LSY, 4);
    Fin fin;
    finish_addr<0>(c, fin);
    const u4 xr = x_load(c, P, 0, fin.oa);
    conv(c);
    if (__builtin_expect(blockIdx.x == 224 && c.w == 0, 0)) {
#pragma unroll 1
      for (int i = 0; i < 4; i++) {
        const int b = 64 * i + c.lane;
        const unsigned fv = b < 224 ? WS_FC + b * CSZ : WS_FLN + (b - 224) * LSZ;
        wait_flags<16>(c.ws, c.tag, ld_flag(c.ws, fv, 0), fv, 0);
      }
      st_flag(c.ws, c.tag, WS_EPOCH, c.lane);
    }
    sfor<0, 17>(LAM(K) { finish_op<CV(K)>(fin, xr); });
    y_gather(c);
    finish_tile<0>(c, P, fin);
  }
}

extern "C" void kernel_launch(void* const* d_in, const int* in_sizes, int n_in, void* d_out, int out_size, void* d_ws, size_t ws_size,
                              hipStream_t stream) {
  Params P;
  P.x = (const u16*)d_in[0]; P.are = (const u16*)d_in[1]; P.aim = (const u16*)d_in[2]; P.bre = (const u16*)d_in[3];
  P.bim = (const u16*)d_in[4]; P.cre = (const u16*)d_in[5]; P.cim = (const u16*)d_in[6]; P.ldt = (const u16*)d_in[7];
  P.dsk = (const u16*)d_in[8]; P.wg = (const u16*)d_in[10]; P.wu = (const u16*)d_in[11]; P.wd = (const u16*)d_in[12];
  P.out = (u16*)d_out; P.ws = (char*)d_ws;
  (void)hipFuncSetAttribute((const void*)fused_s4_geglu, hipFuncAttributeMaxDynamicSharedMemorySize, LDS_BYTES);
  fused_s4_geglu<<<256, 512, LDS_BYTES, stream>>>(P);
}
```

```cpp
#include <hip/hip_runtime.h>
#include <hip/hip_bf16.h>
#include <stdint.h>
#pragma clang diagnostic ignored "-Wint-to-pointer-cast"

typedef unsigned long long u64;
typedef unsigned short u16;
typedef __bf16 bf2 __attribute__((ext_vector_type(2)));
typedef __bf16 bf4 __attribute__((ext_vector_type(4)));
typedef __bf16 bf8 __attribute__((ext_vector_type(8)));
typedef float f2 __attribute__((ext_vector_type(2)));
typedef float f4 __attribute__((ext_vector_type(4)));
typedef int i4 __attribute__((ext_vector_type(4)));
typedef unsigned u2 __attribute__((ext_vector_type(2)));
typedef unsigned u4 __attribute__((ext_vector_type(4)));
typedef __amdgpu_buffer_rsrc_t rsrc_t;
#define LDSP(T, a) ((__attribute__((address_space(3))) T*)(a))
#define DEV __device__ __forceinline__
#define SB() __builtin_amdgcn_sched_barrier(0)
#define SC1 16

template <int V> struct IC { static constexpr int value = V; };
template <int B, int E, class Fn> DEV void sfor(Fn&& f) { if constexpr (B < E) { f(IC<B>{}); sfor<B + 1, E>(f); } }
#define LAM(I) [&](auto I) __attribute__((always_inline))
#define CV(I) decltype(I)::value

constexpr int L = 2048, D = 512, F = 1792;

constexpr int YS = 64, HSZ = 64, CSZ = 32, LSZ = 32;
constexpr unsigned WS_EPOCH = 0;
constexpr unsigned WS_FLN = 8192;
constexpr unsigned WS_FG = 4096;
constexpr unsigned WS_STATS = 16384;
constexpr unsigned WS_CPART = 65536;
constexpr unsigned WS_FC = 327680;
constexpr unsigned WS_FH = 524288;
constexpr unsigned WS_FY = 786432;
constexpr unsigned WS_XNT = 1u << 20;
constexpr unsigned WS_YT = 3u << 20;
constexpr unsigned WS_H = 5u << 20;
constexpr unsigned WS_BYTES = WS_H + 32u * 56u * 4096u;

constexpr unsigned SLOT_C = 98816;
constexpr unsigned slot_of(int s) { return s % 3 == 0 ? 0u : s % 3 == 1 ? 49152u : SLOT_C; }
constexpr unsigned wd_base(int i) { return i < 3 ? SLOT_C + i * 16384 : (i - 3) * 16384; }
constexpr unsigned LKC = 65536;
constexpr unsigned KCS = 4160, KCH = 4 * KCS;
constexpr unsigned LKG = 98304;
constexpr unsigned LU = 147968;
constexpr unsigned UCH = 4608;
constexpr unsigned LCS = LU + 9216;
constexpr unsigned LST = LU + 10240;
constexpr unsigned LKT = 160064;
constexpr unsigned LSY = 163136;
constexpr unsigned LJK = 163328;
constexpr unsigned LDS_BYTES = 163840;

struct Params {
  const u16 *are, *aim, *ldt, *dsk, *bre, *bim, *cre, *cim;
  char* ws;
  const u16 *x, *wg, *wu, *wd;
  u16* out;
};

DEV i4 mksrd(const void* p, unsigned bytes) {
  u64 a = (u64)p; i4 r; r[0] = (int)(unsigned)a; r[1] = (int)(unsigned)(a >> 32); r[2] = (int)bytes; r[3] = 0x00020000; return r;
}
DEV rsrc_t mkrs(const void* p, unsigned bytes) { return __builtin_amdgcn_make_buffer_rsrc((void*)p, 0, (int)bytes, 0x00020000); }
DEV void touch(i4 srd, unsigned voff, unsigned soff) {
  asm volatile("s_mov_b32 m0, %3\n\ts_nop 0\n\tbuffer_load_dword %1, %0, %2 offen lds" ::"s"(srd), "v"(voff), "s"(soff), "s"(LJK) : "memory");
}
DEV void st128(u4 v, rsrc_t rs, unsigned voff, unsigned soff) {
  __builtin_amdgcn_raw_buffer_store_b128(v, rs, voff, soff, SC1);
  asm volatile("s_nop 1" ::"v"(v) : "memory");
}
template <int N> DEV void waitv() { asm volatile("s_waitcnt vmcnt(%0)" ::"i"(N) : "memory"); }
DEV void waitl() { __builtin_amdgcn_s_waitcnt(0xC07F); asm volatile("" ::: "memory"); }
DEV void bar() { asm volatile("" ::: "memory"); __builtin_amdgcn_s_waitcnt(0xC07F); asm volatile("s_barrier" ::: "memory"); }
DEV void set_m0(unsigned base, unsigned imm) { asm volatile("s_add_u32 m0, %0, %1" ::"s"(base), "i"(imm) : "memory", "scc"); }
template <int OFF> DEV void glds_at(i4 srd, unsigned voff, unsigned soff) {
  asm volatile("buffer_load_dwordx4 %1, %0, %2 offen offset:%3 lds" ::"s"(srd), "v"(voff), "s"(soff), "i"(OFF) : "memory");
}
template <class T> DEV void pin(T& x) { asm volatile("" : "+v"(x)); }
template <class T> DEV void pins(T& x) { asm volatile("" : "+s"(x)); }
DEV bf4 trd(unsigned a) { return __builtin_amdgcn_ds_read_tr16_b64_v4bf16(LDSP(bf4, a)); }
DEV bf8 cat(bf4 a, bf4 b) { return __builtin_shufflevector(a, b, 0, 1, 2, 3, 4, 5, 6, 7); }
template <class TA, class TB> DEV void mma(f4& c, TA a, TB b) { asm volatile("v_mfma_f32_16x16x32_bf16 %0, %1, %2, %0" : "+v"(c) : "v"(a), "v"(b)); }
template <class TA, class TB> DEV void mma0(f4& c, TA a, TB b) { asm volatile("v_mfma_f32_16x16x32_bf16 %0, %1, %2, 0" : "=&v"(c) : "v"(a), "v"(b)); }
DEV void mma_pick(f4& c, u4 a, bf8 b0, bf8 b1, bf8 b2, bf8 b3, int t) {
  asm volatile("s_bitcmp1_b32 %6, 1\n\ts_cbranch_scc1 .Lp2_%=\n\ts_bitcmp1_b32 %6, 0\n\ts_cbranch_scc1 .Lp1_%=\n\t"
               "v_mfma_f32_16x16x32_bf16 %0, %1, %2, %0\n\ts_branch .Lp4_%=\n"
               ".Lp1_%=:\n\tv_mfma_f32_16x16x32_bf16 %0, %1, %3, %0\n\ts_branch .Lp4_%=\n"
               ".Lp2_%=:\n\ts_bitcmp1_b32 %6, 0\n\ts_cbranch_scc1 .Lp3_%=\n\t"
               "v_mfma_f32_16x16x32_bf16 %0, %1, %4, %0\n\ts_branch .Lp4_%=\n"
               ".Lp3_%=:\n\tv_mfma_f32_16x16x32_bf16 %0, %1, %5, %0\n"
               ".Lp4_%=:" : "+v"(c) : "v"(a), "v"(b0), "v"(b1), "v"(b2), "v"(b3), "s"(t) : "scc");
}
DEV void mma_fence() { asm volatile("s_nop 7\n\ts_nop 1" ::: "memory"); }
DEV float blo(unsigned v) { return __uint_as_float(v << 16); }
DEV float bhi(unsigned v) { return __uint_as_float(v & 0xffff0000u); }
DEV f2 unpk(unsigned v) { return f2{blo(v), bhi(v)}; }
DEV unsigned pk(f2 v) { return __builtin_bit_cast(unsigned, __builtin_convertvector(v, bf2)); }
DEV float rbf(float v) { return blo(pk(f2{v, v})); }
DEV f2 fma2(f2 a, f2 b, f2 c) { return __builtin_elementwise_fma(a, b, c); }
template <int CTRL> DEV float dpp_add(float v) {
  return v + __builtin_bit_cast(float, __builtin_amdgcn_update_dpp(0, __builtin_bit_cast(int, v), CTRL, 0xf, 0xf, true));
}
DEV void swap16(unsigned& a, unsigned& b) { auto r = __builtin_amdgcn_permlane16_swap(a, b, false, false); a = r[0]; b = r[1]; }
DEV void swap32(unsigned& a, unsigned& b) { auto r = __builtin_amdgcn_permlane32_swap(a, b, false, false); a = r[0]; b = r[1]; }
DEV float swap16_add(float a, float b) { unsigned x = __float_as_uint(a), y = __float_as_uint(b); swap16(x, y); return __uint_as_float(x) + __uint_as_float(y); }
DEV float swap32_add(float a, float b) { unsigned x = __float_as_uint(a), y = __float_as_uint(b); swap32(x, y); return __uint_as_float(x) + __uint_as_float(y); }
DEV float rdl(float v, int l) { return __builtin_bit_cast(float, __builtin_amdgcn_readlane(__builtin_bit_cast(int, v), l)); }
DEV unsigned uni(unsigned v) { return (unsigned)__builtin_amdgcn_readfirstlane((int)v); }
DEV u4 row_load(i4 srd, unsigned voff, unsigned soff) {
  u4 r; asm volatile("buffer_load_dwordx4 %0, %2, %1, %3 offen" : "=v"(r) : "s"(srd), "v"(voff), "s"(soff) : "memory"); return r;
}
constexpr unsigned SPIN_CAP = 1u << 15;
DEV u2 ld_flag(rsrc_t rs, unsigned voff, unsigned soff) { return __builtin_amdgcn_raw_buffer_load_b64(rs, voff, soff, SC1); }
DEV unsigned flag_off(int lane, int size) { return lane < size / 8 ? lane * 8 : 0x7ffffff0u; }
DEV void st_flag(rsrc_t rs, u2 tag, unsigned soff, int lane, int size = 128) { __builtin_amdgcn_raw_buffer_store_b64(tag, rs, flag_off(lane, size), soff, SC1); }
DEV bool flags_up(u2 tag, u2 v) { return __builtin_amdgcn_ballot_w64(__builtin_bit_cast(u64, v) != __builtin_bit_cast(u64, tag)) == 0; }
template <int NAP> DEV void wait_flags(rsrc_t rs, u2 tag, u2 v, unsigned voff, unsigned soff) {
#pragma nounroll
  for (unsigned spin = 0; spin < SPIN_CAP; ++spin) {
    if (flags_up(tag, v)) break;
    __builtin_amdgcn_s_sleep(NAP);
    asm volatile("" ::: "memory");
    v = ld_flag(rs, voff, soff);
  }
  asm volatile("" : "+v"(v)::"memory");
}
DEV void lds_post(unsigned a, int lane) { asm volatile("" ::: "memory"); if (lane == 0) *LDSP(volatile unsigned, a) = 1u; }
template <int NAP> DEV void lds_wait(unsigned a, unsigned want) {
#pragma nounroll
  for (unsigned spin = 0; spin < (SPIN_CAP << 6); ++spin) {
    if (uni(*LDSP(volatile unsigned, a)) >= want) break;
    if (NAP) __builtin_amdgcn_s_sleep(NAP);
  }
  asm volatile("" ::: "memory");
}
DEV unsigned lds_arrive(unsigned a, int lane) {
  asm volatile("s_waitcnt lgkmcnt(0)" ::: "memory");
  unsigned o = 0;
  if (lane == 0) o = __hip_atomic_fetch_add(LDSP(unsigned, a), 1u, __ATOMIC_RELAXED, __HIP_MEMORY_SCOPE_WORKGROUP);
  return uni(o);
}

#define TILE_P(c) (c.xc >> 2)
#define TILE_G(c) (c.xc & 3)
#define TILE_T(c) (TILE_G(c) * 8 + 2 * (c.jj & 3) + TILE_P(c))
#define TILE_C(c) (c.jj >> 2)
struct Ctx {
  int xc, jj, w, v, lane, n, q;
  u2 tag;
  rsrc_t ws;
};

struct alignas(16) F1 { float v; };
DEV float red4(float a, float b, float c, float d) {
  float s = swap16_add(swap32_add(a, b), swap32_add(c, d));
  s = dpp_add<0x128>(s); s = dpp_add<0x124>(s); s = dpp_add<0x122>(s); s = dpp_add<0x121>(s);
  return s;
}
DEV int ln_row(int r) { return r * 1088 + (r & 8) * 4; }
DEV void ln_compute(const Ctx& c, const u4* rows, unsigned img, int tok0) {
  f2 x[4][4]; float t[4];
#pragma unroll
  for (int k = 0; k < 4; k++) {
#pragma unroll
    for (int i = 0; i < 4; i++) x[k][i] = unpk(rows[k][i]);
    const f2 s = (x[k][0] + x[k][1]) + (x[k][2] + x[k][3]); t[k] = s.x + s.y;
  }
  float mean = red4(t[0], t[2], t[1], t[3]) * (1.f / D);
#pragma unroll
  for (int k = 0; k < 4; k++) {
    const float m = rdl(mean, 16 * k);
    f2 qq = {0.f, 0.f};
#pragma unroll
    for (int i = 0; i < 4; i++) { x[k][i] -= m; qq = fma2(x[k][i], x[k][i], qq); }
    t[k] = qq.x + qq.y;
  }
  float var = red4(t[0], t[2], t[1], t[3]) * (1.f / D);
  const unsigned st = pk(f2{mean, __builtin_amdgcn_sqrtf(rbf(rbf(var) + 1e-5f))});
  const float r = __builtin_amdgcn_rcpf(bhi(st)), o = (mean - blo(st)) * r;
#pragma unroll
  for (int k = 0; k < 4; k++) {
    const float rk = rdl(r, 16 * k), ok = rdl(o, 16 * k);
    u4 nv;
#pragma unroll
    for (int i = 0; i < 4; i++) nv[i] = pk(fma2(x[k][i], f2{rk, rk}, f2{ok, ok}));
    *LDSP(u4, img + ln_row(4 * c.w + k) + c.lane * 16) = nv;
  }
  if (c.n == 0) *LDSP(unsigned, img + 1024 + (c.w >> 1) * 1088 + (4 * (c.w & 1) + c.q) * 4) = st;
}
DEV void ln_store(const Ctx& c, unsigned img, int tok0) {
  const unsigned a = img + ln_row(8 * c.q + (c.n >> 2)) + (64 * c.w + 4 * (c.n & 3)) * 2;
  if (c.w == 7) __builtin_amdgcn_raw_buffer_store_b32(*LDSP(unsigned, img + 1024 + ((c.lane >> 3) & 3) * 1088 + (c.lane & 7) * 4), c.ws,
                                                      c.lane < 32 ? c.lane * 4 : 0x7ffffff0u, WS_STATS + tok0 * 4, SC1);
  bf8 t[4];
#pragma unroll
  for (int i = 0; i < 4; i++) t[i] = cat(trd(a + 32 * i), trd(a + 32 * i + 4 * 1088));
#pragma unroll
  for (int i = 0; i < 4; i++) st128(__builtin_bit_cast(u4, t[i]), c.ws, c.n * 64 + c.q * 16 + i * 1024, WS_XNT + tok0 * 1024 + c.w * 4096);
}
DEV unsigned u_off(const Ctx& c) { return (c.lane >> 3) * 32768 + (c.lane & 7) * 16; }
DEV u4 u_load(const Ctx& c, unsigned uv, int i) {
  return __builtin_amdgcn_raw_buffer_load_b128(c.ws, uv, WS_XNT + (16 * c.v + 8 * i) * 32768 + (c.xc * 32 + c.jj) * 128, SC1);
}
DEV void u_write(const Ctx& c, u4 r0, u4 r1) {
  const unsigned a = LU + ((c.lane >> 2) & 1) * UCH + 512 + (16 * c.v + (c.lane >> 3)) * 64 + (c.lane & 3) * 16;
  *LDSP(u4, a) = r0;
  *LDSP(u4, a + 512) = r1;
  if (c.v == 0) *LDSP(u4, LU + (c.lane & 1) * UCH + (c.lane >> 1) * 16) = u4{0, 0, 0, 0};
}
DEV u2 flag_sample(const Params& P, unsigned voff, unsigned soff) {
  u2 r; asm volatile("buffer_load_dwordx2 %0, %2, %1, %3 offen sc1" : "=v"(r) : "s"(mksrd(P.ws, WS_BYTES)), "v"(voff), "s"(soff) : "memory"); return r;
}

struct C2 { f2 re, im; };
DEV C2 cmul(C2 a, C2 b) { C2 r; r.re = fma2(a.re, b.re, -(a.im * b.im)); r.im = fma2(a.re, b.im, a.im * b.re); return r; }
DEV C2 zpow(f2 al, f2 th, float e) {
  const f2 a = al * e, t0 = th * e;
  const f2 t = {__builtin_amdgcn_fractf(t0.x), __builtin_amdgcn_fractf(t0.y)};
  const f2 m = {__builtin_amdgcn_exp2f(a.x), __builtin_amdgcn_exp2f(a.y)};
  C2 r; r.re = m * f2{__builtin_amdgcn_cosf(t.x), __builtin_amdgcn_cosf(t.y)}; r.im = m * f2{__builtin_amdgcn_sinf(t.x), __builtin_amdgcn_sinf(t.y)};
  return r;
}
struct KIn { unsigned are, aim, bre, bim, cre, cim, ldt, dsk; u64 epoch; };
DEV unsigned par_load(const u16* p, unsigned voff) {
  unsigned r; asm volatile("global_load_dword %0, %1, %2" : "=v"(r) : "v"(voff), "s"(p) : "memory"); return r;
}
DEV unsigned spar_load(const u16* p, unsigned off) { unsigned r; asm volatile("s_load_dword %0, %1, %2" : "=s"(r) : "s"(p), "s"(off) : "memory"); return r; }
template <int ROLE> DEV KIn kgen_load(const Ctx& c, const Params& P) {
  const unsigned pair = c.xc * 32 + c.jj, o = (2 * pair + (c.w & 1)) * 256 + c.lane * 4;
  KIn r;
  r.are = par_load(P.are, o); r.aim = par_load(P.aim, o);
  r.ldt = spar_load(P.ldt, pair * 4); r.dsk = spar_load(P.dsk, pair * 4);
  asm volatile("s_load_dwordx2 %0, %1, 0" : "=s"(r.epoch) : "s"(P.ws + WS_EPOCH) : "memory");
  if (ROLE == 0 && c.w < 2) { r.bre = par_load(P.bre, o); r.bim = par_load(P.bim, o); r.cre = par_load(P.cre, o); r.cim = par_load(P.cim, o); }
  return r;
}
template <int ROLE, int N> DEV void kgen_take(Ctx& c, const Params& P, KIn& k) {
  KIn o; char* ws = P.ws;
  asm volatile("s_waitcnt lgkmcnt(0)\n\ts_mov_b32 %0, %4\n\ts_mov_b32 %1, %5\n\ts_mov_b64 %2, %6"
               : "=&s"(o.ldt), "=&s"(o.dsk), "=&s"(o.epoch), "+s"(ws) : "s"(k.ldt), "s"(k.dsk), "s"(k.epoch) : "memory");
  asm volatile("s_waitcnt vmcnt(%4)\n\tv_mov_b32 %0, %2\n\tv_mov_b32 %1, %3" : "=&v"(o.are), "=&v"(o.aim) : "v"(k.are), "v"(k.aim), "i"(N) : "memory");
  if (ROLE == 0) asm volatile("v_mov_b32 %0, %4\n\tv_mov_b32 %1, %5\n\tv_mov_b32 %2, %6\n\tv_mov_b32 %3, %7"
               : "=&v"(o.bre), "=&v"(o.bim), "=&v"(o.cre), "=&v"(o.cim) : "v"(k.bre), "v"(k.bim), "v"(k.cre), "v"(k.cim) : "memory");
  k = o;
  c.tag = u2{(unsigned)(o.epoch + 1), (unsigned)((o.epoch + 1) >> 32)};
  c.ws = mkrs(ws, WS_BYTES);
}
constexpr unsigned LKB = LKG + 44416;
struct KPre { float del, g0, g1, rev; unsigned wa, wb, rd, sh, rs, ta, tb, s0, s1; bool sw; };
template <int ROLE> DEV void kgen_pre(const Ctx& c, KPre& k) {
  const bool hi = c.v >= 2;
  const int ch = c.w & 1;
  const unsigned T = ROLE == 0 ? (hi ? LKG : LKB) + ch * 8672 : (hi ? LKG + 17344 + ch * 8672 : LKG + 34688 + ch * 4864);
  k.del = ROLE == 0 ? (hi ? 1.f : 8.f) : (hi ? 128.f : 2.f);
  k.g0 = ROLE == 0 ? (hi ? 128.f : 64.f) : (hi ? 512.f : 256.f);
  k.g1 = hi ? 516.f : 2048.f;
  k.rev = (ROLE == 0 && !hi) ? -0.15915494309189535f : 0.15915494309189535f;
  k.wa = T + c.lane * 8; k.wb = k.wa + ((ROLE == 1 && !hi) ? 4 : 8) * 544; k.rd = T + c.n * 544 + c.q * 16;
  k.sh = ch ? 0 : 16;
  k.rs = hi ? 1088 : 544;
  pin(k.del); pin(k.g0); pin(k.wa); pin(k.rd);
  if (ROLE == 0) pin(k.rev); else { pin(k.g1); pin(k.wb); }
}
template <int ROLE> DEV void ktab_pre(const Ctx& c, KPre& k) {
  const bool hi = c.v >= 2;
  const int n = c.n, q = c.q;
  const unsigned tb = LKT + (c.w & 1) * 1536;
  if (ROLE == 0) { k.ta = hi ? tb + 256 * (q >> 1) + 8 * (q & 1) + 16 * n : LJK + c.lane * 8; pin(k.ta); }
  else {
    const bool far = hi && q >= 2;
    k.ta = tb + 128 * q + (!hi ? (q < 2 ? 512 : 1024) + 8 * n : 768 + (q < 2 ? 4 * n : 62 * (n & 1) + 2 * n));
    k.tb = k.ta + (!hi ? 4 : q < 2 ? 64 : 32);
    k.s0 = !far ? 0x03020100u : (n & 1) ? 0x01000504u : 0x05040100u;
    k.s1 = k.s0 + (far ? 0x02020202u : 0x04040404u);
    k.sw = far && (n & 1);
    pin(k.ta); pin(k.tb); pin(k.s0); pin(k.s1);
  }
}
template <int ROLE, class Hook>
DEV void kgen_rows(const Ctx& c, const KPre& p, const KIn& k, bf8 (&A)[8], Hook&& hook) {
  const float dt = rbf(__builtin_amdgcn_exp2f(bhi(k.ldt << p.sh) * 1.4426950408889634f));
  const f2 al = (unpk(k.are) * dt) * 1.4426950408889634f;
  const f2 th = (unpk(k.aim) * dt) * p.rev;
  auto put = [&](unsigned ad, C2 z) __attribute__((always_inline)) { *LDSP(u2, ad) = u2{pk(z.re), pk(z.im)}; };
  C2 st = zpow(al, th, p.del);
  pin(st.re); pin(st.im);
  hook(IC<0>{});
  C2 a, b, e;
  if (ROLE == 0) {
    a = C2{f2{1.f, 1.f}, f2{0.f, 0.f}}; b = zpow(al, th, p.g0);
    if (c.w < 2) { a = cmul(C2{unpk(k.cre), -unpk(k.cim)}, C2{unpk(k.bre) * dt, unpk(k.bim) * -dt}); b = cmul(b, a); }
  } else {
    a = zpow(al, th, p.g0);
    if (c.w >= 6) b = cmul(a, a); else b = zpow(al, th, 384.f);
  }
  pin(b.re); pin(b.im);
  hook(IC<1>{});
  sfor<0, 4>(LAM(I) {
    constexpr int i = CV(I);
    put(p.wa + i * (ROLE ? p.rs : 544), a); put(p.wb + i * 544, b);
    hook(IC<2 + i>{});
    if constexpr (ROLE == 0 || i < 3) a = cmul(a, st); else a = zpow(al, th, p.g1);
    b = cmul(b, st);
  });
  if (ROLE == 1 && c.w < 6) { put(p.wa + 8 * 544, a); hook(IC<6>{}); hook(IC<7>{}); hook(IC<8>{}); }
  else sfor<4, 8>(LAM(I) {
    constexpr int i = CV(I);
    put(p.wa + (ROLE ? 2 * i - 7 : i) * 544, a); put(p.wb + i * 544, b);
    if constexpr (i < 7) { hook(IC<2 + i>{}); a = cmul(a, st); b = cmul(b, st); }
  });
#pragma unroll
  for (int i = 0; i < 8; i++) A[i] = *LDSP(bf8, p.rd + i * 64);
}
DEV void kgen_cols(const Ctx& c, bf8 (&B)[8]) {
#pragma unroll
  for (int i = 0; i < 8; i++) B[i] = *LDSP(bf8, LKB + (c.w & 1) * 8672 + c.n * 544 + c.q * 16 + i * 64);
}
DEV void kgen_mma(f4& t, const bf8 (&A)[8], const bf8 (&B)[8]) {
  mma0(t, A[0], B[0]);
#pragma unroll
  for (int i = 1; i < 8; i++) mma(t, A[i], B[i]);
}
struct alignas(16) U1 { unsigned v; };
struct KTab { f4 t; U1 a, b, p[2]; };
constexpr int KT_OPS = 9;
template <int ROLE, int k> DEV void ktab_op(const Ctx& c, const KPre& p, KTab& z) {
  if constexpr (ROLE == 0) {
    if constexpr (k == 0) { pin(z.t); z.t[0] += __uint_as_float(z.a.v); pin(z.t); }
    if constexpr (k == 1) { z.p[1].v = pk(f2{z.t[2], z.t[3]}); pin(z.p[1].v); }
    if constexpr (k == 2) { z.p[0].v = pk(f2{z.t[0], z.t[1]}); pin(z.p[0].v); }
    if constexpr (k == 3) *LDSP(u2, p.ta) = u2{z.p[0].v, z.p[1].v};
  } else {
    if constexpr (k == 0) { pin(z.t); z.p[0].v = pk(f2{z.t[0], z.t[1]}); pin(z.p[0].v); }
    if constexpr (k == 1) { z.p[1].v = pk(f2{z.t[2], z.t[3]}); pin(z.p[1].v); }
    if constexpr (k == 2) { z.b.v = p.sw ? z.p[0].v : z.p[1].v; pin(z.b.v); }
    if constexpr (k == 3) { z.a.v = p.sw ? z.p[1].v : z.p[0].v; pin(z.a.v); }
    if constexpr (k == 4) { z.b.v = (unsigned)__builtin_amdgcn_update_dpp((int)z.b.v, (int)z.b.v, 0xb1, 0xc, 0xf, false); pin(z.b.v); }
    if constexpr (k == 5) { z.p[0].v = __builtin_amdgcn_perm(z.b.v, z.a.v, p.s0); pin(z.p[0].v); }
    if constexpr (k == 6) { z.p[1].v = __builtin_amdgcn_perm(z.b.v, z.a.v, p.s1); pin(z.p[1].v); }
    if constexpr (k == 7) *LDSP(unsigned, p.ta) = z.p[0].v;
    if constexpr (k == 8) *LDSP(unsigned, p.tb) = z.p[1].v;
  }
}
DEV void ktab_skip(const Ctx& c, const KPre& p, KTab& z, unsigned dsk) { z.a.v = (c.w >= 2 && c.lane == 0) ? (dsk << p.sh) & 0xffff0000u : 0u; pin(z.a.v); }
typedef unsigned u2a __attribute__((ext_vector_type(2), aligned(4)));
struct KExp { U1 a0, a1, a2, a3, s[7], wr, h0, pv[2][2], o[2][4][4], es[3]; u2 e[4]; };
template <int PART> DEV void kexp_pre(const Ctx& c, KExp& z) {
  const int kh = c.v >> 1, l = c.lane;
  const unsigned CC = LKT + (c.v & 1) * 1536;
  const bool up = l >= 32, odd = l & 1;
  if (PART == 0) {
    z.s[0].v = (kh || up) ? 0x01000100u : 0x03020100u;
    z.s[1].v = kh ? 0x03020100u : up ? 0x05040302u : 0x07060504u;
    z.s[2].v = kh ? 0x03020302u : z.s[0].v;
    z.s[3].v = kh ? 0x05040504u : z.s[1].v;
    z.s[4].v = odd ? 0x03020302u : 0x01000100u;
    z.s[5].v = odd ? 0x05040504u : 0x03020100u;
    z.s[6].v = odd ? 0x05040504u : 0x03020302u;
    z.es[0].v = kh ? 0x01000100u : 0x0c0c0c0cu; z.es[1].v = kh ? 0x05040302u : 0x0c0c0c0cu; z.es[2].v = kh ? 0x05040504u : 0x03020302u;
    asm volatile("" : "+s"(z.es[0].v), "+s"(z.es[1].v), "+s"(z.es[2].v));
    asm volatile("" : "+v"(z.s[0].v), "+v"(z.s[1].v), "+v"(z.s[2].v), "+v"(z.s[3].v), "+v"(z.s[4].v), "+v"(z.s[5].v), "+v"(z.s[6].v));
  } else {
    z.a0.v = CC + (kh ? 768 + l * 4 : up ? 256 + l * 8 : l * 16);
    z.a1.v = z.a0.v + (kh ? 0 : up ? 4 : 8);
    z.a2.v = CC + 1024 + 128 * kh + (l >> 1) * 4;
    z.a3.v = CC + 764;
    z.wr.v = LKC + (c.v & 1) * KCH + 32 + kh * 1024 + l * 16;
    asm volatile("" : "+v"(z.a0.v), "+v"(z.a1.v), "+v"(z.a2.v), "+v"(z.a3.v), "+v"(z.wr.v));
  }
}
constexpr int KX_OPS = 45;
template <int k> DEV void kexp_op(const Ctx& c, KExp& z) {
  auto set = [&](U1& d, unsigned x) __attribute__((always_inline)) { d.v = x; pin(d.v); };
  if constexpr (k == 0) z.e[0] = *LDSP(u2a, z.a0.v);
  else if constexpr (k == 1) z.e[1] = *LDSP(u2a, z.a1.v);
  else if constexpr (k == 2) z.e[2] = *LDSP(u2a, z.a2.v);
  else if constexpr (k == 3) z.e[3] = *LDSP(u2a, z.a3.v);
  else if constexpr (k == 4) z.h0.v = *LDSP(unsigned, z.a3.v + 256);
  else if constexpr (k < 9) { constexpr int i = k - 5; set(z.o[0][0][i], __builtin_amdgcn_perm(z.e[i >> 1][1], z.e[i >> 1][0], z.s[i].v)); }
  else if constexpr (k < 13) { constexpr int i = k - 9;
    if constexpr (i == 1) asm volatile("v_mov_b32 %0, %1" : "=v"(z.o[1][0][1].v) : "v"(z.o[1][0][0].v));
    else set(z.o[1][0][i], __builtin_amdgcn_perm(z.e[2][1], z.e[2][0], z.s[i ? 3 + i : 4].v));
  }
  else if constexpr (k == 13) { z.pv[0][0].v = __builtin_amdgcn_perm(z.e[3][1], z.e[3][0], z.es[0].v); pin(z.pv[0][0].v); }
  else if constexpr (k == 14) { z.pv[0][1].v = __builtin_amdgcn_perm(z.e[3][1], z.e[3][0], z.es[1].v); pin(z.pv[0][1].v); }
  else if constexpr (k == 15) { z.pv[1][0].v = __builtin_amdgcn_perm(z.e[2][0], z.h0.v, z.es[2].v); pin(z.pv[1][0].v); }
  else if constexpr (k == 16) { z.pv[1][1].v = z.o[1][0][0].v; pin(z.pv[1][1].v); }
  else if constexpr (k < 21) {
    constexpr int i = (k - 17) >> 1, j = (k - 17) & 1;
    z.pv[i][j].v = (unsigned)__builtin_amdgcn_update_dpp((int)z.pv[i][j].v, (int)z.o[i][0][2 + j].v, 0x138, 0xf, 0xf, false); pin(z.pv[i][j].v);
  } else {
    constexpr int i = (k - 21) / 12, cp = 1 + (k - 21) % 12 / 4, j = (k - 21) % 4;
    auto dw = [&](int m) __attribute__((always_inline)) { return m < 0 ? z.pv[i][2 + m].v : z.o[i][0][m].v; };
    if constexpr (cp == 2) asm volatile("v_mov_b32 %0, %1" : "=v"(z.o[i][2][j].v) : "v"(dw(j - 1)));
    else asm volatile("v_alignbit_b32 %0, %1, %2, 16" : "=v"(z.o[i][cp][j].v) : "v"(dw(j - (cp >> 1))), "v"(dw(j - 1 - (cp >> 1))));
  }
}
template <int k> DEV void kexp_put(const Ctx& c, const KExp& z) {
  if constexpr (k < 8) *LDSP(u4, z.wr.v + (k >> 2) * 2048 + (k & 3) * KCS) = u4{z.o[k >> 2][k & 3][0].v, z.o[k >> 2][k & 3][1].v, z.o[k >> 2][k & 3][2].v, z.o[k >> 2][k & 3][3].v};
  else *LDSP(unsigned, LKC + (c.lane >> 3) * KCS + (c.lane & 7) * 4) = 0u;
}

struct WdSrc { i4 srd; unsigned voff[4], par; };
DEV WdSrc wd_src(const Ctx& c, const Params& P) {
  WdSrc s; s.srd = mksrd(P.wd, F * D * 2); s.par = TILE_P(c) * 32768;
  const int r = c.lane >> 3;
#pragma unroll
  for (int k = 0; k < 4; k++) { s.voff[k] = (448 * c.v + r) * 1024 + TILE_C(c) * 128 + (((c.lane & 7) ^ (r & 6)) * 16) + k * 7168; pin(s.voff[k]); }
  return s;
}
DEV unsigned wd_raddr(int v, int n, int q) { return v * 4096 + (4 * q + (n >> 2)) * 128 + (((2 * q + (n >> 3)) & 3) * 32) + (n & 3) * 8; }
DEV void wd_ld(const WdSrc& s, int i, int r) {
  const unsigned so = s.par ^ ((i % 7) * 65536 + (i / 7) * 32768);
  if (r == 0) glds_at<0>(s.srd, s.voff[0], so);
  else if (r == 1) glds_at<1024>(s.srd, s.voff[1], so);
  else if (r == 2) glds_at<2048>(s.srd, s.voff[2], so);
  else glds_at<3072>(s.srd, s.voff[3], so);
}
DEV void wd_chunk(const Ctx& c, const WdSrc& s, int i) {
  set_m0(c.v * 4096, wd_base(i % 7)); asm volatile("s_nop 0");
#pragma unroll
  for (int r = 0; r < 4; r++) wd_ld(s, i, r);
}

constexpr int snake(int i) { return (i & 4) ? 3 - (i & 3) : i & 3; }
struct Frags { bf4 a[4][2]; bf8 b[4]; };
DEV void rd_piece(Frags& Fr, int i, unsigned wa0, unsigned wa1, unsigned xa, int ks) {
  if (i == 0) Fr.b[0] = *LDSP(bf8, xa);
  else if (i < 9) { const int t = (i - 1) >> 1, h = (i - 1) & 1; Fr.a[t][h] = trd(((t & 1) ? wa1 : wa0) + (t >> 1) * 16384 + ks * 8192 + h * 1024); }
  else Fr.b[i - 8] = *LDSP(bf8, xa + (i - 8) * 2048);
}

struct G1Src { i4 sx, sg, su; unsigned vx0, vx1, vw0, vw1; int k0; };
DEV G1Src g1_src(const Ctx& c) {
  const int p = (c.xc & 3) * 4 + (c.jj & 3), ft = (c.xc >> 2) * 7 + (c.jj >> 2);
  G1Src g;
  g.k0 = 2 * (c.xc & 3);
  const int xr = c.lane >> 3;
  g.vx0 = (p * 128 + 16 * c.w + xr) * 1024 + (((c.lane & 7) ^ (xr >> 1)) * 16);
  g.vx1 = (g.vx0 ^ 64) + 8192 - 1024;
  g.vw0 = (8 * c.w + c.q) * 3584 + ft * 256 + ((c.n ^ ((c.q | ((c.w & 1) << 2)) << 1)) * 16);
  g.vw1 = g.vw0 + 4 * 3584 - 1024;
  asm volatile("" : "+v"(g.vx0), "+v"(g.vx1), "+v"(g.vw0), "+v"(g.vw1));
  return g;
}
DEV void g1_m0(const Ctx& c, int s, int i) { set_m0(c.w * 2048, slot_of(s) + (i >> 1) * 16384); }
DEV void g1_ld(const G1Src& g, int s, int i) {
  const int sk = (s + g.k0) & 7;
  if (i == 0) glds_at<0>(g.sx, g.vx0, sk * 128);
  else if (i == 1) glds_at<1024>(g.sx, g.vx1, sk * 128);
  else if (i & 1) glds_at<1024>(i > 3 ? g.su : g.sg, g.vw1, sk * 64 * 3584);
  else glds_at<0>(i > 3 ? g.su : g.sg, g.vw0, sk * 64 * 3584);
}
struct G1Rot { i4 srd[2]; unsigned vo[2], dst[4], so[2]; };
DEV G1Rot g1_rot(const Ctx& c, const G1Src& g) {
  const int mine = c.jj & 3;
  G1Rot r;
#pragma unroll
  for (int k = 0; k < 2; k++) {
    const bool up = ((mine >> 1) ^ k) & 1;
    r.srd[k] = g.sg; r.srd[k][0] = up ? g.su[0] : g.sg[0]; r.srd[k][1] = up ? g.su[1] : g.sg[1];
    r.vo[k] = g.vw0 + ((mine ^ k) & 1) * 4 * 3584;
    r.so[k] = (g.k0 + k) * 64 * 3584;
  }
#pragma unroll
  for (int k = 0; k < 4; k++) r.dst[k] = 16384 + ((mine ^ k) >> 1) * 16384 + c.w * 2048 + ((mine ^ k) & 1) * 1024;
  return r;
}
template <unsigned IT0, unsigned IT1> DEV void g1_fetch(const Ctx& c, const G1Src& gs, const G1Rot& rot) {
    const unsigned bx0 = gs.vx0 - 65536, bx1 = gs.vx1 - 65536, bw0 = rot.vo[0] - 32 * 3584, bw1 = rot.vo[1] - 32 * 3584;
    const unsigned ox0 = gs.k0 * 128, ow0 = rot.so[0], dx = c.v * 2048, d1 = rot.dst[1] - 8192, d2 = rot.dst[2] - 8192, d3 = rot.dst[3] - 8192;
#pragma unroll
    for (unsigned it = IT0; it < IT1; it++) {
      const unsigned st = it >> 2, k = it & 3, dm = st * slot_of(1) + k * 4096;
      unsigned ox, ow;
      asm volatile("s_add_u32 m0, %11, %15\n\ts_add_u32 %0, %9, %16\n\tbuffer_load_dwordx4 %5, %2, %0 offen lds\n\t"
                   "buffer_load_dwordx4 %6, %2, %0 offen offset:1024 lds\n\t"
                   "s_add_u32 m0, %12, %15\n\ts_add_u32 %1, %10, %17\n\tbuffer_load_dwordx4 %8, %3, %1 offen lds\n\t"
                   "s_add_u32 m0, %13, %15\n\ts_nop 0\n\tbuffer_load_dwordx4 %7, %4, %1 offen lds\n\t"
                   "s_add_u32 m0, %14, %15\n\ts_nop 0\n\tbuffer_load_dwordx4 %8, %4, %1 offen lds"
                   : "=&s"(ox), "=&s"(ow)
                   : "s"(gs.sx), "s"(rot.srd[0]), "s"(rot.srd[1]), "v"(bx0), "v"(bx1), "v"(bw0), "v"(bw1), "s"(ox0), "s"(ow0),
                     "s"(dx), "s"(d1), "s"(d2), "s"(d3), "i"(dm), "i"(st * 128 + k * 32768), "i"((st * 64 + k * 16) * 3584)
                   : "memory", "scc");
      if (it < 2) __builtin_amdgcn_s_sleep(2);
    }
}

constexpr int FEED_N[11] = {4, 4, 3, 4, 3, 3, 3, 3, 3, 3, 3};
constexpr int feed_done(int h) { int e = 12; for (int k = 0; k <= h && k < 11; k++) e += FEED_N[k]; return e; }
constexpr int feed_at(int h, int i) {
  if (h > 10) return -1;
  const int nn = FEED_N[h], e0 = feed_done(h) - nn;
  for (int k = 0; k < nn; k++) if ((nn == 4 ? 1 + 4 * k : 4 + 5 * k) == i) return e0 + k;
  return -1;
}
constexpr int feed_younger(int s) { return feed_done(2 * s) - 6 * (s + 2); }
constexpr bool feed_ok() {
  for (int s = 0; s < 6; s++) if (feed_younger(s) < 0 || feed_done(2 * s) > 6 * (s + 3)) return false;
  return feed_done(10) == 48 && FEED_N[0] <= 4;
}
static_assert(feed_ok(), "load schedule");
constexpr int wd_at(int h, int i) { for (int e = 0; e < 28; e++) if (11 + e / 6 == h && 1 + (e % 6) * 5 / 2 == i) return e; return -1; }

template <int ROLE>
DEV void gemm1(Ctx& c, const Params& P, G1Src gs, KIn kin, WdSrc& wds, f4 (&acc)[4][4], bf4 (&W1)[7][4][2], unsigned (&wa)[4], KExp& kx, u4 (&ur)[2], f4 (&cv)[4], f4 (&tc)[4]) {
  const int p = (c.xc & 3) * 4 + (c.jj & 3), ft = (c.xc >> 2) * 7 + (c.jj >> 2);
  const int lane = c.lane, n = c.n, q = c.q, v = c.v;
  gs.sx = mksrd(P.x, L * D * 2); gs.sg = mksrd(P.wg, D * F * 2); gs.su = mksrd(P.wu, D * F * 2);
  G1Rot rot = g1_rot(c, gs);
  set_m0(rot.dst[0], 0); asm volatile("s_nop 0"); glds_at<0>(rot.srd[0], rot.vo[0], rot.so[0]);
  if (ROLE == 0) touch(gs.sx, (lane < 10 ? lane >> 1 : 4) * 1024 + (lane & 1) * 128, (p * 128 + (c.jj >> 2) * 18 + 5 * v) * 1024 + gs.k0 * 128);
  set_m0(rot.dst[0], slot_of(1)); asm volatile("s_nop 0"); glds_at<0>(rot.srd[0], rot.vo[0], rot.so[1]);
  if (ROLE == 1) __builtin_amdgcn_s_setprio(1);
  SB();
  KPre kp;
  kgen_pre<ROLE>(c, kp);
  unsigned xa[2][2], wr[3][2];
  xa[0][0] = (((ROLE ^ v) & 1) * 64 + n) * 128 + ((q ^ (n >> 1)) * 16); xa[0][1] = xa[0][0] ^ 64;
  wr[0][0] = 16384 + (8 * q + (n >> 2)) * 256 + (((2 * v) ^ ((n >> 2) | ((q & 1) << 2))) * 32) + (n & 3) * 8; wr[0][1] = wr[0][0] ^ 32;
  unsigned kbr = LKB + (c.w & 1) * 8672 + n * 544 + q * 16;
  asm volatile("" : "+v"(xa[0][0]), "+v"(xa[0][1]), "+v"(wr[0][0]), "+v"(wr[0][1]), "+v"(kbr));
  unsigned lfv, cpv, l4, stv, csa, sta, one0, uv, csv, fco; u4 ones; f4 cs0;
  if (ROLE == 0) {
    if (threadIdx.x < 12) *LDSP(unsigned, LSY + threadIdx.x * 4) = 0u;
    ones = u4{0x3f803f80u, 0x3f803f80u, 0x3f803f80u, 0x3f803f80u}; pin(ones);
    cs0 = f4{0.f, 0.f, 0.f, 0.f}; pin(cs0);
  }
  kgen_take<ROLE, 3 - ROLE>(c, P, kin);
  bf8 KA[8], KB[8]; KTab kt;
  kgen_rows<ROLE>(c, kp, kin, KA, LAM(H) {});
  u2 tagv;
  U1 ga, gb;
  if (ROLE == 1 && v < 2) g1_fetch<0, 4>(c, gs, rot);
  {
#pragma unroll
    for (int k = 0; k < 2; k++) { wr[1][k] = wr[0][k] + slot_of(1); wr[2][k] = wr[0][k] + SLOT_C; xa[1][k] = xa[0][k] + SLOT_C; pin(wr[1][k]); pin(wr[2][k]); pin(xa[1][k]); }
    ga.v = q * 16 + (LCS + v * 256); gb.v = n * 16 + (LST + ((ROLE ^ v) & 1) * 1024); pin(ga.v); pin(gb.v);
    if (ROLE == 1) {
      ktab_pre<1>(c, kp);
      wds = wd_src(c, P);
#pragma unroll
      for (int d = 0; d < 4; d++) { wa[d] = (wd_raddr(v, n, q) ^ (d * 32)) + SLOT_C; pin(wa[d]); }
    }
  }
  if (ROLE == 1 && v < 2) { g1_fetch<4, 8>(c, gs, rot); waitv<20>(); } else waitv<1>();
  __builtin_amdgcn_s_setprio(1);
  bar();
  Frags F0, Fb;
  auto rd = [&](Frags& Fr, int i, int s, int ks) __attribute__((always_inline)) {
    rd_piece(Fr, i, wr[s % 3][0], wr[s % 3][1], xa[s % 3 == 2][ks] + (s % 3 == 1 ? slot_of(1) : 0), ks);
  };
#pragma unroll
  for (int i = 0; i < 12; i++) rd(F0, i, 0, 0);
  SB();
  f4 cs = cs0;
  auto colsum = [&](const Frags& Fr) __attribute__((always_inline)) {
    if (ROLE == 0) {
      mma_pick(cs, ones, cat(Fr.a[0][0], Fr.a[0][1]), cat(Fr.a[1][0], Fr.a[1][1]), cat(Fr.a[2][0], Fr.a[2][1]), cat(Fr.a[3][0], Fr.a[3][1]), c.jj);
    }
    SB();
  };
  u2 lf; u4 cpr; unsigned str; F1 cs4[4], tk[4], tr;
  auto feed = [&](auto H, auto I) __attribute__((always_inline)) {
    constexpr int h = CV(H), i = CV(I);
    constexpr int e = feed_at(h, i), en = feed_at(h, i + 1);
    if constexpr (e >= 0) g1_ld(gs, e / 6, e % 6);
    if constexpr (en >= 0 && !(en & 1)) g1_m0(c, en / 6, en % 6);
    constexpr int d = ROLE ? wd_at(h, i) : -1, dn = ROLE ? wd_at(h, i + 1) : -1;
    if constexpr (d >= 0) wd_ld(wds, d >> 2, d & 3);
    if constexpr (dn >= 0 && !(dn & 3)) set_m0(v * 4096, wd_base(dn >> 2));
  };
  sfor<0, 8>(LAM(S) {
    constexpr int s = CV(S);
    sfor<0, 16>(LAM(I) {
      constexpr int i = CV(I);
      constexpr int ct = snake(i);
      if constexpr (s == 0) mma0(acc[i >> 2][ct], cat(F0.a[ct][0], F0.a[ct][1]), F0.b[i >> 2]);
      else mma(acc[i >> 2][ct], cat(F0.a[ct][0], F0.a[ct][1]), F0.b[i >> 2]);
      if constexpr (s == 0) {
        if constexpr (i < 12) rd(Fb, i, s, 1);
        if constexpr (i >= 6 && i < 14) KB[i - 6] = *LDSP(bf8, kbr + (i - 6) * 64);
      } else if constexpr (i != 4 && i != 9 && i < 14) rd(Fb, i - (i > 4) - (i > 9), s, 1);
      feed(IC<2 * s>{}, I);
      if constexpr (ROLE == 0 && s == 3) kexp_op<16 + i>(c, kx);
      if constexpr (ROLE == 0 && s == 2 && i == 15) {
        pin(cs);
        __builtin_amdgcn_raw_buffer_store_b32(__float_as_uint(cs[0]), c.ws, csv, WS_CPART + (ft * 4 + v) * 1024 + p * 64, SC1);
      }
      if constexpr (ROLE == 0) {
        if constexpr (s == 5 && i == 15) lf = flag_sample(P, lfv, 0);
        if constexpr (s == 6 && i == 4) cpr = __builtin_amdgcn_raw_buffer_load_b128(c.ws, cpv, WS_CPART + (ft * 4 + v) * 1024, SC1);
        if constexpr (s == 7) {
          if constexpr (i == 1) { cs4[0].v = swap32_add(__uint_as_float(cpr[0]), __uint_as_float(cpr[1])); pin(cs4[0].v); }
          if constexpr (i == 4) { cs4[2].v = swap32_add(__uint_as_float(cpr[2]), __uint_as_float(cpr[3])); pin(cs4[2].v); }
          if constexpr (i == 7) { cs4[0].v = swap16_add(cs4[0].v, cs4[2].v); pin(cs4[0].v); }
          if constexpr (i == 7) *LDSP(float, csa) = cs4[0].v;
          if constexpr (i == 8) *LDSP(f4, sta) = f4{tk[0].v, tk[1].v, tk[2].v, tk[3].v};
        }
        if constexpr (s == 6 && i == 2) str = __builtin_amdgcn_raw_buffer_load_b32(c.ws, stv, WS_STATS + (p * 128 + 32 * v) * 4, SC1);
      }
      SB();
    });
    if constexpr (s < 2) colsum(F0);
    if constexpr (ROLE == 0 && s < 5) {
      int ln = lane; pin(ln);
      Ctx d = c; d.lane = ln; d.n = ln & 15; d.q = ln >> 4;
      __builtin_amdgcn_s_setprio(0);
      if constexpr (s == 0) { ktab_pre<0>(d, kp); ktab_skip(d, kp, kt, kin.dsk); l4 = ln * 4; csv = ln < 16 ? l4 : 0x7ffffff0u; pin(l4); pin(csv); }
      if constexpr (s == 1) kexp_pre<0>(d, kx);
      if constexpr (s == 2) kexp_pre<1>(d, kx);
      if constexpr (s == 3) {
        tagv = c.tag; one0 = flag_off(ln, CSZ); pin(tagv); pin(one0);
        unsigned f0 = WS_FC + (v * 14 + ft) * 16 * CSZ, f1 = WS_FLN + (8 * v - 16) * LSZ, f2 = WS_FLN + (2 * p + (v >> 1)) * LSZ;
        fco = f0 + p * CSZ; pins(f0); pins(f1); pins(f2); pins(fco);
        unsigned a0 = ln * CSZ + f0, a1 = ln * LSZ + f1; pin(a0); pin(a1);
        lfv = ln < 16 ? a0 : ln < 24 ? a1 : f2; pin(lfv);
      }
      if constexpr (s == 4) {
        cpv = ln * 16; stv = l4 & 124; sta = LST + 512 * v + 4 * stv;
        csa = LCS + v * 256 + d.n * 16 + (((d.q & 1) << 1) | (d.q >> 1)) * 4; uv = u_off(d);
        pin(cpv); pin(stv); pin(sta); pin(csa); pin(uv);
      }
      __builtin_amdgcn_s_setprio(1);
      SB();
    }
    if constexpr (s == 3 && ROLE == 0) waitv<(feed_younger(s) < FEED_N[5] + FEED_N[6] ? feed_younger(s) : FEED_N[5] + FEED_N[6])>();
    else if constexpr (s < 6) waitv<feed_younger(s) + (ROLE == 0 && (s == 2 || s == 5))>();
    else if constexpr (ROLE == 1) waitv<12>();
    bar();
    SB();
    if constexpr (ROLE == 1) __builtin_amdgcn_s_setprio(1);
    sfor<0, 16>(LAM(I) {
      constexpr int i = CV(I);
      mma(acc[i >> 2][snake(i)], cat(Fb.a[snake(i)][0], Fb.a[snake(i)][1]), Fb.b[i >> 2]);
      if constexpr (s < 7 && i != 4 && i != 9 && i < 14) rd(F0, i - (i > 4) - (i > 9), s + 1, 0);
      feed(IC<2 * s + 1>{}, I);
      if constexpr (s == 0 && i == 0) mma0(kt.t, KA[0], KB[0]);
      if constexpr (s == 0 && i > 0 && !(i & 1)) mma(kt.t, KA[i >> 1], KB[i >> 1]);
      if constexpr (s == 1 && i < KT_OPS) ktab_op<ROLE, i>(c, kp, kt);
      if constexpr (ROLE == 0 && s == 3 && i == 0) __builtin_amdgcn_raw_buffer_store_b64(tagv, c.ws, one0, fco, SC1);
      if constexpr (ROLE == 0 && (s == 2 || s == 3) && 32 * (s - 2) + i < KX_OPS) kexp_op<32 * (s - 2) + i>(c, kx);
      if constexpr (s == 7 && (i & 1)) { if constexpr (i & 2) tc[i >> 2] = *LDSP(f4, gb.v + (i >> 2) * 256); else cv[i >> 2] = *LDSP(f4, ga.v + (i >> 2) * 64); }
      if constexpr (ROLE == 1 && s == 7) W1[i >> 3][(i >> 1) & 3][i & 1] = trd(wa[(i >> 1) & 3] + (i >> 3) * 16384 + (i & 1) * 2048);
      if constexpr (ROLE == 0) {
        if constexpr (s == 5 && i == 15) {
          asm volatile("s_waitcnt vmcnt(0)" : "+v"(lf)::"memory");
          if (!flags_up(c.tag, lf)) wait_flags<4>(c.ws, c.tag, ld_flag(c.ws, lfv, 0), lfv, 0);
          asm volatile("" ::: "memory");
        }
        if constexpr (s == 7 && (i == 0 || i == 2)) ur[i >> 1] = u_load(c, uv, i >> 1);
        if constexpr (s == 6 && i < 8) {
          if constexpr (i == 0) { tk[3].v = bhi(str); pin(tk[3].v); }
          if constexpr (i == 1) { tr.v = __builtin_amdgcn_rcpf(tk[3].v); pin(tr.v); }
          if constexpr (i == 2) { tk[0].v = blo(str); pin(tk[0].v); }
          if constexpr (i == 3) { tk[1].v = (-2.f * 0.7978845608028654f * 1.4426950408889634f) * tr.v; pin(tk[1].v); }
          if constexpr (i == 4) { tr.v *= tr.v; pin(tr.v); }
          if constexpr (i == 5) { tk[3].v *= tk[3].v; pin(tk[3].v); }
          if constexpr (i == 6) { tr.v *= 0.044715f; pin(tr.v); }
          if constexpr (i == 7) { tk[2].v = tr.v * tk[1].v; pin(tk[2].v); }
        }
      }
      SB();
    });
    if constexpr (s < 2) colsum(Fb);
  });
  __builtin_amdgcn_s_setprio(0);
}

template <int ROLE>
DEV void geglu(const Ctx& c, f4 (&acc)[4][4], const f4 (&cv)[4], const f4 (&tc)[4]) {
  const int p = (c.xc & 3) * 4 + (c.jj & 3), ft = (c.xc >> 2) * 7 + (c.jj >> 2);
  const unsigned hbase = WS_H + uni((2 * p + ((ROLE ^ c.v) & 1)) * 56 + 4 * ft + c.v) * 4096;
#pragma unroll
  for (int i = 0; i < 16; i++) pin(acc[i >> 2][i & 3]);
#pragma unroll
  for (int tt = 0; tt < 4; tt++) {
    const f2 nm = -f2{tc[tt][0], tc[tt][0]}, k0 = {tc[tt][1], tc[tt][1]}, k1 = {tc[tt][2], tc[tt][2]}, s2 = {tc[tt][3], tc[tt][3]};
    f2 a[4], b[4], z[4];
    auto half = [](const f4& x, int e) { return f2{x[2 * e], x[2 * e + 1]}; };
#define STEP(X) _Pragma("unroll") for (int j = 0; j < 4; j++) { X; } SB();
    STEP(a[j] = fma2(nm, half(cv[j >> 1], j & 1), half(acc[tt][j >> 1], j & 1)))
    STEP(z[j] = a[j] * a[j])
    STEP(z[j] = fma2(z[j], k1, k0))
    STEP(z[j] *= a[j])
    STEP(z[j] = (f2{__builtin_amdgcn_exp2f(z[j].x), __builtin_amdgcn_exp2f(z[j].y)}))
    STEP(b[j] = fma2(nm, half(cv[2 + (j >> 1)], j & 1), half(acc[tt][2 + (j >> 1)], j & 1)))
    STEP(z[j] = fma2(z[j], s2, s2))
    STEP(z[j] = (f2{__builtin_amdgcn_rcpf(z[j].x), __builtin_amdgcn_rcpf(z[j].y)}))
    STEP(a[j] *= b[j])
    STEP(a[j] *= z[j])
#undef STEP
    st128(u4{pk(a[0]), pk(a[1]), pk(a[2]), pk(a[3])}, c.ws, c.lane * 16 + tt * 1024, hbase);
    SB();
  }
}
template <int ROLE, int YOUNGER> DEV void h_publish(const Ctx& c) {
  const int p = (c.xc & 3) * 4 + (c.jj & 3), ft = (c.xc >> 2) * 7 + (c.jj >> 2);
  waitv<YOUNGER>();
  st_flag(c.ws, c.tag, WS_FH + uni((2 * p + ((ROLE ^ c.v) & 1)) * 56 + 28 * ROLE + 2 * ft + (c.v >> 1)) * HSZ, c.lane, HSZ);
}

DEV void ln_block(Ctx& c, const Params& P, KIn kin) {
  const int tok0 = (c.xc & 3) * 512 + (c.xc >> 2) * 256 + (c.jj - 28) * 64;
  const i4 sx = mksrd(P.x, L * D * 2);
  u4 rows[8];
#pragma unroll
  for (int i = 0; i < 8; i++) rows[i] = row_load(sx, c.lane * 16, (tok0 + 32 * (i >> 2) + 4 * c.w + (i & 3)) * 1024);
  if (threadIdx.x < 12) *LDSP(unsigned, LSY + threadIdx.x * 4) = 0u;
  kgen_take<0, 8>(c, P, kin);
  asm volatile("s_waitcnt vmcnt(4)" : "+v"(rows[0]), "+v"(rows[1]), "+v"(rows[2]), "+v"(rows[3])::"memory");
  ln_compute(c, rows, 0, tok0);
  asm volatile("s_waitcnt vmcnt(0)" : "+v"(rows[4]), "+v"(rows[5]), "+v"(rows[6]), "+v"(rows[7])::"memory");
  bar();
  ln_store(c, 0, tok0);
  ln_compute(c, rows + 4, 34816, tok0 + 32);
  bar();
  ln_store(c, 34816, tok0 + 32);
  waitv<0>();
  bar();
  if (c.w == 0) st_flag(c.ws, c.tag, WS_FLN + (tok0 >> 6) * LSZ, c.lane, LSZ);
  constexpr int NW = 4;
  const i4 swd = mksrd(P.wd, F * D * 2);
  const unsigned wq = (c.jj - 28) * 448 * 1024;
  bf8 KA[8], KB[8]; KTab kt;
  KPre kp;
  auto warm = LAM(H) {
    constexpr int h = CV(H);
    if constexpr (h == 0) { if (c.w >= 4 && c.v < 7 - NW) touch(swd, c.lane * 128, wq + NW * 65536 + (c.xc * (7 - NW) + c.v) * 8192); }
    if constexpr (h < NW) {
      if ((c.w >> 2) == (h & 1)) { unsigned k = h + (TILE_G(c) * NW >> 2); k = k >= NW ? k - NW : k;
        touch(swd, c.lane * 128, wq + (1 - TILE_P(c)) * 32768 + k * 65536 + c.v * 8192); }
    }
  };
  if (c.w < 4) { kgen_pre<0>(c, kp); kgen_rows<0>(c, kp, kin, KA, warm); } else { kgen_pre<1>(c, kp); kgen_rows<1>(c, kp, kin, KA, warm); }
  bar();
  kgen_cols(c, KB); kgen_mma(kt.t, KA, KB); mma_fence();
  if (c.w < 4) { ktab_pre<0>(c, kp); ktab_skip(c, kp, kt, kin.dsk); sfor<0, KT_OPS>(LAM(K) { ktab_op<0, CV(K)>(c, kp, kt); }); }
  else { ktab_pre<1>(c, kp); sfor<0, KT_OPS>(LAM(K) { ktab_op<1, CV(K)>(c, kp, kt); }); }
  bar();
  asm volatile("fused_end_a:");
}

struct CFr { bf4 a[8][2]; bf8 b[8]; };
DEV void conv_rd(CFr& Fr, int i, unsigned ka, unsigned ua, int sp) {
  const int g = i / 3, t = i % 3;
  if (t == 0) Fr.b[g] = *LDSP(bf8, ua + 512 * g + 192 - 64 * sp);
  else Fr.a[g][t - 1] = trd(ka + 512 * g + 64 * sp + (t == 1 ? 8 : 0));
}
DEV void conv(Ctx& c) {
  const int chl = c.v & 1, kh = c.v >> 1, n = c.n, q = c.q;
  unsigned ka = LKC + chl * KCH + (n >> 2) * KCS + 2 * (16 + 4 * (n & 3) + 128 * kh + 16 - 8 * q) - 8;
  unsigned ua = LU + chl * UCH + 2 * (256 + 16 * n - 16 - 128 * kh + 8 * q) - 192;
  asm volatile("" : "+v"(ka), "+v"(ua));
  f4 acc[8];
  CFr Fr[2];
#pragma unroll
  for (int i = 0; i < 24; i++) conv_rd(Fr[0], i, ka, ua, 0);
  unsigned xw = LKC + chl * KCH + 32 + (c.lane >> 3) * 512 + (c.lane & 7) * 16, yo = (q & 1) * 512 + n * 32 + (q >> 1) * 16;
  asm volatile("" : "+v"(xw), "+v"(yo));
  lds_wait<1>(LSY + 44, 4);
  if (c.jj < 4 && c.w == 0) st_flag(c.ws, c.tag, WS_FG + (c.xc * 4 + c.jj) * 128, c.lane);
  SB();
#pragma unroll
  for (int sp = 0; sp < 4; sp++) {
    int i = 0;
#pragma unroll
    for (int o = 0; o < 8; o++)
#pragma unroll
      for (int g = 0; g <= o; g++) {
        const int m = o - g;
        if (sp == 0 && g == 0) mma0(acc[m], cat(Fr[0].a[0][0], Fr[0].a[0][1]), Fr[0].b[m]);
        else mma(acc[g + m], cat(Fr[sp & 1].a[g][0], Fr[sp & 1].a[g][1]), Fr[sp & 1].b[m]);
        if (sp < 3 && i < 24) conv_rd(Fr[(sp + 1) & 1], i, ka, ua, sp + 1);
        i++;
        SB();
      }
  }
  mma_fence();
#pragma unroll
  for (int i = 0; i < 8; i++) asm volatile("" : "+v"(acc[i]));
  unsigned pkd[4][2];
  auto finish = [&](auto KH) __attribute__((always_inline)) {
    constexpr int h = CV(KH);
#pragma unroll
    for (int j = 0; j < 4; j++) *LDSP(f4, xw + h * 256 + j * KCS) = acc[4 * (1 - h) + j];
    lds_post(LSY + 16 + c.v * 4, c.lane);
    lds_wait<0>(LSY + 16 + (c.v ^ 2) * 4, 1);
#pragma unroll
    for (int j = 0; j < 4; j++) { const f4 t = acc[4 * h + j] + *LDSP(f4, xw + (1 - h) * 256 + j * KCS); pkd[j][0] = pk(f2{t[0], t[1]}); pkd[j][1] = pk(f2{t[2], t[3]}); }
  };
  if (kh) finish(IC<1>{}); else finish(IC<0>{});
  const int ch = 2 * (c.xc * 32 + c.jj) + chl;
#pragma unroll
  for (int j = 0; j < 4; j += 2) {
    swap16(pkd[j][0], pkd[j + 1][0]); swap16(pkd[j][1], pkd[j + 1][1]);
    st128(u4{pkd[j][0], pkd[j][1], pkd[j + 1][0], pkd[j + 1][1]}, c.ws,
                                           yo + j * 512, WS_YT + ch * 4096 + kh * 2048);
  }
  waitv<0>();
  st_flag(c.ws, c.tag, WS_FY + (kh * 512 + ch) * YS, c.lane, YS);
}
DEV u4 x_load(const Ctx& c, const Params& P, int role, unsigned oa) {
  return __builtin_amdgcn_raw_buffer_load_b128(mkrs(P.x, L * D * 2), oa, (TILE_T(c) * 64 + 16 * c.v + 8 * role) * 1024 + TILE_C(c) * 128, 0);
}
DEV void y_gather(Ctx& c) {
  const int T64 = TILE_T(c), ct = TILE_C(c), l = c.lane;
  const int r0 = 32 * (c.v & 1) + 16 * (c.v >> 1);
  const unsigned fo = WS_FY + ((T64 >> 4) * 512 + 64 * ct + r0) * YS;
  unsigned fv = (l < 16 ? l : 15) * YS, yv = (l >> 3) * 4096 + (l & 7) * 16, ya = LU + (c.v & 1) * (UCH + 64) + (c.v >> 1) * 2080 + l * 16;
  asm volatile("" : "+v"(fv), "+v"(yv), "+v"(ya));
  lds_wait<2>(LSY + 40, 1);
  wait_flags<8>(c.ws, c.tag, ld_flag(c.ws, fv, fo), fv, fo);
#pragma unroll
  for (int i = 0; i < 2; i++)
    *LDSP(u4, ya + i * 1040) = __builtin_amdgcn_raw_buffer_load_b128(c.ws, yv, WS_YT + (64 * ct + r0 + 8 * i) * 4096 + T64 * 128, SC1);
}
struct Fin { unsigned a0, a1, ya, oa; F1 s[8], y[8]; };
template <int ROLE> DEV void finish_addr(const Ctx& c, Fin& f) {
  const int t = c.lane >> 3, c8 = c.lane & 7;
  f.a0 = c.v * 16384 + (8 * ROLE + t) * 256 + ((2 * c8) ^ t) * 16;
  f.ya = LU + (c8 >> 2) * (UCH + 64) + (c8 & 3) * 1040 + (16 * c.v + 8 * ROLE + t) * 2;
  f.oa = t * 1024 + c8 * 16;
  pin(f.a0); pin(f.ya); pin(f.oa);
}
template <int k> DEV void finish_op(Fin& f, const u4& x) {
  if constexpr (k < 8) { f.s[k].v = (k & 1) ? bhi(x[k >> 1]) : blo(x[k >> 1]); pin(f.s[k].v); }
  else if constexpr (k < 16) asm volatile("v_mov_b32 %0, 0" : "=v"(f.y[k - 8].v));
  else { f.a1 = f.a0 ^ 16; pin(f.a1); }
}
template <int ROLE> DEV void finish_tile(Ctx& c, const Params& P, Fin f) {
  const int T64 = TILE_T(c), ct = TILE_C(c);
  const rsrc_t ro = mkrs(P.out, L * D * 2);
  bar();
  float y[8] = {f.y[0].v, f.y[1].v, f.y[2].v, f.y[3].v, f.y[4].v, f.y[5].v, f.y[6].v, f.y[7].v};
  f4 sa = {f.s[0].v, f.s[1].v, f.s[2].v, f.s[3].v}, sb = {f.s[4].v, f.s[5].v, f.s[6].v, f.s[7].v};
  asm volatile("ds_read_u16_d16_hi %0, %8\n\tds_read_u16_d16_hi %1, %8 offset:128\n\tds_read_u16_d16_hi %2, %8 offset:256\n\tds_read_u16_d16_hi %3, %8 offset:384\n\t"
               "ds_read_u16_d16_hi %4, %8 offset:512\n\tds_read_u16_d16_hi %5, %8 offset:640\n\tds_read_u16_d16_hi %6, %8 offset:768\n\tds_read_u16_d16_hi %7, %8 offset:896"
               : "+v"(y[0]), "+v"(y[1]), "+v"(y[2]), "+v"(y[3]), "+v"(y[4]), "+v"(y[5]), "+v"(y[6]), "+v"(y[7]) : "v"(f.ya) : "memory");
  f4 pa[4], pb[4];
#pragma unroll
  for (int k = 0; k < 4; k++) { pa[k] = *LDSP(f4, f.a0 + k * 4096); pb[k] = *LDSP(f4, f.a1 + k * 4096); }
  SB();
#pragma unroll
  for (int k = 0; k < 4; k++) { sa += pa[k]; sb += pb[k]; }
  asm volatile("s_waitcnt lgkmcnt(0)" : "+v"(y[0]), "+v"(y[1]), "+v"(y[2]), "+v"(y[3]), "+v"(y[4]), "+v"(y[5]), "+v"(y[6]), "+v"(y[7])::"memory");
  sa += f4{y[0], y[1], y[2], y[3]}; sb += f4{y[4], y[5], y[6], y[7]};
  st128(u4{pk(f2{sa[0], sa[1]}), pk(f2{sa[2], sa[3]}), pk(f2{sb[0], sb[1]}), pk(f2{sb[2], sb[3]})}, ro, f.oa,
                                         (T64 * 64 + 16 * c.v + 8 * ROLE) * 1024 + ct * 128);
}

DEV void gemm2(Ctx& c, const Params& P, const bf4 (&W1)[7][4][2], u2 hfl, unsigned (&wa)[4], Fin& fin, unsigned pw0) {
  const int T64 = TILE_T(c), v = c.v;
  unsigned l16 = c.lane * 16;
  asm volatile("" : "+v"(l16));
  const unsigned hk = uni(T64 * 56 + 14 * v + TILE_P(c));
  const unsigned fo = WS_FH + uni(T64 * 56 + 7 * v) * HSZ, fo2 = fo + 28 * HSZ, fv = (l16 < 112 ? l16 : 96) * (HSZ / 16);
  if (!flags_up(c.tag, hfl)) wait_flags<2>(c.ws, c.tag, ld_flag(c.ws, fv, fo), fv, fo);
  asm volatile("" ::: "memory");
  constexpr int WIN = 2;
  u4 H[14][4];
  auto hload = [&](int i, int t) __attribute__((always_inline)) {
    H[i][t] = __builtin_amdgcn_raw_buffer_load_b128(c.ws, l16 + t * 1024, WS_H + ((i < 7 ? hk : hk ^ 1) + 2 * (i % 7)) * 4096, SC1);
  };
#pragma unroll
  for (int i = 0; i < WIN; i++)
#pragma unroll
    for (int t = 0; t < 4; t++) hload(i, t);
  SB();
  f4 acc[4][4];
  bf4 W2[7][4][2];
  unsigned pw[4]; u4 xr;
  sfor<0, 14>(LAM(I) {
    constexpr int i = CV(I);
    sfor<0, 16>(LAM(J) {
      constexpr int j = CV(J), t = j >> 2, d = snake(j);
      if constexpr (i == 0) mma0(acc[t][d], cat(W1[0][d][0], W1[0][d][1]), H[0][t]);
      else if constexpr (i < 7) mma(acc[t][d], cat(W1[i][d][0], W1[i][d][1]), H[i][t]);
      else mma(acc[t][d], cat(W2[i - 7][d][0], W2[i - 7][d][1]), H[i][t]);
      if constexpr (i == 2 && j == 1) hfl = ld_flag(c.ws, fv, fo2);
      if constexpr (i == 7 - WIN && j == 2) { if (!flags_up(c.tag, hfl)) wait_flags<2>(c.ws, c.tag, ld_flag(c.ws, fv, fo2), fv, fo2); asm volatile("" ::: "memory"); }
      if constexpr (i + WIN < 14 && (j & 3) == 3) hload(i + WIN, t);
      if constexpr (i >= 6 && i < 13 && (j & 1) == 0) W2[i - 6][j >> 2][(j >> 1) & 1] = trd(wa[j >> 2] + (i < 9 ? i - 6 : i - 9) * 16384 + ((j >> 1) & 1) * 2048);
      if constexpr (i == 11 && j == 14) *LDSP(volatile unsigned, LSY + 40) = 1u;
      if constexpr (i == 5 && (j & 3) == 1) { wa[t] += SLOT_C; pin(wa[t]); }
      if constexpr ((i == 8 && (j & 3) == 3)) { wa[t] -= SLOT_C; pin(wa[t]); }
      if constexpr (i == 6 && j == 1) xr = x_load(c, P, 1, fin.oa);
      if constexpr (i >= 10 && (j & 1) && 8 * (i - 10) + (j >> 1) < 17) finish_op<8 * (i - 10) + (j >> 1)>(fin, xr);
      if constexpr (i == 12 && (j & 3) == 3) { pw[t] = pw0 ^ (t * 64); pin(pw[t]); }
      if constexpr (i == 13 && j >= 8) { pin(acc[t - 2][d]); *LDSP(f4, pw[d] + (t - 2) * 16384) = acc[t - 2][d]; }
      SB();
    });
  });
  asm volatile("s_nop 1");
#pragma unroll
  for (int j = 8; j < 16; j++) { pin(acc[j >> 2][snake(j)]); *LDSP(f4, pw[snake(j)] + (j >> 2) * 16384) = acc[j >> 2][snake(j)]; if (j == 11) asm volatile("s_nop 3"); SB(); }
  finish_tile<1>(c, P, fin);
}

__global__ void __launch_bounds__(512, 2) fused_s4_geglu(Params P) {
  Ctx c;
  c.w = __builtin_amdgcn_readfirstlane(threadIdx.x >> 6);
  c.v = c.w & 3; c.lane = threadIdx.x & 63; c.n = c.lane & 15; c.q = c.lane >> 4;
  c.xc = blockIdx.x & 7; c.jj = blockIdx.x >> 3;
  {
    u64 pc; unsigned len;
    asm volatile("s_getpc_b64 %0\n.Lpc%=:\n\ts_mov_b32 %1, fused_end_a-.Lpc%=\n\ts_max_u32 %1, %1, fused_end_b-.Lpc%=" : "=s"(pc), "=s"(len)::"scc");
    if (c.w == 3) touch(mksrd((const void*)pc, len), (c.lane >> 2) * 128, (c.jj & 31) * 2048);
  }
  const bool g1 = c.jj < 28;
  const G1Src gs = g1_src(c);
  const KIn kin = kgen_load<0>(c, P);
  if (__builtin_expect(!g1, 0)) ln_block(c, P, kin);
  if (c.w >= 4) {
    WdSrc wds;
    f4 acc[4][4], cv[4], tc[4]; bf4 W1[7][4][2]; unsigned wa[4];
    if (__builtin_expect(g1, 1)) { KExp kx; u4 ur[2]; gemm1<1>(c, P, gs, kin, wds, acc, W1, wa, kx, ur, cv, tc); }
    else {
      wds = wd_src(c, P);
      lds_arrive(LSY + 44, c.lane);
      lds_wait<2>(LSY + 8, 4);
#pragma unroll
      for (int i = 0; i < 7; i++) wd_chunk(c, wds, i);
#pragma unroll
      for (int d = 0; d < 4; d++) { wa[d] = (wd_raddr(c.v, c.n, c.q) ^ (d * 32)) + SLOT_C; pin(wa[d]); }
      waitv<16>();
#pragma unroll
      for (int i = 0; i < 16; i++) W1[i >> 3][(i >> 1) & 3][i & 1] = trd(wa[(i >> 1) & 3] + (i >> 3) * 16384 + (i & 1) * 2048);
      waitl();
    }
    auto w1_read = [&](int i) __attribute__((always_inline)) {
      if (i == 3) {
#pragma unroll
        for (int d = 0; d < 4; d++) { wa[d] -= SLOT_C; pin(wa[d]); }
      }
#pragma unroll
      for (int k = 0; k < 8; k++) W1[i][k >> 1][k & 1] = trd(wa[k >> 1] + (i < 3 ? i : i - 3) * 16384 + (k & 1) * 2048);
      SB();
    };
    if (g1) { geglu<1>(c, acc, cv, tc); waitv<4>(); } else waitv<0>();
    lds_arrive(LSY + 44, c.lane);
    u2 hfl; Fin fin; unsigned pw0;
    const unsigned go = WS_FG + (c.xc * 4 + (c.jj & 3)) * 128;
#pragma unroll
    for (int i = 0; i < 7; i++) {
      if (i + 2 < 7) w1_read(i + 2);
      if (i >= 2) { if (i < 5) asm volatile("s_waitcnt lgkmcnt(15)" ::: "memory"); else if (i == 5) asm volatile("s_waitcnt lgkmcnt(8)" ::: "memory"); else waitl(); }
      if (i == 5) hfl = flag_sample(P, g1 ? (TILE_T(c) * 56 + 7 * c.v + (c.lane < 7 ? c.lane : 6)) * HSZ + WS_FH : go, 0);
      wd_chunk(c, wds, 7 + i);
      if (i == 1) {
        finish_addr<1>(c, fin); pw0 = c.v * 4096 + c.n * 256 + (c.q ^ (c.n & 7)) * 16; pin(pw0);
        if (g1) h_publish<1, 8>(c);
      }
      SB();
    }
    asm volatile("s_waitcnt vmcnt(8)" : "+v"(hfl)::"memory");
    if (!g1) {
      if (!flags_up(c.tag, hfl)) { wait_flags<4>(c.ws, c.tag, hfl, 0, go); __builtin_amdgcn_s_sleep(8); }
      hfl = ~c.tag;
    }
    gemm2(c, P, W1, hfl, wa, fin, pw0);
    asm volatile("fused_end_b:");
  } else {
    if (__builtin_expect(g1, 1)) {
      f4 acc[4][4], cv[4], tc[4]; WdSrc none; bf4 W1[7][4][2]; unsigned wa[4]; KExp kx; u4 ur[2];
      gemm1<0>(c, P, gs, kin, none, acc, W1, wa, kx, ur, cv, tc);
      geglu<0>(c, acc, cv, tc);
      sfor<0, 9>(LAM(K) { kexp_put<CV(K)>(c, kx); });
      u_write(c, ur[0], ur[1]);
    } else {
      const unsigned fv = (8 * c.v + (c.lane >> 3)) * LSZ;
      const u2 fl = ld_flag(c.ws, fv, WS_FLN);
      KExp kx;
      kexp_pre<0>(c, kx); kexp_pre<1>(c, kx);
      sfor<0, KX_OPS>(LAM(K) { kexp_op<CV(K)>(c, kx); });
      wait_flags<8>(c.ws, c.tag, fl, fv, WS_FLN);
      const u4 r0 = u_load(c, u_off(c), 0), r1 = u_load(c, u_off(c), 1);
      sfor<0, 9>(LAM(K) { kexp_put<CV(K)>(c, kx); });
      lds_arrive(LSY + 8, c.lane);
      u_write(c, r0, r1);
    }
    lds_arrive(LSY, c.lane);
    if (g1) h_publish<0, 0>(c);
    lds_wait<1>(LSY, 4);
    Fin fin;
    finish_addr<0>(c, fin);
    const u4 xr = x_load(c, P, 0, fin.oa);
    conv(c);
    if (__builtin_expect(blockIdx.x == 224 && c.w == 0, 0)) {
#pragma unroll 1
      for (int i = 0; i < 4; i++) {
        const int b = 64 * i + c.lane;
        const unsigned fv = b < 224 ? WS_FC + b * CSZ : WS_FLN + (b - 224) * LSZ;
        wait_flags<16>(c.ws, c.tag, ld_flag(c.ws, fv, 0), fv, 0);
      }
      st_flag(c.ws, c.tag, WS_EPOCH, c.lane);
    }
    sfor<0, 17>(LAM(K) { finish_op<CV(K)>(fin, xr); });
    y_gather(c);
    finish_tile<0>(c, P, fin);
  }
}

extern "C" void kernel_launch(void* const* d_in, const int* in_sizes, int n_in, void* d_out, int out_size, void* d_ws, size_t ws_size,
                              hipStream_t stream) {
  Params P;
  P.x = (const u16*)d_in[0]; P.are = (const u16*)d_in[1]; P.aim = (const u16*)d_in[2]; P.bre = (const u16*)d_in[3];
  P.bim = (const u16*)d_in[4]; P.cre = (const u16*)d_in[5]; P.cim = (const u16*)d_in[6]; P.ldt = (const u16*)d_in[7];
  P.dsk = (const u16*)d_in[8]; P.wg = (const u16*)d_in[10]; P.wu = (const u16*)d_in[11]; P.wd = (const u16*)d_in[12];
  P.out = (u16*)d_out; P.ws = (char*)d_ws;
  (void)hipFuncSetAttribute((const void*)fused_s4_geglu, hipFuncAttributeMaxDynamicSharedMemorySize, LDS_BYTES);
  fused_s4_geglu<<<256, 512, LDS_BYTES, stream>>>(P);
}
```
